```python
import torch

def _softmax(x, axis=-1):
    return torch.softmax(x, dim=axis)
def _gelu(x):
    return torch.nn.functional.gelu(x, approximate="tanh")
def _silu(x):
    return torch.nn.functional.silu(x)
def _sigmoid(x):
    return torch.sigmoid(x)
def _layernorm(x, axis=-1, eps=1e-5):
    m = x.mean(dim=axis, keepdim=True)
    v = x.var(dim=axis, keepdim=True, unbiased=False)
    return (x - m) / torch.sqrt(v + eps)
def _scatter_add(tgt, idx, src, axis=0):
    return tgt.clone().index_add_(axis, idx, src)
def _segment_sum(src, seg, n):
    out = torch.zeros((n,) + src.shape[1:], dtype=src.dtype)
    return out.index_add_(0, seg, src)
def _pad(x, pw):
    flat = tuple(v for pair in reversed(pw) for v in pair)
    return torch.nn.functional.pad(x, flat)

kernel_name = 'standalone__mha_block_cached__near_ridge'

def setup_inputs():

    _w = (lambda a: (lambda t: t.to(torch.bfloat16) if t.is_floating_point() else t)(torch.as_tensor(a)))
    B, Sq, D = 8, 288, 512
    H, Sk, Dh = 32, 2048, 128
    return {
        "x": _w((lambda *s: torch.randn(*s, dtype=torch.bfloat16) * 0.8)(B, Sq, D)),
        "k_cache": _w((lambda *s: torch.randn(*s, dtype=torch.bfloat16) * 0.8)(B, H, Sk, Dh)),
        "v_cache": _w((lambda *s: torch.randn(*s, dtype=torch.bfloat16) * 0.05)(B, H, Sk, Dh)),
        "wq": _w((lambda *s: torch.randn(*s, dtype=torch.bfloat16) * 0.8)(D, H * Dh) * (D ** -0.5)),
        "wo": _w((lambda *s: torch.randn(*s, dtype=torch.bfloat16) * 0.05)(H * Dh, D) * ((H * Dh) ** -0.5)),
    }

def reference(x, k_cache, v_cache, wq, wo):
    B, Sq, D = x.shape
    H, Dh = 32, 128
    q = (lambda x, *d: x.permute(*d))((x @ wq).reshape(B, Sq, H, Dh), 0, 2, 1, 3)
    scores = torch.einsum("bhqd,bhkd->bhqk", q, k_cache) * (Dh ** -0.5)
    p = _softmax(scores, axis=-1)
    o = torch.einsum("bhqk,bhkd->bhqd", p, v_cache)
    return (lambda x, *d: x.permute(*d))(o, 0, 2, 1, 3).reshape(B, Sq, H * Dh) @ wo
```

```cpp
#include <hip/hip_runtime.h>
#include <hip/hip_bf16.h>
using bf16 = __hip_bfloat16;
using bf16x8 = __attribute__((ext_vector_type(8))) short;
using s16x4  = __attribute__((ext_vector_type(4))) short;
using f32x16 = __attribute__((ext_vector_type(16))) float;
using f32x4 = __attribute__((ext_vector_type(4))) float;
using u32x4  = __attribute__((ext_vector_type(4))) unsigned;
constexpr int B = 8, SQ = 288, DM = 512, H = 32, SK = 2048, DH = 128;

__device__ __forceinline__ unsigned cvtpk(float lo, float hi) {
  unsigned r; asm volatile("v_cvt_pk_bf16_f32 %0, %1, %2" : "=v"(r) : "v"(lo), "v"(hi)); return r;
}
__device__ __forceinline__ int crow(int r, int hi) { return (r & 3) + 8 * (r >> 2) + 4 * hi; }

__global__ __launch_bounds__(256) void gemm_simple(const unsigned short* __restrict__ A, const unsigned short* __restrict__ W,
                                                   unsigned short* __restrict__ C, int M, int N, int K) {
  const int wid = threadIdx.x >> 6, lane = threadIdx.x & 63, r32 = lane & 31, hi = lane >> 5;
  const int tn = blockIdx.x * 4 + wid, tm = blockIdx.y;
  f32x16 acc = {};
  const unsigned short* a = A + (long)(tm * 32 + r32) * K + hi * 8;
  const unsigned short* w = W + (long)(hi * 8) * N + tn * 32 + r32;
  for (int k = 0; k < K; k += 16) {
    bf16x8 af = *reinterpret_cast<const bf16x8*>(a + k);
    bf16x8 bf;
#pragma unroll
    for (int j = 0; j < 8; ++j) bf[j] = (short)w[(long)(k + j) * N];
    acc = __builtin_amdgcn_mfma_f32_32x32x16_bf16(af, bf, acc, 0, 0, 0);
  }
#pragma unroll
  for (int r = 0; r < 16; ++r) {
    unsigned p = cvtpk(acc[r], acc[r]);
    C[(long)(tm * 32 + crow(r, hi)) * N + tn * 32 + r32] = (unsigned short)(p & 0xffff);
  }
}

__global__ __launch_bounds__(64 * 3) void attn_simple(const unsigned short* __restrict__ Q, const unsigned short* __restrict__ Kc,
                                                      const unsigned short* __restrict__ Vc, unsigned short* __restrict__ O) {
  const int wid = (threadIdx.x >> 6) + 3 * blockIdx.y, lane = threadIdx.x & 63, r32 = lane & 31, hi = lane >> 5;
  const int bh = blockIdx.x, b = bh / H, h = bh % H;
  const unsigned short* q = Q + (long)(b * SQ + wid * 32 + r32) * (H * DH) + h * DH + hi * 8;
  bf16x8 qr[8];
#pragma unroll
  for (int d0 = 0; d0 < 8; ++d0) qr[d0] = *reinterpret_cast<const bf16x8*>(q + d0 * 16);
  const unsigned short* kh = Kc + (long)bh * SK * DH;
  const unsigned short* vh = Vc + (long)bh * SK * DH;
  constexpr float C = 0.088388347648318440f * 1.4426950408889634f;
  float m = -1e30f, l = 0.f;
  f32x16 o[4] = {};
  for (int k0 = 0; k0 < SK; k0 += 32) {
    f32x16 s = {};
#pragma unroll
    for (int d0 = 0; d0 < 8; ++d0) {
      bf16x8 kf = *reinterpret_cast<const bf16x8*>(kh + (long)(k0 + r32) * DH + d0 * 16 + hi * 8);
      s = __builtin_amdgcn_mfma_f32_32x32x16_bf16(kf, qr[d0], s, 0, 0, 0);
    }
    float pm = s[0];
#pragma unroll
    for (int r = 1; r < 16; ++r) pm = fmaxf(pm, s[r]);
    pm = fmaxf(pm, __shfl_xor(pm, 32));
    const float mn = fmaxf(m, pm), alpha = __builtin_amdgcn_exp2f((m - mn) * C);
    m = mn;
    float ps = 0.f;
#pragma unroll
    for (int r = 0; r < 16; ++r) { s[r] = __builtin_amdgcn_exp2f((s[r] - mn) * C); ps += s[r]; }
    ps += __shfl_xor(ps, 32);
    l = l * alpha + ps;
#pragma unroll
    for (int db = 0; db < 4; ++db)
#pragma unroll
      for (int r = 0; r < 16; ++r) o[db][r] *= alpha;
#pragma unroll
    for (int ks = 0; ks < 2; ++ks) {
      u32x4 pw = {cvtpk(s[8 * ks + 0], s[8 * ks + 1]), cvtpk(s[8 * ks + 2], s[8 * ks + 3]), cvtpk(s[8 * ks + 4], s[8 * ks + 5]), cvtpk(s[8 * ks + 6], s[8 * ks + 7])};
      bf16x8 pf = *reinterpret_cast<bf16x8*>(&pw);
#pragma unroll
      for (int db = 0; db < 4; ++db) {
        bf16x8 vf;
#pragma unroll
        for (int j = 0; j < 8; ++j) vf[j] = (short)vh[(long)(k0 + 16 * ks + 8 * (j >> 2) + 4 * hi + (j & 3)) * DH + db * 32 + r32];
        o[db] = __builtin_amdgcn_mfma_f32_32x32x16_bf16(vf, pf, o[db], 0, 0, 0);
      }
    }
  }
  const float il = 1.f / l;
  unsigned short* op = O + (long)(b * SQ + wid * 32 + r32) * (H * DH) + h * DH;
#pragma unroll
  for (int db = 0; db < 4; ++db)
#pragma unroll
    for (int r = 0; r < 16; ++r) op[db * 32 + crow(r, hi)] = (unsigned short)(cvtpk(o[db][r] * il, 0.f) & 0xffff);
}

extern "C" void kernel_launch(void* const* d_in, const int* in_sizes, int n_in, void* d_out, int out_size, void* d_ws, size_t ws_size, hipStream_t stream) {
  const unsigned short* x = (const unsigned short*)d_in[0];
  const unsigned short* kc = (const unsigned short*)d_in[1];
  const unsigned short* vc = (const unsigned short*)d_in[2];
  const unsigned short* wq = (const unsigned short*)d_in[3];
  const unsigned short* wo = (const unsigned short*)d_in[4];
  unsigned short* q = (unsigned short*)d_ws;
  unsigned short* o = q + (size_t)B * SQ * H * DH;
  gemm_simple<<<dim3(H * DH / 128, B * SQ / 32), 256, 0, stream>>>(x, wq, q, B * SQ, H * DH, DM);
  attn_simple<<<dim3(B * H, 3), 64 * 3, 0, stream>>>(q, kc, vc, o);
  gemm_simple<<<dim3(DM / 128, B * SQ / 32), 256, 0, stream>>>(o, wo, (unsigned short*)d_out, B * SQ, DM, H * DH);
}
```

```cpp
#include <hip/hip_runtime.h>
typedef unsigned short u16;
typedef short    bf16x8 __attribute__((ext_vector_type(8)));
typedef float    f32x4  __attribute__((ext_vector_type(4)));
typedef float    f32x2  __attribute__((ext_vector_type(2)));
typedef __bf16   bf16x2 __attribute__((ext_vector_type(2)));
typedef unsigned u32x4  __attribute__((ext_vector_type(4)));
constexpr int B = 8, SQ = 288, DM = 512, H = 32, SK = 2048, DH = 128, NQB = SQ / 16;
constexpr float QSCALE = 0.088388347648318440f * 1.4426950408889634f;
constexpr unsigned PWS = 18874368, H1 = 31457280, H2 = 31588352;
typedef unsigned long long u64;
typedef u64 u64x2 __attribute__((ext_vector_type(2)));
#define AGENT __ATOMIC_RELAXED, __HIP_MEMORY_SCOPE_AGENT

template <class T> __device__ __forceinline__ void store16(char* p, T v) {
  const u64x2 w = __builtin_bit_cast(u64x2, v);
  __hip_atomic_store((u64*)p, w[0], AGENT); __hip_atomic_store((u64*)p + 1, w[1], AGENT);
}
template <class T> __device__ __forceinline__ T load16(char* p) {
  const u64x2 w = {__hip_atomic_load((u64*)p, AGENT), __hip_atomic_load((u64*)p + 1, AGENT)};
  return __builtin_bit_cast(T, w);
}

__device__ __forceinline__ unsigned cvtpk(float lo, float hi) {
  const f32x2 v = {lo, hi};
  return __builtin_bit_cast(unsigned, __builtin_convertvector(v, bf16x2));
}
__device__ __forceinline__ bf16x8 pack8(f32x4 a, f32x4 b, float s) {
  u32x4 w = {cvtpk(a[0] * s, a[1] * s), cvtpk(a[2] * s, a[3] * s), cvtpk(b[0] * s, b[1] * s), cvtpk(b[2] * s, b[3] * s)};
  return __builtin_bit_cast(bf16x8, w);
}
__device__ __forceinline__ f32x4 mfma16(bf16x8 a, bf16x8 b, f32x4 c) { return __builtin_amdgcn_mfma_f32_16x16x32_bf16(a, b, c, 0, 0, 0); }

__device__ __forceinline__ void project_q(const u16* x, const u16* wq, int b, int h, int qb, int lane, bf16x8 (&qf)[4]) {
  const int i = lane & 15, g = lane >> 4;
#pragma unroll
  for (int ds = 0; ds < 4; ++ds) {
    f32x4 acc0 = {}, acc1 = {};
    const int d = h * DH + 32 * ds + 8 * (i >> 2) + (i & 3);
#pragma unroll 1
    for (int c0 = 0; c0 < DM; c0 += 32) {
      const bf16x8 xf = *reinterpret_cast<const bf16x8*>(x + (size_t)(b * SQ + qb * 16 + i) * DM + c0 + 8 * g);
      bf16x8 w0, w1;
#pragma unroll
      for (int j = 0; j < 8; ++j) { const u16* p = wq + (size_t)(c0 + 8 * g + j) * (H * DH) + d; w0[j] = (short)p[0]; w1[j] = (short)p[4]; }
      acc0 = mfma16(w0, xf, acc0); acc1 = mfma16(w1, xf, acc1);
    }
    qf[ds] = pack8(acc0, acc1, QSCALE);
  }
}

__device__ __forceinline__ void attend_exact(const u16* x, const u16* wq, const u16* kh, const u16* vh, char* ows, int b, int h, int qb, int lane, int r0, int r1) {
  const int i = lane & 15, g = lane >> 4;
  bf16x8 qf[4];
  project_q(x, wq, b, h, qb, lane, qf);
  float m = -__builtin_inff(), l = 0.f;
  f32x4 o[8] = {};
#pragma unroll 1
  for (int k0 = 0; k0 < SK; k0 += 32) {
    f32x4 s[2] = {};
#pragma unroll
    for (int kb = 0; kb < 2; ++kb)
#pragma unroll
      for (int ds = 0; ds < 4; ++ds)
        s[kb] = mfma16(*reinterpret_cast<const bf16x8*>(kh + (size_t)(k0 + 16 * kb + i) * DH + 32 * ds + 8 * g), qf[ds], s[kb]);
    float pm = fmaxf(fmaxf(fmaxf(s[0][0], s[0][1]), fmaxf(s[0][2], s[0][3])), fmaxf(fmaxf(s[1][0], s[1][1]), fmaxf(s[1][2], s[1][3])));
    pm = fmaxf(pm, __builtin_bit_cast(float, __builtin_amdgcn_ds_bpermute((lane ^ 16) * 4, __builtin_bit_cast(int, pm))));
    pm = fmaxf(pm, __builtin_bit_cast(float, __builtin_amdgcn_ds_bpermute((lane ^ 32) * 4, __builtin_bit_cast(int, pm))));
    const float mn = fmaxf(m, pm), alpha = __builtin_amdgcn_exp2f(m - mn);
    m = mn; l *= alpha;
#pragma unroll
    for (int kb = 0; kb < 2; ++kb)
#pragma unroll
      for (int r = 0; r < 4; ++r) { s[kb][r] = __builtin_amdgcn_exp2f(s[kb][r] - mn); l += s[kb][r]; }
    const bf16x8 pf = pack8(s[0], s[1], 1.f);
#pragma unroll
    for (int k = 0; k < 8; ++k) {
      bf16x8 vf;
#pragma unroll
      for (int j = 0; j < 8; ++j) vf[j] = (short)vh[(size_t)(k0 + 16 * (j >> 2) + 4 * g + (j & 3)) * DH + 16 * k + i];
      o[k] = mfma16(vf, pf, o[k] * alpha);
    }
  }
  l += __builtin_bit_cast(float, __builtin_amdgcn_ds_bpermute((lane ^ 16) * 4, __builtin_bit_cast(int, l)));
  l += __builtin_bit_cast(float, __builtin_amdgcn_ds_bpermute((lane ^ 32) * 4, __builtin_bit_cast(int, l)));
  const float il = 1.f / l;
  if ((i & 3) >= r0 && (i & 3) < r1) {
#pragma unroll
    for (int r = 0; r < 4; ++r) {
      const f32x4 lo = {o[0][r], o[1][r], o[2][r], o[3][r]}, hi = {o[4][r], o[5][r], o[6][r], o[7][r]};
      store16(ows + (size_t)((b * NQB + qb) * H + h) * 4096 + i * 256 + (((4 * g + r) ^ i) * 16), pack8(lo, hi, il));
    }
  }
}

__device__ __forceinline__ void publish(char* ws, unsigned off, unsigned value, int lane) {
  asm volatile("s_waitcnt vmcnt(0)" ::: "memory");
  if (lane == 0) __hip_atomic_store((unsigned*)(ws + off), value, AGENT);
}
__device__ __forceinline__ void await(char* ws, unsigned off, unsigned snap, bool watched, unsigned need) {
  for (unsigned spin = 0; spin < 0x80000u; ++spin) {
    const unsigned now = watched ? __hip_atomic_load((unsigned*)(ws + off), AGENT) : snap + need;
    if (__builtin_amdgcn_ballot_w64(now - snap < need) == 0) break;
    __builtin_amdgcn_s_sleep(1);
  }
  asm volatile("" ::: "memory");
}

__device__ __forceinline__ void project_out(const u16* wo, u16* out, char* ws, int b, int h, int wave, int lane, unsigned snap) {
  const int hg = h >> 3, i8 = h & 7, rh = i8 & 1, c4 = i8 >> 1, i = lane & 15, g = lane >> 4, bh = b * H + h;
  const unsigned own = (unsigned)(bh * 4 + wave) * 128u;
  const unsigned h1 = __builtin_amdgcn_readlane(snap, 20), h2 = __builtin_amdgcn_readlane(snap, 16 + hg);
#pragma unroll 1
  for (unsigned stage = 1; stage <= 2; ++stage) {
    publish(ws, H1 + own, h1 + stage, lane);
    await(ws, H1 + (unsigned)(((bh & 0xf8) + (lane >> 1)) * 4 + 2 * rh + (lane & 1)) * 128u, snap, lane < 16, stage);
  }
  publish(ws, H1 + own, h1 + 4, lane);
  char* inbox = ws + PWS + (size_t)(((b * H + i8) * 16 + 128 * hg + wave)) * 3072 + lane * 16;
#pragma unroll 1
  for (int n = 0; n < 9; ++n) {
    const int j = (hg + 1 + (n >> 1)) & 3;
    const int sw = (c4 >> 1);
    const int qb = n < 6 ? 8 * rh + 4 * ((n & 1) ^ sw) + j : (n == 6 ? 16 + rh : 8 * rh + 4 * ((n - 7) ^ sw) + hg);
    f32x4 acc[2] = {};
#pragma unroll 1
    for (int ks = 0; ks < 32; ++ks) {
      const bf16x8 of = load16<bf16x8>(ws + (size_t)((b * NQB + qb) * H + 8 * hg + (ks >> 2)) * 4096 + i * 256 + (((4 * (ks & 3) + g) ^ i) * 16));
#pragma unroll
      for (int u = 0; u < 2; ++u) {
        bf16x8 wf;
#pragma unroll
        for (int e = 0; e < 8; ++e) wf[e] = (short)wo[(size_t)(1024 * hg + 128 * (ks >> 2) + 16 * e + 4 * (ks & 3) + g) * DM + 128 * c4 + 32 * wave + 16 * u + i];
        acc[u] = mfma16(wf, of, acc[u]);
      }
    }
    if (n < 7) {
      const int d = n < 6 ? j : wave, unit = n < 6 ? (n & 1) : 2;
      char* dst = ws + PWS + (size_t)((b * H + i8) * 16 + 128 * d + 4 * hg + wave) * 3072 + unit * 1024 + lane * 16;
      store16(dst, pack8(acc[0], acc[1], 1.f));
    }
    if (n == 6) {
      publish(ws, H2 + own, h2 + 1, lane);
      await(ws, H2 + (unsigned)(((bh & 0xe7) + 8 * (lane - 16)) * 4 + wave) * 128u, snap, lane >= 16 && lane < 20, 1);
      publish(ws, H2 + own, h2 + 2, lane);
      if (wave != hg) continue;
    }
    if (n >= 6) {
#pragma unroll
      for (int d = 1; d < 4; ++d) {
        const u32x4 p = load16<u32x4>(inbox + ((hg + d) & 3) * 4 * 3072 + (n == 6 ? 2 : n - 7) * 1024);
#pragma unroll
        for (int e = 0; e < 8; ++e) acc[e >> 2][e & 3] += __builtin_bit_cast(float, (e & 1) ? p[e >> 1] & 0xffff0000u : p[e >> 1] << 16);
      }
    }
    if (n >= 6) {
#pragma unroll
      for (int u = 0; u < 2; ++u)
        *reinterpret_cast<uint2*>(out + (size_t)((b * NQB + qb) * 16 + i) * DM + 128 * c4 + 32 * wave + 16 * u + 4 * g) =
            make_uint2(cvtpk(acc[u][0], acc[u][1]), cvtpk(acc[u][2], acc[u][3]));
    }
  }
}

__global__ __launch_bounds__(256, 1) void mha_block(const u16* x, const u16* kc, const u16* vc, const u16* wq, const u16* wo, u16* out, char* ows) {
  __shared__ __attribute__((aligned(16))) char lds[163840];
  const int wave = __builtin_amdgcn_readfirstlane(threadIdx.x >> 6), blk = blockIdx.x;
  unsigned flag, snap;
  asm volatile(
      "s_load_dwordx8 s[8:15], %[ka], 0\n"
      "s_load_dwordx4 s[16:19], %[ka], 32\n"
      "s_load_dwordx2 s[36:37], %[ka], 48\n"
      "s_mov_b32 s22, %[wave]\n"
      "s_lshl_b32 s23, s22, 10\n"
      "s_and_b32 s61, s22, 1\n"
      "s_and_b32 s62, %[blk], 1\n"
      "s_lshl_b32 s62, s62, 2\n"
      "s_bfe_u32 s63, %[blk], 0x20003\n"
      "s_add_u32 s59, s62, s63\n"
      "s_bfe_u32 s62, %[blk], 0x20001\n"
      "s_lshl_b32 s62, s62, 3\n"
      "s_lshr_b32 s63, %[blk], 5\n"
      "s_add_u32 s60, s62, s63\n"
      "v_mbcnt_lo_u32_b32 v255, -1, 0\n"
      "v_mbcnt_hi_u32_b32 v255, -1, v255\n"
      "s_add_u32 s62, s59, s60\n"
      "s_and_b32 s62, s62, 1\n"
      "s_mov_b32 s87, 0x76543210\n"
      "s_cmp_eq_u32 s62, 1\n"
      "s_cselect_b32 s87, 0x54763210, s87\n"
      "s_lshr_b32 s62, s22, 1\n"
      "s_lshl_b32 s63, s62, 16\n"
      "s_lshl_b32 s64, s61, 7\n"
      "s_add_u32 s63, s63, s64\n"
      "s_lshl_b32 s64, s62, 1\n"
      "v_and_b32 v221, 3, v255\n"
      "v_xor_b32 v221, s64, v221\n"
      "v_lshrrev_b32 v222, 5, v255\n"
      "v_lshl_add_u32 v221, v222, 2, v221\n"
      "v_bfe_u32 v220, v255, 2, 3\n"
      "v_lshlrev_b32 v220, 13, v220\n"
      "v_lshl_add_u32 v220, v221, 4, v220\n"
      "v_add_u32 v211, s63, v220\n"
      "s_bfe_u32 s75, s87, 0x40000\n"
      "s_lshl_b32 s75, s75, 19\n"
      "s_lshl_b32 s62, s60, 8\n"
      "s_sub_u32 s46, 4194304, s62\n"
      "s_mov_b32 s47, 0x20000\n"
      "s_waitcnt lgkmcnt(0)\n"
      "s_add_u32 s44, s14, s62\n"
      "s_addc_u32 s45, s15, 0\n"
      "s_add_u32 m0, s23, 32768\n"
      "s_nop 0\n"
      "buffer_load_dwordx4 v211, s[44:47], s75 offen lds\n"
      "s_add_u32 m0, s23, 36864\n"
      "s_add_u32 s75, s75, 131072\n"
      "buffer_load_dwordx4 v211, s[44:47], s75 offen lds\n"
      "s_add_u32 m0, s23, 40960\n"
      "s_add_u32 s75, s75, 131072\n"
      "buffer_load_dwordx4 v211, s[44:47], s75 offen lds\n"
      "s_add_u32 m0, s23, 45056\n"
      "s_add_u32 s75, s75, 131072\n"
      "buffer_load_dwordx4 v211, s[44:47], s75 offen lds\n"
      "s_bfe_u32 s75, s87, 0x40004\n"
      "s_lshl_b32 s75, s75, 19\n"
      "s_getpc_b64 s[48:49]\n"
      "HERE_%=:\n"
      "s_mov_b32 s50, END_%=-HERE_%=\n"
      "s_mov_b32 s51, 0x20000\n"
      "s_lshr_b32 s62, %[blk], 3\n"
      "s_lshl_b32 s62, s62, 2\n"
      "s_add_u32 s62, s62, s22\n"
      "s_lshl_b32 s62, s62, 9\n"
      "v_lshrrev_b32 v0, 3, v255\n"
      "v_lshlrev_b32 v0, 24, v0\n"
      "v_lshl_add_u32 v0, v255, 7, v0\n"
      "buffer_load_dword a220, v0, s[48:51], s62 offen\n"
      "s_mul_i32 s62, s59, 294912\n"
      "s_add_u32 s40, s8, s62\n"
      "s_addc_u32 s41, s9, 0\n"
      "s_mov_b32 s42, 294912\n"
      "s_mov_b32 s43, 0x20000\n"
      "v_lshrrev_b32 v220, 3, v255\n"
      "v_lshlrev_b32 v220, 10, v220\n"
      "v_and_b32 v221, 7, v255\n"
      "v_lshrrev_b32 v222, 4, v255\n"
      "v_xor_b32 v221, v221, v222\n"
      "v_lshl_add_u32 v208, v221, 4, v220\n"
      "v_xor_b32 v209, 64, v208\n"
      "s_lshl_b32 s64, s61, 6\n"
      "v_xor_b32 v210, s64, v208\n"
      "s_lshl_b32 s73, s22, 16\n"
      "s_lshl_b32 s74, s22, 13\n"
      "s_add_u32 s84, s74, 81920\n"
      "v_add_u32 v208, s73, v208\n"
      "v_add_u32 v209, s73, v209\n"
      "s_add_u32 s86, s74, 262144\n"
      "s_lshr_b32 s85, s60, 3\n"
      "s_add_u32 s85, s85, 1\n"
      "s_and_b32 s85, s85, 3\n"
      "s_lshl_b32 s85, s85, 14\n"
      "s_bfe_u32 s74, s87, 0x40000\n"
      "s_lshl_b32 s74, s74, 7\n"
      "s_or_b32 s74, s74, s86\n"
      "s_bfe_u32 s73, s87, 0x40000\n"
      "s_lshl_b32 s73, s73, 7\n"
      "s_or_b32 s73, s73, s85\n"
      "s_add_u32 m0, s23, 147456\n"
      "s_nop 0\n"
      "buffer_load_dwordx4 v210, s[40:43], s74 offen lds\n"
      "s_add_u32 m0, s84, 0\n"
      "s_bfe_u32 s74, s87, 0x40004\n"
      "s_lshl_b32 s74, s74, 7\n"
      "s_or_b32 s74, s74, s86\n"
      "buffer_load_dwordx4 v208, s[40:43], s73 offen lds\n"
      "s_add_u32 m0, s84, 1024\n"
      "s_add_u32 s73, s73, 8192\n"
      "buffer_load_dwordx4 v209, s[40:43], s73 offen lds\n"
      "s_add_u32 m0, s84, 2048\n"
      "s_add_u32 s73, s73, 8192\n"
      "s_and_b32 s73, s73, 0xffff\n"
      "buffer_load_dwordx4 v208, s[40:43], s73 offen lds\n"
      "s_add_u32 m0, s84, 3072\n"
      "s_add_u32 s73, s73, 8192\n"
      "buffer_load_dwordx4 v209, s[40:43], s73 offen lds\n"
      "s_add_u32 s73, s73, 8192\n"
      "s_and_b32 s73, s73, 0xffff\n"
      "v_and_b32 v220, 15, v255\n"
      "v_lshlrev_b32 v220, 7, v220\n"
      "v_lshrrev_b32 v221, 4, v255\n"
      "v_bfe_u32 v222, v255, 1, 3\n"
      "v_xor_b32 v221, v221, v222\n"
      "v_lshl_add_u32 v220, v221, 4, v220\n"
      "v_xor_b32 v221, 64, v220\n"
      "s_lshr_b32 s62, s22, 1\n"
      "s_lshl_b32 s63, s62, 11\n"
      "s_add_u32 s63, s63, 147456\n"
      "v_add_u32 v212, s84, v220\n"
      "v_add_u32 v214, s63, v220\n"
      "v_add_u32 v213, s84, v221\n"
      "v_add_u32 v215, s63, v221\n"
      "v_lshrrev_b32 v220, 4, v255\n"
      "v_lshlrev_b32 v220, 11, v220\n"
      "v_bfe_u32 v221, v255, 2, 2\n"
      "v_lshl_add_u32 v220, v221, 6, v220\n"
      "v_bfe_u32 v221, v255, 4, 1\n"
      "v_lshl_add_u32 v220, v221, 5, v220\n"
      "v_and_b32 v221, 3, v255\n"
      "v_lshl_add_u32 v220, v221, 3, v220\n"
      "v_xor_b32 v221, 32, v220\n"
      "s_lshl_b32 s63, s61, 10\n"
      "s_xor_b32 s64, s63, 0x400\n"
      "v_add_u32 v216, s63, v220\n"
      "v_add_u32 v217, s63, v221\n"
      "v_add_u32 v218, s64, v220\n"
      "v_add_u32 v219, s64, v221\n"
      "s_add_u32 m0, s84, 4096\n"
      "s_nop 0\n"
      "buffer_load_dwordx4 v208, s[40:43], s73 offen lds\n"
      "s_add_u32 m0, s84, 5120\n"
      "s_add_u32 s73, s73, 8192\n"
      "buffer_load_dwordx4 v209, s[40:43], s73 offen lds\n"
      "s_add_u32 m0, s84, 6144\n"
      "s_add_u32 s73, s73, 8192\n"
      "s_and_b32 s73, s73, 0xffff\n"
      "buffer_load_dwordx4 v208, s[40:43], s73 offen lds\n"
      "s_add_u32 m0, s84, 7168\n"
      "s_add_u32 s73, s73, 8192\n"
      "buffer_load_dwordx4 v209, s[40:43], s73 offen lds\n"
      "s_bfe_u32 s73, s87, 0x40004\n"
      "s_lshl_b32 s73, s73, 7\n"
      "s_or_b32 s73, s73, s85\n"
      "s_waitcnt vmcnt(8)\n"
      "s_barrier\n"
      "s_add_u32 m0, s23, 49152\n"
      "ds_read_b128 a[192:195], v214 offset:0\n"
      "ds_read_b128 a[196:199], v215 offset:0\n"
      "ds_read_b64_tr_b16 a[0:1], v216 offset:32768\n"
      "ds_read_b64_tr_b16 a[2:3], v216 offset:33024\n"
      "buffer_load_dwordx4 v211, s[44:47], s75 offen lds\n"
      "s_add_u32 s75, s75, 131072\n"
      "s_add_u32 m0, s23, 53248\n"
      "ds_read_b64_tr_b16 a[4:5], v216 offset:40960\n"
      "ds_read_b64_tr_b16 a[6:7], v216 offset:41216\n"
      "ds_read_b64_tr_b16 a[8:9], v217 offset:32768\n"
      "ds_read_b64_tr_b16 a[10:11], v217 offset:33024\n"
      "buffer_load_dwordx4 v211, s[44:47], s75 offen lds\n"
      "s_add_u32 s75, s75, 131072\n"
      "s_add_u32 m0, s23, 57344\n"
      "ds_read_b64_tr_b16 a[12:13], v217 offset:40960\n"
      "ds_read_b64_tr_b16 a[14:15], v217 offset:41216\n"
      "ds_read_b64_tr_b16 a[16:17], v216 offset:33280\n"
      "ds_read_b64_tr_b16 a[18:19], v216 offset:33536\n"
      "buffer_load_dwordx4 v211, s[44:47], s75 offen lds\n"
      "s_add_u32 s75, s75, 131072\n"
      "s_add_u32 m0, s23, 61440\n"
      "ds_read_b64_tr_b16 a[20:21], v216 offset:41472\n"
      "ds_read_b64_tr_b16 a[22:23], v216 offset:41728\n"
      "ds_read_b64_tr_b16 a[24:25], v217 offset:33280\n"
      "ds_read_b64_tr_b16 a[26:27], v217 offset:33536\n"
      "buffer_load_dwordx4 v211, s[44:47], s75 offen lds\n"
      "s_bfe_u32 s75, s87, 0x40008\n"
      "s_lshl_b32 s75, s75, 19\n"
      "s_add_u32 m0, s23, 151552\n"
      "ds_read_b64_tr_b16 a[28:29], v217 offset:41472\n"
      "ds_read_b64_tr_b16 a[30:31], v217 offset:41728\n"
      "ds_read_b64_tr_b16 a[32:33], v218 offset:32768\n"
      "ds_read_b64_tr_b16 a[34:35], v218 offset:33024\n"
      "buffer_load_dwordx4 v210, s[40:43], s74 offen lds\n"
      "s_bfe_u32 s74, s87, 0x40008\n"
      "s_lshl_b32 s74, s74, 7\n"
      "s_or_b32 s74, s74, s86\n"
      "s_add_u32 m0, s84, 32768\n"
      "ds_read_b64_tr_b16 a[36:37], v218 offset:40960\n"
      "ds_read_b64_tr_b16 a[38:39], v218 offset:41216\n"
      "ds_read_b64_tr_b16 a[40:41], v219 offset:32768\n"
      "ds_read_b64_tr_b16 a[42:43], v219 offset:33024\n"
      "buffer_load_dwordx4 v208, s[40:43], s73 offen lds\n"
      "s_add_u32 s73, s73, 8192\n"
      "s_add_u32 m0, s84, 33792\n"
      "ds_read_b64_tr_b16 a[44:45], v219 offset:40960\n"
      "ds_read_b64_tr_b16 a[46:47], v219 offset:41216\n"
      "ds_read_b64_tr_b16 a[48:49], v218 offset:33280\n"
      "ds_read_b64_tr_b16 a[50:51], v218 offset:33536\n"
      "buffer_load_dwordx4 v209, s[40:43], s73 offen lds\n"
      "s_add_u32 s73, s73, 8192\n"
      "s_and_b32 s73, s73, 0xffff\n"
      "s_add_u32 m0, s84, 34816\n"
      "ds_read_b64_tr_b16 a[52:53], v218 offset:41472\n"
      "ds_read_b64_tr_b16 a[54:55], v218 offset:41728\n"
      "ds_read_b64_tr_b16 a[56:57], v219 offset:33280\n"
      "ds_read_b64_tr_b16 a[58:59], v219 offset:33536\n"
      "buffer_load_dwordx4 v208, s[40:43], s73 offen lds\n"
      "s_add_u32 s73, s73, 8192\n"
      "s_add_u32 m0, s84, 35840\n"
      "ds_read_b64_tr_b16 a[60:61], v219 offset:41472\n"
      "ds_read_b64_tr_b16 a[62:63], v219 offset:41728\n"
      "buffer_load_dwordx4 v209, s[40:43], s73 offen lds\n"
      "s_add_u32 s73, s73, 8192\n"
      "s_and_b32 s73, s73, 0xffff\n"
      "s_add_u32 m0, s84, 36864\n"
      "s_nop 0\n"
      "buffer_load_dwordx4 v208, s[40:43], s73 offen lds\n"
      "s_add_u32 s73, s73, 8192\n"
      "s_add_u32 m0, s84, 37888\n"
      "s_nop 0\n"
      "buffer_load_dwordx4 v209, s[40:43], s73 offen lds\n"
      "s_add_u32 s73, s73, 8192\n"
      "s_and_b32 s73, s73, 0xffff\n"
      "s_waitcnt vmcnt(11) lgkmcnt(0)\n"
      "ds_read_b128 a[128:131], v212 offset:0\n"
      "ds_read_b128 a[132:135], v213 offset:0\n"
      "v_mfma_f32_16x16x32_bf16 v[192:195], a[0:3], a[192:195], 0\n"
      "s_add_u32 m0, s84, 38912\n"
      "ds_read_b128 a[144:147], v212 offset:2048\n"
      "v_mfma_f32_16x16x32_bf16 v[192:195], a[4:7], a[196:199], v[192:195]\n"
      "buffer_load_dwordx4 v208, s[40:43], s73 offen lds\n"
      "v_mfma_f32_16x16x32_bf16 v[196:199], a[8:11], a[192:195], 0\n"
      "s_add_u32 s73, s73, 8192\n"
      "ds_read_b128 a[148:151], v213 offset:2048\n"
      "v_mfma_f32_16x16x32_bf16 v[196:199], a[12:15], a[196:199], v[196:199]\n"
      "ds_read_b128 a[160:163], v212 offset:4096\n"
      "v_mfma_f32_16x16x32_bf16 v[200:203], a[16:19], a[192:195], 0\n"
      "s_add_u32 m0, s84, 39936\n"
      "ds_read_b128 a[164:167], v213 offset:4096\n"
      "v_mfma_f32_16x16x32_bf16 v[200:203], a[20:23], a[196:199], v[200:203]\n"
      "s_waitcnt lgkmcnt(4)\n"
      "buffer_load_dwordx4 v209, s[40:43], s73 offen lds\n"
      "v_mfma_f32_16x16x32_bf16 v[204:207], a[24:27], a[192:195], 0\n"
      "s_bfe_u32 s73, s87, 0x40008\n"
      "ds_read_b128 a[176:179], v212 offset:6144\n"
      "v_mfma_f32_16x16x32_bf16 v[204:207], a[28:31], a[196:199], v[204:207]\n"
      "s_lshl_b32 s73, s73, 7\n"
      "s_or_b32 s73, s73, s85\n"
      "ds_read_b128 a[180:183], v213 offset:6144\n"
      "v_mfma_f32_16x16x32_bf16 v[160:163], a[0:3], a[128:131], 0\n"
      "v_mfma_f32_16x16x32_bf16 v[160:163], a[4:7], a[132:135], v[160:163]\n"
      "s_add_u32 m0, s23, 16384\n"
      "v_mfma_f32_16x16x32_bf16 v[164:167], a[8:11], a[128:131], 0\n"
      "buffer_load_dwordx4 v211, s[44:47], s75 offen lds\n"
      "v_mfma_f32_16x16x32_bf16 v[164:167], a[12:15], a[132:135], v[164:167]\n"
      "s_add_u32 s75, s75, 131072\n"
      "v_mfma_f32_16x16x32_bf16 v[168:171], a[16:19], a[128:131], 0\n"
      "v_mfma_f32_16x16x32_bf16 v[168:171], a[20:23], a[132:135], v[168:171]\n"
      "s_add_u32 m0, s23, 20480\n"
      "v_mfma_f32_16x16x32_bf16 v[172:175], a[24:27], a[128:131], 0\n"
      "buffer_load_dwordx4 v211, s[44:47], s75 offen lds\n"
      "v_mfma_f32_16x16x32_bf16 v[172:175], a[28:31], a[132:135], v[172:175]\n"
      "s_add_u32 s75, s75, 131072\n"
      "v_mfma_f32_16x16x32_bf16 v[176:179], a[32:35], a[128:131], 0\n"
      "v_mfma_f32_16x16x32_bf16 v[176:179], a[36:39], a[132:135], v[176:179]\n"
      "v_mfma_f32_16x16x32_bf16 v[180:183], a[40:43], a[128:131], 0\n"
      "s_add_u32 m0, s23, 24576\n"
      "v_mfma_f32_16x16x32_bf16 v[180:183], a[44:47], a[132:135], v[180:183]\n"
      "buffer_load_dwordx4 v211, s[44:47], s75 offen lds\n"
      "v_mfma_f32_16x16x32_bf16 v[184:187], a[48:51], a[128:131], 0\n"
      "s_add_u32 s75, s75, 131072\n"
      "v_mfma_f32_16x16x32_bf16 v[184:187], a[52:55], a[132:135], v[184:187]\n"
      "s_waitcnt lgkmcnt(0)\n"
      "v_mfma_f32_16x16x32_bf16 v[188:191], a[56:59], a[128:131], 0\n"
      "s_add_u32 m0, s23, 28672\n"
      "v_mfma_f32_16x16x32_bf16 v[188:191], a[60:63], a[132:135], v[188:191]\n"
      "buffer_load_dwordx4 v211, s[44:47], s75 offen lds\n"
      "v_mfma_f32_16x16x32_bf16 v[0:3], a[0:3], a[144:147], 0\n"
      "s_bfe_u32 s75, s87, 0x4000c\n"
      "v_mfma_f32_16x16x32_bf16 v[0:3], a[4:7], a[148:151], v[0:3]\n"
      "s_lshl_b32 s75, s75, 19\n"
      "v_mfma_f32_16x16x32_bf16 v[4:7], a[8:11], a[144:147], 0\n"
      "v_mfma_f32_16x16x32_bf16 v[4:7], a[12:15], a[148:151], v[4:7]\n"
      "s_add_u32 m0, s23, 155648\n"
      "v_mfma_f32_16x16x32_bf16 v[8:11], a[16:19], a[144:147], 0\n"
      "buffer_load_dwordx4 v210, s[40:43], s74 offen lds\n"
      "v_mfma_f32_16x16x32_bf16 v[8:11], a[20:23], a[148:151], v[8:11]\n"
      "s_bfe_u32 s74, s87, 0x4000c\n"
      "v_mfma_f32_16x16x32_bf16 v[12:15], a[24:27], a[144:147], 0\n"
      "s_lshl_b32 s74, s74, 7\n"
      "s_or_b32 s74, s74, s86\n"
      "v_mfma_f32_16x16x32_bf16 v[12:15], a[28:31], a[148:151], v[12:15]\n"
      "s_waitcnt lgkmcnt(0)\n"
      "s_add_u32 m0, s84, 0\n"
      "v_mfma_f32_16x16x32_bf16 v[16:19], a[32:35], a[144:147], 0\n"
      "buffer_load_dwordx4 v208, s[40:43], s73 offen lds\n"
      "v_mfma_f32_16x16x32_bf16 v[16:19], a[36:39], a[148:151], v[16:19]\n"
      "s_add_u32 s73, s73, 8192\n"
      "v_mfma_f32_16x16x32_bf16 v[20:23], a[40:43], a[144:147], 0\n"
      "v_mfma_f32_16x16x32_bf16 v[20:23], a[44:47], a[148:151], v[20:23]\n"
      "v_mfma_f32_16x16x32_bf16 v[24:27], a[48:51], a[144:147], 0\n"
      "s_add_u32 m0, s84, 1024\n"
      "v_mfma_f32_16x16x32_bf16 v[24:27], a[52:55], a[148:151], v[24:27]\n"
      "buffer_load_dwordx4 v209, s[40:43], s73 offen lds\n"
      "v_mfma_f32_16x16x32_bf16 v[28:31], a[56:59], a[144:147], 0\n"
      "s_add_u32 s73, s73, 8192\n"
      "v_mfma_f32_16x16x32_bf16 v[28:31], a[60:63], a[148:151], v[28:31]\n"
      "s_and_b32 s73, s73, 0xffff\n"
      "v_mfma_f32_16x16x32_bf16 v[32:35], a[0:3], a[160:163], 0\n"
      "s_add_u32 m0, s84, 2048\n"
      "v_mfma_f32_16x16x32_bf16 v[32:35], a[4:7], a[164:167], v[32:35]\n"
      "buffer_load_dwordx4 v208, s[40:43], s73 offen lds\n"
      "v_mfma_f32_16x16x32_bf16 v[36:39], a[8:11], a[160:163], 0\n"
      "s_add_u32 s73, s73, 8192\n"
      "v_mfma_f32_16x16x32_bf16 v[36:39], a[12:15], a[164:167], v[36:39]\n"
      "v_mfma_f32_16x16x32_bf16 v[40:43], a[16:19], a[160:163], 0\n"
      "v_mfma_f32_16x16x32_bf16 v[40:43], a[20:23], a[164:167], v[40:43]\n"
      "s_add_u32 m0, s84, 3072\n"
      "s_waitcnt vmcnt(8) lgkmcnt(0)\n"
      "v_mfma_f32_16x16x32_bf16 v[44:47], a[24:27], a[160:163], 0\n"
      "s_barrier\n"
      "buffer_load_dwordx4 v209, s[40:43], s73 offen lds\n"
      "v_mfma_f32_16x16x32_bf16 v[44:47], a[28:31], a[164:167], v[44:47]\n"
      "s_add_u32 s73, s73, 8192\n"
      "ds_read_b128 a[216:219], v212 offset:32768\n"
      "v_mfma_f32_16x16x32_bf16 v[48:51], a[32:35], a[160:163], 0\n"
      "s_and_b32 s73, s73, 0xffff\n"
      "ds_read_b128 a[220:223], v213 offset:32768\n"
      "v_mfma_f32_16x16x32_bf16 v[48:51], a[36:39], a[164:167], v[48:51]\n"
      "s_add_u32 m0, s84, 4096\n"
      "ds_read_b128 a[224:227], v212 offset:34816\n"
      "ds_read_b128 a[228:231], v213 offset:34816\n"
      "v_mfma_f32_16x16x32_bf16 v[52:55], a[40:43], a[160:163], 0\n"
      "buffer_load_dwordx4 v208, s[40:43], s73 offen lds\n"
      "v_mfma_f32_16x16x32_bf16 v[52:55], a[44:47], a[164:167], v[52:55]\n"
      "s_add_u32 s73, s73, 8192\n"
      "ds_read_b128 a[232:235], v212 offset:36864\n"
      "v_mfma_f32_16x16x32_bf16 v[56:59], a[48:51], a[160:163], 0\n"
      "ds_read_b128 a[236:239], v213 offset:36864\n"
      "ds_read_b128 a[240:243], v212 offset:38912\n"
      "v_mfma_f32_16x16x32_bf16 v[56:59], a[52:55], a[164:167], v[56:59]\n"
      "ds_read_b128 a[244:247], v213 offset:38912\n"
      "v_mfma_f32_16x16x32_bf16 v[60:63], a[56:59], a[160:163], 0\n"
      "s_add_u32 m0, s84, 5120\n"
      "ds_read_b128 a[248:251], v214 offset:4096\n"
      "v_mfma_f32_16x16x32_bf16 v[60:63], a[60:63], a[164:167], v[60:63]\n"
      "buffer_load_dwordx4 v209, s[40:43], s73 offen lds\n"
      "v_mfma_f32_16x16x32_bf16 v[64:67], a[0:3], a[176:179], 0\n"
      "s_add_u32 s73, s73, 8192\n"
      "ds_read_b128 a[252:255], v215 offset:4096\n"
      "v_mfma_f32_16x16x32_bf16 v[64:67], a[4:7], a[180:183], v[64:67]\n"
      "s_and_b32 s73, s73, 0xffff\n"
      "ds_read_b64_tr_b16 a[64:65], v216 offset:49152\n"
      "v_mfma_f32_16x16x32_bf16 v[68:71], a[8:11], a[176:179], 0\n"
      "s_add_u32 m0, s84, 6144\n"
      "ds_read_b64_tr_b16 a[66:67], v216 offset:49408\n"
      "ds_read_b64_tr_b16 a[68:69], v216 offset:57344\n"
      "v_mfma_f32_16x16x32_bf16 v[68:71], a[12:15], a[180:183], v[68:71]\n"
      "buffer_load_dwordx4 v208, s[40:43], s73 offen lds\n"
      "v_mfma_f32_16x16x32_bf16 v[72:75], a[16:19], a[176:179], 0\n"
      "s_add_u32 s73, s73, 8192\n"
      "ds_read_b64_tr_b16 a[70:71], v216 offset:57600\n"
      "v_mfma_f32_16x16x32_bf16 v[72:75], a[20:23], a[180:183], v[72:75]\n"
      "ds_read_b64_tr_b16 a[72:73], v217 offset:49152\n"
      "ds_read_b64_tr_b16 a[74:75], v217 offset:49408\n"
      "v_mfma_f32_16x16x32_bf16 v[76:79], a[24:27], a[176:179], 0\n"
      "ds_read_b64_tr_b16 a[76:77], v217 offset:57344\n"
      "v_mfma_f32_16x16x32_bf16 v[76:79], a[28:31], a[180:183], v[76:79]\n"
      "s_add_u32 m0, s84, 7168\n"
      "ds_read_b64_tr_b16 a[78:79], v217 offset:57600\n"
      "v_mfma_f32_16x16x32_bf16 v[80:83], a[32:35], a[176:179], 0\n"
      "buffer_load_dwordx4 v209, s[40:43], s73 offen lds\n"
      "v_mfma_f32_16x16x32_bf16 v[80:83], a[36:39], a[180:183], v[80:83]\n"
      "s_bfe_u32 s73, s87, 0x4000c\n"
      "ds_read_b64_tr_b16 a[80:81], v216 offset:49664\n"
      "v_mfma_f32_16x16x32_bf16 v[84:87], a[40:43], a[176:179], 0\n"
      "s_lshl_b32 s73, s73, 7\n"
      "s_or_b32 s73, s73, s85\n"
      "ds_read_b64_tr_b16 a[82:83], v216 offset:49920\n"
      "v_mfma_f32_16x16x32_bf16 v[84:87], a[44:47], a[180:183], v[84:87]\n"
      "s_add_u32 m0, s23, 32768\n"
      "ds_read_b64_tr_b16 a[84:85], v216 offset:57856\n"
      "ds_read_b64_tr_b16 a[86:87], v216 offset:58112\n"
      "v_mfma_f32_16x16x32_bf16 v[88:91], a[48:51], a[176:179], 0\n"
      "buffer_load_dwordx4 v211, s[44:47], s75 offen lds\n"
      "v_mfma_f32_16x16x32_bf16 v[88:91], a[52:55], a[180:183], v[88:91]\n"
      "s_add_u32 s75, s75, 131072\n"
      "ds_read_b64_tr_b16 a[88:89], v217 offset:49664\n"
      "v_mfma_f32_16x16x32_bf16 v[92:95], a[56:59], a[176:179], 0\n"
      "ds_read_b64_tr_b16 a[90:91], v217 offset:49920\n"
      "ds_read_b64_tr_b16 a[92:93], v217 offset:57856\n"
      "v_mfma_f32_16x16x32_bf16 v[92:95], a[60:63], a[180:183], v[92:95]\n"
      "s_waitcnt lgkmcnt(6)\n"
      "ds_read_b64_tr_b16 a[94:95], v217 offset:58112\n"
      "v_mfma_f32_16x16x32_bf16 v[192:195], a[64:67], a[248:251], v[192:195]\n"
      "s_add_u32 m0, s23, 36864\n"
      "ds_read_b64_tr_b16 a[96:97], v218 offset:49152\n"
      "v_mfma_f32_16x16x32_bf16 v[192:195], a[68:71], a[252:255], v[192:195]\n"
      "buffer_load_dwordx4 v211, s[44:47], s75 offen lds\n"
      "v_mfma_f32_16x16x32_bf16 v[196:199], a[72:75], a[248:251], v[196:199]\n"
      "s_add_u32 s75, s75, 131072\n"
      "ds_read_b64_tr_b16 a[98:99], v218 offset:49408\n"
      "v_mfma_f32_16x16x32_bf16 v[196:199], a[76:79], a[252:255], v[196:199]\n"
      "s_waitcnt lgkmcnt(4)\n"
      "ds_read_b64_tr_b16 a[100:101], v218 offset:57344\n"
      "v_mfma_f32_16x16x32_bf16 v[200:203], a[80:83], a[248:251], v[200:203]\n"
      "s_waitcnt lgkmcnt(3)\n"
      "s_add_u32 m0, s23, 40960\n"
      "ds_read_b64_tr_b16 a[102:103], v218 offset:57600\n"
      "ds_read_b64_tr_b16 a[104:105], v219 offset:49152\n"
      "v_mfma_f32_16x16x32_bf16 v[200:203], a[84:87], a[252:255], v[200:203]\n"
      "buffer_load_dwordx4 v211, s[44:47], s75 offen lds\n"
      "v_mfma_f32_16x16x32_bf16 v[204:207], a[88:91], a[248:251], v[204:207]\n"
      "s_add_u32 s75, s75, 131072\n"
      "ds_read_b64_tr_b16 a[106:107], v219 offset:49408\n"
      "v_mfma_f32_16x16x32_bf16 v[204:207], a[92:95], a[252:255], v[204:207]\n"
      "ds_read_b64_tr_b16 a[108:109], v219 offset:57344\n"
      "ds_read_b64_tr_b16 a[110:111], v219 offset:57600\n"
      "v_mfma_f32_16x16x32_bf16 v[160:163], a[64:67], a[216:219], v[160:163]\n"
      "s_lshl_b32 s62, s59, 5\n"
      "s_add_u32 s62, s62, s60\n"
      "ds_read_b64_tr_b16 a[112:113], v218 offset:49664\n"
      "v_mfma_f32_16x16x32_bf16 v[160:163], a[68:71], a[220:223], v[160:163]\n"
      "s_add_u32 m0, s23, 45056\n"
      "s_lshl_b32 s63, s62, 19\n"
      "ds_read_b64_tr_b16 a[114:115], v218 offset:49920\n"
      "v_mfma_f32_16x16x32_bf16 v[164:167], a[72:75], a[216:219], v[164:167]\n"
      "s_add_u32 s24, s10, s63\n"
      "s_addc_u32 s25, s11, 0\n"
      "buffer_load_dwordx4 v211, s[44:47], s75 offen lds\n"
      "v_mfma_f32_16x16x32_bf16 v[164:167], a[76:79], a[220:223], v[164:167]\n"
      "s_bfe_u32 s75, s87, 0x40010\n"
      "s_mov_b32 s26, 0x80000\n"
      "ds_read_b64_tr_b16 a[116:117], v218 offset:57856\n"
      "v_mfma_f32_16x16x32_bf16 v[168:171], a[80:83], a[216:219], v[168:171]\n"
      "s_lshl_b32 s75, s75, 19\n"
      "s_mov_b32 s27, 0x20000\n"
      "ds_read_b64_tr_b16 a[118:119], v218 offset:58112\n"
      "v_mfma_f32_16x16x32_bf16 v[168:171], a[84:87], a[220:223], v[168:171]\n"
      "s_waitcnt lgkmcnt(4)\n"
      "s_add_u32 m0, s23, 147456\n"
      "ds_read_b64_tr_b16 a[120:121], v219 offset:49664\n"
      "ds_read_b64_tr_b16 a[122:123], v219 offset:49920\n"
      "v_mfma_f32_16x16x32_bf16 v[172:175], a[88:91], a[216:219], v[172:175]\n"
      "s_add_u32 s28, s12, s63\n"
      "s_addc_u32 s29, s13, 0\n"
      "buffer_load_dwordx4 v210, s[40:43], s74 offen lds\n"
      "v_mfma_f32_16x16x32_bf16 v[172:175], a[92:95], a[220:223], v[172:175]\n"
      "s_bfe_u32 s74, s87, 0x40010\n"
      "s_mov_b32 s30, 0x80000\n"
      "ds_read_b64_tr_b16 a[124:125], v219 offset:57856\n"
      "v_mfma_f32_16x16x32_bf16 v[176:179], a[96:99], a[216:219], v[176:179]\n"
      "s_lshl_b32 s74, s74, 7\n"
      "s_or_b32 s74, s74, s86\n"
      "ds_read_b64_tr_b16 a[126:127], v219 offset:58112\n"
      "v_mfma_f32_16x16x32_bf16 v[176:179], a[100:103], a[220:223], v[176:179]\n"
      "s_mov_b32 s31, 0x20000\n"
      "s_mov_b32 s38, 31719424\n"
      "s_mov_b32 s39, 0x20000\n"
      "v_mfma_f32_16x16x32_bf16 v[180:183], a[104:107], a[216:219], v[180:183]\n"
      "s_waitcnt lgkmcnt(4)\n"
      "s_add_u32 m0, s84, 32768\n"
      "s_lshr_b32 s65, s22, 1\n"
      "v_mfma_f32_16x16x32_bf16 v[180:183], a[108:111], a[220:223], v[180:183]\n"
      "s_waitcnt lgkmcnt(2)\n"
      "s_lshl_b32 s64, s65, 11\n"
      "buffer_load_dwordx4 v208, s[40:43], s73 offen lds\n"
      "v_mfma_f32_16x16x32_bf16 v[184:187], a[112:115], a[216:219], v[184:187]\n"
      "s_waitcnt lgkmcnt(0)\n"
      "s_add_u32 s73, s73, 8192\n"
      "s_lshl_b32 s66, s61, 7\n"
      "s_add_u32 s64, s64, s66\n"
      "v_mfma_f32_16x16x32_bf16 v[184:187], a[116:119], a[220:223], v[184:187]\n"
      "s_lshl_b32 s65, s65, 2\n"
      "v_lshrrev_b32 v220, 3, v255\n"
      "v_lshrrev_b32 v222, 4, v255\n"
      "v_mfma_f32_16x16x32_bf16 v[188:191], a[120:123], a[216:219], v[188:191]\n"
      "s_add_u32 m0, s84, 33792\n"
      "v_add_u32 v222, s65, v222\n"
      "v_and_b32 v221, 7, v255\n"
      "v_mfma_f32_16x16x32_bf16 v[188:191], a[124:127], a[220:223], v[188:191]\n"
      "buffer_load_dwordx4 v209, s[40:43], s73 offen lds\n"
      "v_mfma_f32_16x16x32_bf16 v[0:3], a[64:67], a[224:227], v[0:3]\n"
      "s_add_u32 s73, s73, 8192\n"
      "v_xor_b32 v221, v221, v222\n"
      "v_lshlrev_b32 v220, 8, v220\n"
      "v_mfma_f32_16x16x32_bf16 v[0:3], a[68:71], a[228:231], v[0:3]\n"
      "s_and_b32 s73, s73, 0xffff\n"
      "v_lshl_add_u32 v220, v221, 4, v220\n"
      "v_add_u32 v236, s64, v220\n"
      "v_mfma_f32_16x16x32_bf16 v[4:7], a[72:75], a[224:227], v[4:7]\n"
      "v_bfe_u32 v220, v255, 2, 3\n"
      "v_bfe_u32 v222, v255, 4, 1\n"
      "v_mfma_f32_16x16x32_bf16 v[4:7], a[76:79], a[228:231], v[4:7]\n"
      "s_add_u32 m0, s84, 34816\n"
      "v_lshlrev_b32 v222, 1, v222\n"
      "v_and_b32 v221, 3, v255\n"
      "v_mfma_f32_16x16x32_bf16 v[8:11], a[80:83], a[224:227], v[8:11]\n"
      "buffer_load_dwordx4 v208, s[40:43], s73 offen lds\n"
      "v_mfma_f32_16x16x32_bf16 v[8:11], a[84:87], a[228:231], v[8:11]\n"
      "s_add_u32 s73, s73, 8192\n"
      "v_xor_b32 v221, v221, v222\n"
      "v_lshrrev_b32 v222, 5, v255\n"
      "v_mfma_f32_16x16x32_bf16 v[12:15], a[88:91], a[224:227], v[12:15]\n"
      "v_lshl_add_u32 v221, v222, 2, v221\n"
      "v_lshlrev_b32 v220, 8, v220\n"
      "v_mfma_f32_16x16x32_bf16 v[12:15], a[92:95], a[228:231], v[12:15]\n"
      "s_add_u32 m0, s84, 35840\n"
      "v_lshl_add_u32 v220, v221, 4, v220\n"
      "v_add_u32 v237, s64, v220\n"
      "v_mfma_f32_16x16x32_bf16 v[16:19], a[96:99], a[224:227], v[16:19]\n"
      "s_mov_b32 s56, 102400\n"
      "buffer_load_dwordx4 v209, s[40:43], s73 offen lds\n"
      "v_mfma_f32_16x16x32_bf16 v[16:19], a[100:103], a[228:231], v[16:19]\n"
      "s_add_u32 s73, s73, 8192\n"
      "s_waitcnt vmcnt(9) lgkmcnt(0)\n"
      "s_mov_b32 s57, 102400\n"
      "v_mfma_f32_16x16x32_bf16 v[20:23], a[104:107], a[224:227], v[20:23]\n"
      "s_and_b32 s73, s73, 0xffff\n"
      "s_barrier\n"
      "ds_read_b128 a[128:131], v212 offset:0\n"
      "v_mfma_f32_16x16x32_bf16 v[20:23], a[108:111], a[228:231], v[20:23]\n"
      "s_lshr_b32 s94, s60, 3\n"
      "s_lshl_b32 s62, s59, 5\n"
      "ds_read_b128 a[132:135], v213 offset:0\n"
      "v_mfma_f32_16x16x32_bf16 v[24:27], a[112:115], a[224:227], v[24:27]\n"
      "s_add_u32 m0, s84, 36864\n"
      "s_add_u32 s63, s62, s60\n"
      "ds_read_b128 a[144:147], v212 offset:2048\n"
      "v_mfma_f32_16x16x32_bf16 v[24:27], a[116:119], a[228:231], v[24:27]\n"
      "s_lshl_b32 s64, s63, 2\n"
      "buffer_load_dwordx4 v208, s[40:43], s73 offen lds\n"
      "v_mfma_f32_16x16x32_bf16 v[28:31], a[120:123], a[224:227], v[28:31]\n"
      "s_add_u32 s73, s73, 8192\n"
      "s_add_u32 s64, s64, s22\n"
      "ds_read_b128 a[148:151], v213 offset:2048\n"
      "v_mfma_f32_16x16x32_bf16 v[28:31], a[124:127], a[228:231], v[28:31]\n"
      "s_lshl_b32 s64, s64, 7\n"
      "s_add_u32 s89, s64, 31457280\n"
      "ds_read_b128 a[160:163], v212 offset:4096\n"
      "v_mfma_f32_16x16x32_bf16 v[32:35], a[64:67], a[232:235], v[32:35]\n"
      "s_add_u32 m0, s84, 37888\n"
      "s_add_u32 s90, s64, 31588352\n"
      "ds_read_b128 a[164:167], v213 offset:4096\n"
      "v_mfma_f32_16x16x32_bf16 v[32:35], a[68:71], a[236:239], v[32:35]\n"
      "s_and_b32 s64, s63, 0xf8\n"
      "buffer_load_dwordx4 v209, s[40:43], s73 offen lds\n"
      "v_mfma_f32_16x16x32_bf16 v[36:39], a[72:75], a[232:235], v[36:39]\n"
      "s_add_u32 s73, s73, 8192\n"
      "s_lshl_b32 s64, s64, 2\n"
      "ds_read_b128 a[176:179], v212 offset:6144\n"
      "v_mfma_f32_16x16x32_bf16 v[36:39], a[76:79], a[236:239], v[36:39]\n"
      "s_and_b32 s73, s73, 0xffff\n"
      "s_and_b32 s65, s60, 1\n"
      "ds_read_b128 a[180:183], v213 offset:6144\n"
      "v_mfma_f32_16x16x32_bf16 v[40:43], a[80:83], a[232:235], v[40:43]\n"
      "s_lshl_b32 s65, s65, 1\n"
      "s_add_u32 s64, s64, s65\n"
      "ds_read_b128 a[192:195], v214 offset:8192\n"
      "v_mfma_f32_16x16x32_bf16 v[40:43], a[84:87], a[236:239], v[40:43]\n"
      "s_add_u32 m0, s84, 38912\n"
      "s_lshl_b32 s64, s64, 7\n"
      "ds_read_b128 a[196:199], v215 offset:8192\n"
      "v_mfma_f32_16x16x32_bf16 v[44:47], a[88:91], a[232:235], v[44:47]\n"
      "s_add_u32 s64, s64, 31457280\n"
      "buffer_load_dwordx4 v208, s[40:43], s73 offen lds\n"
      "v_mfma_f32_16x16x32_bf16 v[44:47], a[92:95], a[236:239], v[44:47]\n"
      "s_add_u32 s73, s73, 8192\n"
      "ds_read_b64_tr_b16 a[0:1], v216 offset:16384\n"
      "v_lshrrev_b32 v220, 1, v255\n"
      "v_mfma_f32_16x16x32_bf16 v[48:51], a[96:99], a[232:235], v[48:51]\n"
      "ds_read_b64_tr_b16 a[2:3], v216 offset:16640\n"
      "v_and_b32 v221, 1, v255\n"
      "v_mfma_f32_16x16x32_bf16 v[48:51], a[100:103], a[236:239], v[48:51]\n"
      "s_add_u32 m0, s84, 39936\n"
      "ds_read_b64_tr_b16 a[4:5], v216 offset:24576\n"
      "v_lshl_add_u32 v220, v220, 2, v221\n"
      "v_mfma_f32_16x16x32_bf16 v[52:55], a[104:107], a[232:235], v[52:55]\n"
      "buffer_load_dwordx4 v209, s[40:43], s73 offen lds\n"
      "v_mfma_f32_16x16x32_bf16 v[52:55], a[108:111], a[236:239], v[52:55]\n"
      "s_bfe_u32 s73, s87, 0x40010\n"
      "ds_read_b64_tr_b16 a[6:7], v216 offset:24832\n"
      "v_lshlrev_b32 v220, 7, v220\n"
      "v_mfma_f32_16x16x32_bf16 v[56:59], a[112:115], a[232:235], v[56:59]\n"
      "s_lshl_b32 s73, s73, 7\n"
      "s_or_b32 s73, s73, s85\n"
      "ds_read_b64_tr_b16 a[8:9], v217 offset:16384\n"
      "v_mfma_f32_16x16x32_bf16 v[56:59], a[116:119], a[236:239], v[56:59]\n"
      "ds_read_b64_tr_b16 a[10:11], v217 offset:16640\n"
      "v_add_u32 v253, s64, v220\n"
      "v_mfma_f32_16x16x32_bf16 v[60:63], a[120:123], a[232:235], v[60:63]\n"
      "s_add_u32 m0, s23, 49152\n"
      "s_and_b32 s64, s63, 0xe7\n"
      "ds_read_b64_tr_b16 a[12:13], v217 offset:24576\n"
      "v_mfma_f32_16x16x32_bf16 v[60:63], a[124:127], a[236:239], v[60:63]\n"
      "s_lshl_b32 s64, s64, 2\n"
      "buffer_load_dwordx4 v211, s[44:47], s75 offen lds\n"
      "v_mfma_f32_16x16x32_bf16 v[64:67], a[64:67], a[240:243], v[64:67]\n"
      "s_add_u32 s75, s75, 131072\n"
      "s_add_u32 s64, s64, s22\n"
      "ds_read_b64_tr_b16 a[14:15], v217 offset:24832\n"
      "v_mfma_f32_16x16x32_bf16 v[64:67], a[68:71], a[244:247], v[64:67]\n"
      "s_lshl_b32 s64, s64, 7\n"
      "s_add_u32 s64, s64, 31522816\n"
      "ds_read_b64_tr_b16 a[16:17], v216 offset:16896\n"
      "v_mfma_f32_16x16x32_bf16 v[68:71], a[72:75], a[240:243], v[68:71]\n"
      "s_add_u32 m0, s23, 53248\n"
      "ds_read_b64_tr_b16 a[18:19], v216 offset:17152\n"
      "v_lshlrev_b32 v220, 12, v255\n"
      "v_mfma_f32_16x16x32_bf16 v[68:71], a[76:79], a[244:247], v[68:71]\n"
      "buffer_load_dwordx4 v211, s[44:47], s75 offen lds\n"
      "v_mfma_f32_16x16x32_bf16 v[72:75], a[80:83], a[240:243], v[72:75]\n"
      "s_add_u32 s75, s75, 131072\n"
      "ds_read_b64_tr_b16 a[20:21], v216 offset:25088\n"
      "v_add_u32 v220, s64, v220\n"
      "v_mfma_f32_16x16x32_bf16 v[72:75], a[84:87], a[244:247], v[72:75]\n"
      "ds_read_b64_tr_b16 a[22:23], v216 offset:25344\n"
      "v_cmp_lt_u32 vcc, 15, v255\n"
      "v_mfma_f32_16x16x32_bf16 v[76:79], a[88:91], a[240:243], v[76:79]\n"
      "ds_read_b64_tr_b16 a[24:25], v217 offset:16896\n"
      "v_cndmask_b32 v253, v253, v220, vcc\n"
      "v_mfma_f32_16x16x32_bf16 v[76:79], a[92:95], a[244:247], v[76:79]\n"
      "s_add_u32 m0, s23, 57344\n"
      "ds_read_b64_tr_b16 a[26:27], v217 offset:17152\n"
      "v_mov_b32 v220, 0x7ffffff0\n"
      "v_mfma_f32_16x16x32_bf16 v[80:83], a[96:99], a[240:243], v[80:83]\n"
      "buffer_load_dwordx4 v211, s[44:47], s75 offen lds\n"
      "v_mfma_f32_16x16x32_bf16 v[80:83], a[100:103], a[244:247], v[80:83]\n"
      "s_add_u32 s75, s75, 131072\n"
      "ds_read_b64_tr_b16 a[28:29], v217 offset:25088\n"
      "v_cmp_lt_u32 vcc, 19, v255\n"
      "v_mfma_f32_16x16x32_bf16 v[84:87], a[104:107], a[240:243], v[84:87]\n"
      "ds_read_b64_tr_b16 a[30:31], v217 offset:25344\n"
      "v_cndmask_b32 v253, v253, v220, vcc\n"
      "v_mfma_f32_16x16x32_bf16 v[84:87], a[108:111], a[244:247], v[84:87]\n"
      "s_add_u32 m0, s23, 61440\n"
      "ds_read_b64_tr_b16 a[32:33], v218 offset:16384\n"
      "v_writelane_b32 v253, s89, 20\n"
      "v_mfma_f32_16x16x32_bf16 v[88:91], a[112:115], a[240:243], v[88:91]\n"
      "s_lshl_b32 s64, s94, 20\n"
      "buffer_load_dwordx4 v211, s[44:47], s75 offen lds\n"
      "v_mfma_f32_16x16x32_bf16 v[88:91], a[116:119], a[244:247], v[88:91]\n"
      "s_waitcnt lgkmcnt(3)\n"
      "s_bfe_u32 s75, s87, 0x40014\n"
      "s_bfe_u32 s65, s60, 0x20001\n"
      "ds_read_b64_tr_b16 a[34:35], v218 offset:16640\n"
      "v_mfma_f32_16x16x32_bf16 v[92:95], a[120:123], a[240:243], v[92:95]\n"
      "s_lshl_b32 s75, s75, 19\n"
      "s_lshl_b32 s65, s65, 8\n"
      "ds_read_b64_tr_b16 a[36:37], v218 offset:24576\n"
      "v_mfma_f32_16x16x32_bf16 v[92:95], a[124:127], a[244:247], v[92:95]\n"
      "s_add_u32 s64, s64, s65\n"
      "s_add_u32 s48, s16, s64\n"
      "s_addc_u32 s49, s17, 0\n"
      "ds_read_b64_tr_b16 a[38:39], v218 offset:24832\n"
      "v_mfma_f32_16x16x32_bf16 v[192:195], a[0:3], a[192:195], v[192:195]\n"
      "s_add_u32 m0, s23, 151552\n"
      "s_sub_u32 s50, 4194304, s64\n"
      "ds_read_b64_tr_b16 a[40:41], v219 offset:16384\n"
      "v_mfma_f32_16x16x32_bf16 v[192:195], a[4:7], a[196:199], v[192:195]\n"
      "s_mov_b32 s51, 0x20000\n"
      "buffer_load_dwordx4 v210, s[40:43], s74 offen lds\n"
      "v_mfma_f32_16x16x32_bf16 v[196:199], a[8:11], a[192:195], v[196:199]\n"
      "s_bfe_u32 s74, s87, 0x40014\n"
      "s_mov_b64 s[52:53], s[18:19]\n"
      "ds_read_b64_tr_b16 a[42:43], v219 offset:16640\n"
      "v_mfma_f32_16x16x32_bf16 v[196:199], a[12:15], a[196:199], v[196:199]\n"
      "s_lshl_b32 s74, s74, 7\n"
      "s_or_b32 s74, s74, s86\n"
      "ds_read_b64_tr_b16 a[44:45], v219 offset:24576\n"
      "v_mfma_f32_16x16x32_bf16 v[200:203], a[16:19], a[192:195], v[200:203]\n"
      "s_waitcnt lgkmcnt(4)\n"
      "s_add_u32 m0, s84, 0\n"
      "ds_read_b64_tr_b16 a[46:47], v219 offset:24832\n"
      "v_mfma_f32_16x16x32_bf16 v[200:203], a[20:23], a[196:199], v[200:203]\n"
      "s_mov_b32 s54, 2359296\n"
      "buffer_load_dwordx4 v208, s[40:43], s73 offen lds\n"
      "v_mfma_f32_16x16x32_bf16 v[204:207], a[24:27], a[192:195], v[204:207]\n"
      "s_add_u32 s73, s73, 8192\n"
      "s_mov_b32 s55, 0x20000\n"
      "ds_read_b64_tr_b16 a[48:49], v218 offset:16896\n"
      "v_mfma_f32_16x16x32_bf16 v[204:207], a[28:31], a[196:199], v[204:207]\n"
      "ds_read_b64_tr_b16 a[50:51], v218 offset:17152\n"
      "v_bfe_u32 v220, v237, 8, 2\n"
      "v_mfma_f32_16x16x32_bf16 v[160:163], a[0:3], a[128:131], v[160:163]\n"
      "ds_read_b64_tr_b16 a[52:53], v218 offset:25088\n"
      "v_bfe_u32 v221, v237, 10, 2\n"
      "v_mfma_f32_16x16x32_bf16 v[160:163], a[4:7], a[132:135], v[160:163]\n"
      "s_add_u32 m0, s84, 1024\n"
      "ds_read_b64_tr_b16 a[54:55], v218 offset:25344\n"
      "v_lshl_add_u32 v220, v220, 4, v221\n"
      "v_mfma_f32_16x16x32_bf16 v[164:167], a[8:11], a[128:131], v[164:167]\n"
      "buffer_load_dwordx4 v209, s[40:43], s73 offen lds\n"
      "v_mfma_f32_16x16x32_bf16 v[164:167], a[12:15], a[132:135], v[164:167]\n"
      "s_add_u32 s73, s73, 8192\n"
      "ds_read_b64_tr_b16 a[56:57], v219 offset:16896\n"
      "v_and_b32 v221, 0xff, v237\n"
      "v_mfma_f32_16x16x32_bf16 v[168:171], a[16:19], a[128:131], v[168:171]\n"
      "s_and_b32 s73, s73, 0xffff\n"
      "ds_read_b64_tr_b16 a[58:59], v219 offset:17152\n"
      "v_lshl_add_u32 v250, v220, 10, v221\n"
      "v_mfma_f32_16x16x32_bf16 v[168:171], a[20:23], a[132:135], v[168:171]\n"
      "s_add_u32 m0, s84, 2048\n"
      "s_lshl_b32 s64, s59, 1\n"
      "ds_read_b64_tr_b16 a[60:61], v219 offset:25088\n"
      "v_mfma_f32_16x16x32_bf16 v[172:175], a[24:27], a[128:131], v[172:175]\n"
      "s_waitcnt lgkmcnt(4)\n"
      "s_and_b32 s65, s60, 1\n"
      "buffer_load_dwordx4 v208, s[40:43], s73 offen lds\n"
      "v_mfma_f32_16x16x32_bf16 v[172:175], a[28:31], a[132:135], v[172:175]\n"
      "s_add_u32 s73, s73, 8192\n"
      "s_add_u32 s64, s64, s65\n"
      "ds_read_b64_tr_b16 a[62:63], v219 offset:25344\n"
      "v_mfma_f32_16x16x32_bf16 v[176:179], a[32:35], a[128:131], v[176:179]\n"
      "s_and_b32 s65, s60, 2\n"
      "s_lshl_b32 s65, s65, 1\n"
      "s_add_u32 s64, s64, s65\n"
      "v_mfma_f32_16x16x32_bf16 v[176:179], a[36:39], a[132:135], v[176:179]\n"
      "s_and_b32 s64, s64, 7\n"
      "s_lshl_b32 s93, s64, 17\n"
      "v_bfe_u32 v220, v255, 2, 2\n"
      "v_mfma_f32_16x16x32_bf16 v[180:183], a[40:43], a[128:131], v[180:183]\n"
      "s_waitcnt lgkmcnt(2)\n"
      "s_add_u32 m0, s84, 3072\n"
      "v_xor_b32 v221, s22, v220\n"
      "v_lshlrev_b32 v220, 15, v220\n"
      "v_mfma_f32_16x16x32_bf16 v[180:183], a[44:47], a[132:135], v[180:183]\n"
      "buffer_load_dwordx4 v209, s[40:43], s73 offen lds\n"
      "v_mfma_f32_16x16x32_bf16 v[184:187], a[48:51], a[128:131], v[184:187]\n"
      "s_add_u32 s73, s73, 8192\n"
      "v_lshl_add_u32 v220, v221, 8, v220\n"
      "v_lshrrev_b32 v221, 4, v255\n"
      "v_mfma_f32_16x16x32_bf16 v[184:187], a[52:55], a[132:135], v[184:187]\n"
      "s_and_b32 s73, s73, 0xffff\n"
      "s_waitcnt vmcnt(9) lgkmcnt(0)\n"
      "v_lshl_add_u32 v220, v221, 6, v220\n"
      "v_mfma_f32_16x16x32_bf16 v[188:191], a[56:59], a[128:131], v[188:191]\n"
      "s_add_u32 m0, s84, 4096\n"
      "s_barrier\n"
      "ds_read_b128 a[216:219], v212 offset:32768\n"
      "v_mfma_f32_16x16x32_bf16 v[188:191], a[60:63], a[132:135], v[188:191]\n"
      "buffer_load_dwordx4 v208, s[40:43], s73 offen lds\n"
      "v_mfma_f32_16x16x32_bf16 v[0:3], a[0:3], a[144:147], v[0:3]\n"
      "s_add_u32 s73, s73, 8192\n"
      "ds_read_b128 a[220:223], v213 offset:32768\n"
      "v_and_b32 v221, 3, v255\n"
      "v_mfma_f32_16x16x32_bf16 v[0:3], a[4:7], a[148:151], v[0:3]\n"
      "ds_read_b128 a[224:227], v212 offset:34816\n"
      "v_lshl_add_u32 v220, v221, 4, v220\n"
      "v_mfma_f32_16x16x32_bf16 v[4:7], a[8:11], a[144:147], v[4:7]\n"
      "s_sub_u32 s62, 0, s22\n"
      "s_and_b32 s62, s62, 3\n"
      "ds_read_b128 a[228:231], v213 offset:34816\n"
      "v_mfma_f32_16x16x32_bf16 v[4:7], a[12:15], a[148:151], v[4:7]\n"
      "s_add_u32 m0, s84, 5120\n"
      "s_lshl_b32 s62, s62, 4\n"
      "ds_read_b128 a[232:235], v212 offset:36864\n"
      "v_mfma_f32_16x16x32_bf16 v[8:11], a[16:19], a[144:147], v[8:11]\n"
      "s_lshl_b32 s63, s61, 5\n"
      "buffer_load_dwordx4 v209, s[40:43], s73 offen lds\n"
      "v_mfma_f32_16x16x32_bf16 v[8:11], a[20:23], a[148:151], v[8:11]\n"
      "s_add_u32 s73, s73, 8192\n"
      "ds_read_b128 a[236:239], v213 offset:36864\n"
      "v_xor_b32 v236, s62, v220\n"
      "v_mfma_f32_16x16x32_bf16 v[12:15], a[24:27], a[144:147], v[12:15]\n"
      "s_and_b32 s73, s73, 0xffff\n"
      "ds_read_b128 a[240:243], v212 offset:38912\n"
      "v_xor_b32 v237, s63, v220\n"
      "v_mfma_f32_16x16x32_bf16 v[12:15], a[28:31], a[148:151], v[12:15]\n"
      "s_add_u32 m0, s84, 6144\n"
      "s_lshl_b32 s62, s61, 13\n"
      "ds_read_b128 a[244:247], v213 offset:38912\n"
      "v_mfma_f32_16x16x32_bf16 v[16:19], a[32:35], a[144:147], v[16:19]\n"
      "s_xor_b32 s63, s62, 0x2000\n"
      "buffer_load_dwordx4 v208, s[40:43], s73 offen lds\n"
      "v_mfma_f32_16x16x32_bf16 v[16:19], a[36:39], a[148:151], v[16:19]\n"
      "s_add_u32 s73, s73, 8192\n"
      "ds_read_b128 a[248:251], v214 offset:0\n"
      "v_bfe_u32 v220, v255, 2, 2\n"
      "v_mfma_f32_16x16x32_bf16 v[20:23], a[40:43], a[144:147], v[20:23]\n"
      "ds_read_b128 a[252:255], v215 offset:0\n"
      "v_sub_u32 v222, 0, v220\n"
      "v_mfma_f32_16x16x32_bf16 v[20:23], a[44:47], a[148:151], v[20:23]\n"
      "ds_read_b64_tr_b16 a[64:65], v216 offset:32768\n"
      "v_lshlrev_b32 v220, 10, v220\n"
      "v_mfma_f32_16x16x32_bf16 v[24:27], a[48:51], a[144:147], v[24:27]\n"
      "s_add_u32 m0, s84, 7168\n"
      "ds_read_b64_tr_b16 a[66:67], v216 offset:33024\n"
      "v_and_b32 v221, 3, v255\n"
      "v_mfma_f32_16x16x32_bf16 v[24:27], a[52:55], a[148:151], v[24:27]\n"
      "buffer_load_dwordx4 v209, s[40:43], s73 offen lds\n"
      "v_mfma_f32_16x16x32_bf16 v[28:31], a[56:59], a[144:147], v[28:31]\n"
      "s_bfe_u32 s73, s87, 0x40014\n"
      "ds_read_b64_tr_b16 a[68:69], v216 offset:40960\n"
      "v_lshl_add_u32 v220, v221, 6, v220\n"
      "v_mfma_f32_16x16x32_bf16 v[28:31], a[60:63], a[148:151], v[28:31]\n"
      "s_lshl_b32 s73, s73, 7\n"
      "s_or_b32 s73, s73, s85\n"
      "ds_read_b64_tr_b16 a[70:71], v216 offset:41216\n"
      "v_mfma_f32_16x16x32_bf16 v[32:35], a[0:3], a[160:163], v[32:35]\n"
      "s_add_u32 m0, s23, 16384\n"
      "ds_read_b64_tr_b16 a[72:73], v217 offset:32768\n"
      "v_lshrrev_b32 v221, 4, v255\n"
      "v_mfma_f32_16x16x32_bf16 v[32:35], a[4:7], a[164:167], v[32:35]\n"
      "buffer_load_dwordx4 v211, s[44:47], s75 offen lds\n"
      "v_mfma_f32_16x16x32_bf16 v[36:39], a[8:11], a[160:163], v[36:39]\n"
      "s_add_u32 s75, s75, 131072\n"
      "ds_read_b64_tr_b16 a[74:75], v217 offset:33024\n"
      "v_xor_b32 v221, v221, v222\n"
      "v_mfma_f32_16x16x32_bf16 v[36:39], a[12:15], a[164:167], v[36:39]\n"
      "ds_read_b64_tr_b16 a[76:77], v217 offset:40960\n"
      "v_and_b32 v221, 3, v221\n"
      "v_mfma_f32_16x16x32_bf16 v[40:43], a[16:19], a[160:163], v[40:43]\n"
      "ds_read_b64_tr_b16 a[78:79], v217 offset:41216\n"
      "v_lshl_add_u32 v245, v221, 4, v220\n"
      "v_mfma_f32_16x16x32_bf16 v[40:43], a[20:23], a[164:167], v[40:43]\n"
      "s_add_u32 m0, s23, 20480\n"
      "ds_read_b64_tr_b16 a[80:81], v216 offset:33280\n"
      "v_mov_b32 v246, v245\n"
      "v_mfma_f32_16x16x32_bf16 v[44:47], a[24:27], a[160:163], v[44:47]\n"
      "buffer_load_dwordx4 v211, s[44:47], s75 offen lds\n"
      "v_mfma_f32_16x16x32_bf16 v[44:47], a[28:31], a[164:167], v[44:47]\n"
      "s_add_u32 s75, s75, 131072\n"
      "ds_read_b64_tr_b16 a[82:83], v216 offset:33536\n"
      "v_add_u32 v228, s63, v245\n"
      "v_mfma_f32_16x16x32_bf16 v[48:51], a[32:35], a[160:163], v[48:51]\n"
      "ds_read_b64_tr_b16 a[84:85], v216 offset:41472\n"
      "v_add_u32 v230, s62, v245\n"
      "v_mfma_f32_16x16x32_bf16 v[48:51], a[36:39], a[164:167], v[48:51]\n"
      "s_add_u32 m0, s23, 24576\n"
      "ds_read_b64_tr_b16 a[86:87], v216 offset:41728\n"
      "v_add_u32 v229, s63, v246\n"
      "v_mfma_f32_16x16x32_bf16 v[52:55], a[40:43], a[160:163], v[52:55]\n"
      "buffer_load_dwordx4 v211, s[44:47], s75 offen lds\n"
      "v_mfma_f32_16x16x32_bf16 v[52:55], a[44:47], a[164:167], v[52:55]\n"
      "s_add_u32 s75, s75, 131072\n"
      "ds_read_b64_tr_b16 a[88:89], v217 offset:33280\n"
      "v_add_u32 v231, s62, v246\n"
      "v_mfma_f32_16x16x32_bf16 v[56:59], a[48:51], a[160:163], v[56:59]\n"
      "s_add_u32 s62, s62, 65536\n"
      "s_add_u32 s63, s63, 65536\n"
      "ds_read_b64_tr_b16 a[90:91], v217 offset:33536\n"
      "v_mfma_f32_16x16x32_bf16 v[56:59], a[52:55], a[164:167], v[56:59]\n"
      "ds_read_b64_tr_b16 a[92:93], v217 offset:41472\n"
      "v_lshrrev_b32 v220, 4, v255\n"
      "v_mfma_f32_16x16x32_bf16 v[60:63], a[56:59], a[160:163], v[60:63]\n"
      "s_add_u32 m0, s23, 28672\n"
      "ds_read_b64_tr_b16 a[94:95], v217 offset:41728\n"
      "v_lshlrev_b32 v220, 10, v220\n"
      "v_mfma_f32_16x16x32_bf16 v[60:63], a[60:63], a[164:167], v[60:63]\n"
      "buffer_load_dwordx4 v211, s[44:47], s75 offen lds\n"
      "v_mfma_f32_16x16x32_bf16 v[64:67], a[0:3], a[176:179], v[64:67]\n"
      "s_bfe_u32 s75, s87, 0x40018\n"
      "ds_read_b64_tr_b16 a[96:97], v218 offset:32768\n"
      "v_bfe_u32 v221, v255, 2, 2\n"
      "v_mfma_f32_16x16x32_bf16 v[64:67], a[4:7], a[180:183], v[64:67]\n"
      "s_lshl_b32 s75, s75, 19\n"
      "ds_read_b64_tr_b16 a[98:99], v218 offset:33024\n"
      "v_lshl_add_u32 v220, v221, 6, v220\n"
      "v_mfma_f32_16x16x32_bf16 v[68:71], a[8:11], a[176:179], v[68:71]\n"
      "s_add_u32 m0, s23, 155648\n"
      "ds_read_b64_tr_b16 a[100:101], v218 offset:40960\n"
      "v_bfe_u32 v221, v255, 4, 1\n"
      "v_mfma_f32_16x16x32_bf16 v[68:71], a[12:15], a[180:183], v[68:71]\n"
      "buffer_load_dwordx4 v210, s[40:43], s74 offen lds\n"
      "v_mfma_f32_16x16x32_bf16 v[72:75], a[16:19], a[176:179], v[72:75]\n"
      "s_bfe_u32 s74, s87, 0x40018\n"
      "ds_read_b64_tr_b16 a[102:103], v218 offset:41216\n"
      "v_lshl_add_u32 v220, v221, 5, v220\n"
      "v_mfma_f32_16x16x32_bf16 v[72:75], a[20:23], a[180:183], v[72:75]\n"
      "s_lshl_b32 s74, s74, 7\n"
      "s_or_b32 s74, s74, s86\n"
      "ds_read_b64_tr_b16 a[104:105], v219 offset:32768\n"
      "v_mfma_f32_16x16x32_bf16 v[76:79], a[24:27], a[176:179], v[76:79]\n"
      "ds_read_b64_tr_b16 a[106:107], v219 offset:33024\n"
      "v_and_b32 v221, 3, v255\n"
      "v_mfma_f32_16x16x32_bf16 v[76:79], a[28:31], a[180:183], v[76:79]\n"
      "s_waitcnt lgkmcnt(15)\n"
      "s_add_u32 m0, s84, 32768\n"
      "ds_read_b64_tr_b16 a[108:109], v219 offset:40960\n"
      "v_mfma_f32_16x16x32_bf16 v[80:83], a[32:35], a[176:179], v[80:83]\n"
      "buffer_load_dwordx4 v208, s[40:43], s73 offen lds\n"
      "v_mfma_f32_16x16x32_bf16 v[80:83], a[36:39], a[180:183], v[80:83]\n"
      "s_add_u32 s73, s73, 8192\n"
      "ds_read_b64_tr_b16 a[110:111], v219 offset:41216\n"
      "v_lshl_add_u32 v220, v221, 3, v220\n"
      "v_mfma_f32_16x16x32_bf16 v[84:87], a[40:43], a[176:179], v[84:87]\n"
      "ds_read_b64_tr_b16 a[112:113], v218 offset:33280\n"
      "v_xor_b32 v221, 32, v220\n"
      "v_mfma_f32_16x16x32_bf16 v[84:87], a[44:47], a[180:183], v[84:87]\n"
      "s_add_u32 m0, s84, 33792\n"
      "ds_read_b64_tr_b16 a[114:115], v218 offset:33536\n"
      "v_add_u32 v232, s63, v220\n"
      "v_mfma_f32_16x16x32_bf16 v[88:91], a[48:51], a[176:179], v[88:91]\n"
      "buffer_load_dwordx4 v209, s[40:43], s73 offen lds\n"
      "v_mfma_f32_16x16x32_bf16 v[88:91], a[52:55], a[180:183], v[88:91]\n"
      "s_add_u32 s73, s73, 8192\n"
      "ds_read_b64_tr_b16 a[116:117], v218 offset:41472\n"
      "v_add_u32 v234, s62, v220\n"
      "v_mfma_f32_16x16x32_bf16 v[92:95], a[56:59], a[176:179], v[92:95]\n"
      "s_and_b32 s73, s73, 0xffff\n"
      "ds_read_b64_tr_b16 a[118:119], v218 offset:41728\n"
      "v_add_u32 v233, s63, v221\n"
      "v_mfma_f32_16x16x32_bf16 v[92:95], a[60:63], a[180:183], v[92:95]\n"
      "ds_read_b64_tr_b16 a[120:121], v219 offset:33280\n"
      "v_add_u32 v235, s62, v221\n"
      "v_mfma_f32_16x16x32_bf16 v[192:195], a[64:67], a[248:251], v[192:195]\n"
      "s_waitcnt lgkmcnt(4)\n"
      "s_add_u32 m0, s84, 34816\n"
      "s_lshr_b32 s62, s22, 1\n"
      "ds_read_b64_tr_b16 a[122:123], v219 offset:33536\n"
      "v_mfma_f32_16x16x32_bf16 v[192:195], a[68:71], a[252:255], v[192:195]\n"
      "buffer_load_dwordx4 v208, s[40:43], s73 offen lds\n"
      "v_mfma_f32_16x16x32_bf16 v[196:199], a[72:75], a[248:251], v[196:199]\n"
      "s_add_u32 s73, s73, 8192\n"
      "ds_read_b64_tr_b16 a[124:125], v219 offset:41472\n"
      "v_and_b32 v220, 15, v255\n"
      "v_mfma_f32_16x16x32_bf16 v[196:199], a[76:79], a[252:255], v[196:199]\n"
      "ds_read_b64_tr_b16 a[126:127], v219 offset:41728\n"
      "v_lshlrev_b32 v220, 4, v220\n"
      "v_mfma_f32_16x16x32_bf16 v[200:203], a[80:83], a[248:251], v[200:203]\n"
      "s_add_u32 m0, s84, 35840\n"
      "v_lshrrev_b32 v221, 5, v255\n"
      "v_lshl_add_u32 v220, v221, 8, v220\n"
      "v_mfma_f32_16x16x32_bf16 v[200:203], a[84:87], a[252:255], v[200:203]\n"
      "buffer_load_dwordx4 v209, s[40:43], s73 offen lds\n"
      "v_mfma_f32_16x16x32_bf16 v[204:207], a[88:91], a[248:251], v[204:207]\n"
      "s_add_u32 s73, s73, 8192\n"
      "v_bfe_u32 v221, v255, 4, 1\n"
      "v_lshl_add_u32 v220, v221, 3, v220\n"
      "v_mfma_f32_16x16x32_bf16 v[204:207], a[92:95], a[252:255], v[204:207]\n"
      "s_and_b32 s73, s73, 0xffff\n"
      "s_add_u32 s65, s84, 32768\n"
      "s_lshl_b32 s63, s61, 11\n"
      "v_mfma_f32_16x16x32_bf16 v[160:163], a[64:67], a[216:219], v[160:163]\n"
      "s_xor_b32 s64, s63, 0x800\n"
      "s_lshl_b32 s66, s62, 12\n"
      "s_add_u32 s66, s66, 155648\n"
      "v_mfma_f32_16x16x32_bf16 v[160:163], a[68:71], a[220:223], v[160:163]\n"
      "s_add_u32 m0, s84, 36864\n"
      "v_lshlrev_b32 v221, 4, v255\n"
      "v_add_u32 v243, s65, v221\n"
      "v_mfma_f32_16x16x32_bf16 v[164:167], a[72:75], a[216:219], v[164:167]\n"
      "s_lshl_b32 s67, s61, 5\n"
      "buffer_load_dwordx4 v208, s[40:43], s73 offen lds\n"
      "v_mfma_f32_16x16x32_bf16 v[164:167], a[76:79], a[220:223], v[164:167]\n"
      "s_add_u32 s73, s73, 8192\n"
      "s_waitcnt vmcnt(10) lgkmcnt(0)\n"
      "v_xor_b32 v221, s67, v221\n"
      "v_mfma_f32_16x16x32_bf16 v[168:171], a[80:83], a[216:219], v[168:171]\n"
      "s_barrier\n"
      "ds_read_b128 a[128:131], v212 offset:0\n"
      "v_add_u32 v247, s66, v221\n"
      "v_mfma_f32_16x16x32_bf16 v[168:171], a[84:87], a[220:223], v[168:171]\n"
      "s_add_u32 m0, s84, 37888\n"
      "s_add_u32 s66, s66, s63\n"
      "ds_read_b128 a[132:135], v213 offset:0\n"
      "v_mfma_f32_16x16x32_bf16 v[172:175], a[88:91], a[216:219], v[172:175]\n"
      "buffer_load_dwordx4 v209, s[40:43], s73 offen lds\n"
      "v_mfma_f32_16x16x32_bf16 v[172:175], a[92:95], a[220:223], v[172:175]\n"
      "s_add_u32 s73, s73, 8192\n"
      "ds_read_b128 a[144:147], v212 offset:2048\n"
      "v_add_u32 v244, s66, v220\n"
      "v_mfma_f32_16x16x32_bf16 v[176:179], a[96:99], a[216:219], v[176:179]\n"
      "s_and_b32 s73, s73, 0xffff\n"
      "s_add_u32 s66, s65, s63\n"
      "ds_read_b128 a[148:151], v213 offset:2048\n"
      "v_mfma_f32_16x16x32_bf16 v[176:179], a[100:103], a[220:223], v[176:179]\n"
      "ds_read_b128 a[160:163], v212 offset:4096\n"
      "v_add_u32 v241, s66, v220\n"
      "v_mfma_f32_16x16x32_bf16 v[180:183], a[104:107], a[216:219], v[180:183]\n"
      "s_add_u32 m0, s84, 38912\n"
      "s_add_u32 s66, s65, s64\n"
      "ds_read_b128 a[164:167], v213 offset:4096\n"
      "v_mfma_f32_16x16x32_bf16 v[180:183], a[108:111], a[220:223], v[180:183]\n"
      "buffer_load_dwordx4 v208, s[40:43], s73 offen lds\n"
      "v_mfma_f32_16x16x32_bf16 v[184:187], a[112:115], a[216:219], v[184:187]\n"
      "s_add_u32 s73, s73, 8192\n"
      "ds_read_b128 a[176:179], v212 offset:6144\n"
      "v_add_u32 v242, s66, v220\n"
      "v_mfma_f32_16x16x32_bf16 v[184:187], a[116:119], a[220:223], v[184:187]\n"
      "ds_read_b128 a[180:183], v213 offset:6144\n"
      "v_mov_b32 v248, 0x3e0293ee\n"
      "v_mfma_f32_16x16x32_bf16 v[188:191], a[120:123], a[216:219], v[188:191]\n"
      "s_add_u32 m0, s84, 39936\n"
      "ds_read_b128 a[192:195], v214 offset:4096\n"
      "v_mov_b32 v249, 0x3e0293ee\n"
      "v_mfma_f32_16x16x32_bf16 v[188:191], a[124:127], a[220:223], v[188:191]\n"
      "buffer_load_dwordx4 v209, s[40:43], s73 offen lds\n"
      "v_mfma_f32_16x16x32_bf16 v[0:3], a[64:67], a[224:227], v[0:3]\n"
      "s_bfe_u32 s73, s87, 0x40018\n"
      "ds_read_b128 a[196:199], v215 offset:4096\n"
      "v_bfe_i32 v220, v255, 1, 1\n"
      "v_mfma_f32_16x16x32_bf16 v[0:3], a[68:71], a[228:231], v[0:3]\n"
      "s_lshl_b32 s73, s73, 7\n"
      "s_or_b32 s73, s73, s85\n"
      "ds_read_b64_tr_b16 a[0:1], v216 offset:49152\n"
      "v_mfma_f32_16x16x32_bf16 v[4:7], a[72:75], a[224:227], v[4:7]\n"
      "ds_read_b64_tr_b16 a[2:3], v216 offset:49408\n"
      "v_and_b32 v220, 0x3e803e80, v220\n"
      "v_mfma_f32_16x16x32_bf16 v[4:7], a[76:79], a[228:231], v[4:7]\n"
      "s_add_u32 m0, s23, 32768\n"
      "ds_read_b64_tr_b16 a[4:5], v216 offset:57344\n"
      "v_xor_b32 v221, 0x3e803e80, v220\n"
      "v_mfma_f32_16x16x32_bf16 v[8:11], a[80:83], a[224:227], v[8:11]\n"
      "buffer_load_dwordx4 v211, s[44:47], s75 offen lds\n"
      "v_mfma_f32_16x16x32_bf16 v[8:11], a[84:87], a[228:231], v[8:11]\n"
      "s_add_u32 s75, s75, 131072\n"
      "ds_read_b64_tr_b16 a[6:7], v216 offset:57600\n"
      "v_accvgpr_write_b32 a208, v221\n"
      "v_mfma_f32_16x16x32_bf16 v[12:15], a[88:91], a[224:227], v[12:15]\n"
      "ds_read_b64_tr_b16 a[8:9], v217 offset:49152\n"
      "v_accvgpr_write_b32 a212, v220\n"
      "v_mfma_f32_16x16x32_bf16 v[12:15], a[92:95], a[228:231], v[12:15]\n"
      "s_add_u32 m0, s23, 36864\n"
      "ds_read_b64_tr_b16 a[10:11], v217 offset:49408\n"
      "v_accvgpr_write_b32 a209, v221\n"
      "v_mfma_f32_16x16x32_bf16 v[16:19], a[96:99], a[224:227], v[16:19]\n"
      "buffer_load_dwordx4 v211, s[44:47], s75 offen lds\n"
      "v_mfma_f32_16x16x32_bf16 v[16:19], a[100:103], a[228:231], v[16:19]\n"
      "s_add_u32 s75, s75, 131072\n"
      "ds_read_b64_tr_b16 a[12:13], v217 offset:57344\n"
      "v_accvgpr_write_b32 a213, v220\n"
      "v_mfma_f32_16x16x32_bf16 v[20:23], a[104:107], a[224:227], v[20:23]\n"
      "ds_read_b64_tr_b16 a[14:15], v217 offset:57600\n"
      "v_accvgpr_write_b32 a210, v221\n"
      "v_mfma_f32_16x16x32_bf16 v[20:23], a[108:111], a[228:231], v[20:23]\n"
      "ds_read_b64_tr_b16 a[16:17], v216 offset:49664\n"
      "v_accvgpr_write_b32 a214, v220\n"
      "v_mfma_f32_16x16x32_bf16 v[24:27], a[112:115], a[224:227], v[24:27]\n"
      "s_add_u32 m0, s23, 40960\n"
      "ds_read_b64_tr_b16 a[18:19], v216 offset:49920\n"
      "v_accvgpr_write_b32 a211, v221\n"
      "v_mfma_f32_16x16x32_bf16 v[24:27], a[116:119], a[228:231], v[24:27]\n"
      "buffer_load_dwordx4 v211, s[44:47], s75 offen lds\n"
      "v_mfma_f32_16x16x32_bf16 v[28:31], a[120:123], a[224:227], v[28:31]\n"
      "s_add_u32 s75, s75, 131072\n"
      "ds_read_b64_tr_b16 a[20:21], v216 offset:57856\n"
      "v_accvgpr_write_b32 a215, v220\n"
      "v_mfma_f32_16x16x32_bf16 v[28:31], a[124:127], a[228:231], v[28:31]\n"
      "ds_read_b64_tr_b16 a[22:23], v216 offset:58112\n"
      "v_mov_b32 v238, 0\n"
      "v_mfma_f32_16x16x32_bf16 v[32:35], a[64:67], a[232:235], v[32:35]\n"
      "s_add_u32 m0, s23, 45056\n"
      "ds_read_b64_tr_b16 a[24:25], v217 offset:49664\n"
      "v_mov_b32 v239, 0\n"
      "v_mfma_f32_16x16x32_bf16 v[32:35], a[68:71], a[236:239], v[32:35]\n"
      "buffer_load_dwordx4 v211, s[44:47], s75 offen lds\n"
      "v_mfma_f32_16x16x32_bf16 v[36:39], a[72:75], a[232:235], v[36:39]\n"
      "s_bfe_u32 s75, s87, 0x4001c\n"
      "ds_read_b64_tr_b16 a[26:27], v217 offset:49920\n"
      "v_mov_b32 v240, 0\n"
      "v_mfma_f32_16x16x32_bf16 v[36:39], a[76:79], a[236:239], v[36:39]\n"
      "s_lshl_b32 s75, s75, 19\n"
      "ds_read_b64_tr_b16 a[28:29], v217 offset:57856\n"
      "v_mov_b32 v96, 0x0\n"
      "v_mfma_f32_16x16x32_bf16 v[40:43], a[80:83], a[232:235], v[40:43]\n"
      "ds_read_b64_tr_b16 a[30:31], v217 offset:58112\n"
      "v_mov_b32 v97, 0x0\n"
      "v_mfma_f32_16x16x32_bf16 v[40:43], a[84:87], a[236:239], v[40:43]\n"
      "s_add_u32 m0, s23, 147456\n"
      "ds_read_b64_tr_b16 a[32:33], v218 offset:49152\n"
      "v_mov_b32 v98, 0x0\n"
      "v_mfma_f32_16x16x32_bf16 v[44:47], a[88:91], a[232:235], v[44:47]\n"
      "buffer_load_dwordx4 v210, s[40:43], s74 offen lds\n"
      "v_mfma_f32_16x16x32_bf16 v[44:47], a[92:95], a[236:239], v[44:47]\n"
      "s_bfe_u32 s74, s87, 0x4001c\n"
      "ds_read_b64_tr_b16 a[34:35], v218 offset:49408\n"
      "v_mov_b32 v99, 0x0\n"
      "v_mfma_f32_16x16x32_bf16 v[48:51], a[96:99], a[232:235], v[48:51]\n"
      "s_lshl_b32 s74, s74, 7\n"
      "s_or_b32 s74, s74, s86\n"
      "ds_read_b64_tr_b16 a[36:37], v218 offset:57344\n"
      "v_mfma_f32_16x16x32_bf16 v[48:51], a[100:103], a[236:239], v[48:51]\n"
      "s_waitcnt lgkmcnt(15)\n"
      "s_add_u32 m0, s84, 0\n"
      "ds_read_b64_tr_b16 a[38:39], v218 offset:57600\n"
      "v_mfma_f32_16x16x32_bf16 v[52:55], a[104:107], a[232:235], v[52:55]\n"
      "buffer_load_dwordx4 v208, s[40:43], s73 offen lds\n"
      "v_mfma_f32_16x16x32_bf16 v[52:55], a[108:111], a[236:239], v[52:55]\n"
      "s_add_u32 s73, s73, 8192\n"
      "ds_read_b64_tr_b16 a[40:41], v219 offset:49152\n"
      "v_mov_b32 v100, 0x0\n"
      "v_mfma_f32_16x16x32_bf16 v[56:59], a[112:115], a[232:235], v[56:59]\n"
      "ds_read_b64_tr_b16 a[42:43], v219 offset:49408\n"
      "v_mov_b32 v101, 0x0\n"
      "v_mfma_f32_16x16x32_bf16 v[56:59], a[116:119], a[236:239], v[56:59]\n"
      "ds_read_b64_tr_b16 a[44:45], v219 offset:57344\n"
      "v_mov_b32 v102, 0x0\n"
      "v_mfma_f32_16x16x32_bf16 v[60:63], a[120:123], a[232:235], v[60:63]\n"
      "s_add_u32 m0, s84, 1024\n"
      "ds_read_b64_tr_b16 a[46:47], v219 offset:57600\n"
      "v_mov_b32 v103, 0x0\n"
      "v_mfma_f32_16x16x32_bf16 v[60:63], a[124:127], a[236:239], v[60:63]\n"
      "buffer_load_dwordx4 v209, s[40:43], s73 offen lds\n"
      "v_mfma_f32_16x16x32_bf16 v[64:67], a[64:67], a[240:243], v[64:67]\n"
      "s_add_u32 s73, s73, 8192\n"
      "ds_read_b64_tr_b16 a[48:49], v218 offset:49664\n"
      "v_mov_b32 v104, 0x0\n"
      "v_mfma_f32_16x16x32_bf16 v[64:67], a[68:71], a[244:247], v[64:67]\n"
      "s_and_b32 s73, s73, 0xffff\n"
      "ds_read_b64_tr_b16 a[50:51], v218 offset:49920\n"
      "v_mov_b32 v105, 0x0\n"
      "v_mfma_f32_16x16x32_bf16 v[68:71], a[72:75], a[240:243], v[68:71]\n"
      "s_add_u32 m0, s84, 2048\n"
      "ds_read_b64_tr_b16 a[52:53], v218 offset:57856\n"
      "v_mov_b32 v106, 0x0\n"
      "v_mfma_f32_16x16x32_bf16 v[68:71], a[76:79], a[244:247], v[68:71]\n"
      "buffer_load_dwordx4 v208, s[40:43], s73 offen lds\n"
      "v_mfma_f32_16x16x32_bf16 v[72:75], a[80:83], a[240:243], v[72:75]\n"
      "s_add_u32 s73, s73, 8192\n"
      "ds_read_b64_tr_b16 a[54:55], v218 offset:58112\n"
      "v_mov_b32 v107, 0x0\n"
      "v_mfma_f32_16x16x32_bf16 v[72:75], a[84:87], a[244:247], v[72:75]\n"
      "ds_read_b64_tr_b16 a[56:57], v219 offset:49664\n"
      "v_mov_b32 v108, 0x0\n"
      "v_mfma_f32_16x16x32_bf16 v[76:79], a[88:91], a[240:243], v[76:79]\n"
      "ds_read_b64_tr_b16 a[58:59], v219 offset:49920\n"
      "v_mov_b32 v109, 0x0\n"
      "v_mfma_f32_16x16x32_bf16 v[76:79], a[92:95], a[244:247], v[76:79]\n"
      "s_add_u32 m0, s84, 3072\n"
      "ds_read_b64_tr_b16 a[60:61], v219 offset:57856\n"
      "v_mov_b32 v110, 0x0\n"
      "v_mfma_f32_16x16x32_bf16 v[80:83], a[96:99], a[240:243], v[80:83]\n"
      "buffer_load_dwordx4 v209, s[40:43], s73 offen lds\n"
      "v_mfma_f32_16x16x32_bf16 v[80:83], a[100:103], a[244:247], v[80:83]\n"
      "s_add_u32 s73, s73, 8192\n"
      "ds_read_b64_tr_b16 a[62:63], v219 offset:58112\n"
      "v_mov_b32 v111, 0x0\n"
      "v_mfma_f32_16x16x32_bf16 v[84:87], a[104:107], a[240:243], v[84:87]\n"
      "s_and_b32 s73, s73, 0xffff\n"
      "v_mov_b32 v112, 0x0\n"
      "v_mov_b32 v113, 0x0\n"
      "v_mfma_f32_16x16x32_bf16 v[84:87], a[108:111], a[244:247], v[84:87]\n"
      "s_add_u32 m0, s84, 4096\n"
      "v_mov_b32 v114, 0x0\n"
      "v_mov_b32 v115, 0x0\n"
      "v_mfma_f32_16x16x32_bf16 v[88:91], a[112:115], a[240:243], v[88:91]\n"
      "buffer_load_dwordx4 v208, s[40:43], s73 offen lds\n"
      "v_mfma_f32_16x16x32_bf16 v[88:91], a[116:119], a[244:247], v[88:91]\n"
      "s_add_u32 s73, s73, 8192\n"
      "v_mov_b32 v116, 0x0\n"
      "v_mov_b32 v117, 0x0\n"
      "v_mfma_f32_16x16x32_bf16 v[92:95], a[120:123], a[240:243], v[92:95]\n"
      "v_mov_b32 v118, 0x0\n"
      "v_mov_b32 v119, 0x0\n"
      "v_mfma_f32_16x16x32_bf16 v[92:95], a[124:127], a[244:247], v[92:95]\n"
      "s_waitcnt lgkmcnt(0)\n"
      "v_mov_b32 v120, 0x0\n"
      "v_mov_b32 v121, 0x0\n"
      "v_mfma_f32_16x16x32_bf16 v[192:195], a[0:3], a[192:195], v[192:195]\n"
      "s_add_u32 m0, s84, 5120\n"
      "v_mov_b32 v122, 0x0\n"
      "v_mov_b32 v123, 0x0\n"
      "v_mfma_f32_16x16x32_bf16 v[192:195], a[4:7], a[196:199], v[192:195]\n"
      "buffer_load_dwordx4 v209, s[40:43], s73 offen lds\n"
      "v_mfma_f32_16x16x32_bf16 v[196:199], a[8:11], a[192:195], v[196:199]\n"
      "s_add_u32 s73, s73, 8192\n"
      "v_mov_b32 v124, 0x0\n"
      "v_mov_b32 v125, 0x0\n"
      "v_mfma_f32_16x16x32_bf16 v[196:199], a[12:15], a[196:199], v[196:199]\n"
      "s_and_b32 s73, s73, 0xffff\n"
      "v_mov_b32 v126, 0x0\n"
      "v_mov_b32 v127, 0x0\n"
      "v_mfma_f32_16x16x32_bf16 v[200:203], a[16:19], a[192:195], v[200:203]\n"
      "s_add_u32 m0, s84, 6144\n"
      "v_mov_b32 v128, 0x0\n"
      "v_mov_b32 v129, 0x0\n"
      "v_mfma_f32_16x16x32_bf16 v[200:203], a[20:23], a[196:199], v[200:203]\n"
      "buffer_load_dwordx4 v208, s[40:43], s73 offen lds\n"
      "v_mfma_f32_16x16x32_bf16 v[204:207], a[24:27], a[192:195], v[204:207]\n"
      "s_add_u32 s73, s73, 8192\n"
      "v_mov_b32 v130, 0x0\n"
      "v_mov_b32 v131, 0x0\n"
      "v_mfma_f32_16x16x32_bf16 v[204:207], a[28:31], a[196:199], v[204:207]\n"
      "v_mov_b32 v132, 0x0\n"
      "v_mov_b32 v133, 0x0\n"
      "v_mfma_f32_16x16x32_bf16 v[160:163], a[0:3], a[128:131], v[160:163]\n"
      "v_mov_b32 v134, 0x0\n"
      "v_mov_b32 v135, 0x0\n"
      "v_mfma_f32_16x16x32_bf16 v[160:163], a[4:7], a[132:135], v[160:163]\n"
      "s_add_u32 m0, s84, 7168\n"
      "v_mov_b32 v136, 0x0\n"
      "v_mov_b32 v137, 0x0\n"
      "v_mfma_f32_16x16x32_bf16 v[164:167], a[8:11], a[128:131], v[164:167]\n"
      "buffer_load_dwordx4 v209, s[40:43], s73 offen lds\n"
      "v_mfma_f32_16x16x32_bf16 v[164:167], a[12:15], a[132:135], v[164:167]\n"
      "s_bfe_u32 s73, s87, 0x4001c\n"
      "s_waitcnt vmcnt(13) lgkmcnt(0)\n"
      "v_mov_b32 v138, 0x0\n"
      "v_mfma_f32_16x16x32_bf16 v[168:171], a[16:19], a[128:131], v[168:171]\n"
      "s_lshl_b32 s73, s73, 7\n"
      "s_or_b32 s73, s73, s85\n"
      "s_barrier\n"
      "ds_read_b128 a[216:219], v212 offset:32768\n"
      "v_mfma_f32_16x16x32_bf16 v[168:171], a[20:23], a[132:135], v[168:171]\n"
      "s_add_u32 m0, s23, 49152\n"
      "ds_read_b128 a[220:223], v213 offset:32768\n"
      "v_mov_b32 v139, 0x0\n"
      "v_mfma_f32_16x16x32_bf16 v[172:175], a[24:27], a[128:131], v[172:175]\n"
      "buffer_load_dwordx4 v211, s[44:47], s75 offen lds\n"
      "v_mfma_f32_16x16x32_bf16 v[172:175], a[28:31], a[132:135], v[172:175]\n"
      "s_add_u32 s75, s75, 131072\n"
      "ds_read_b128 a[224:227], v212 offset:34816\n"
      "v_mov_b32 v140, 0x0\n"
      "v_mfma_f32_16x16x32_bf16 v[176:179], a[32:35], a[128:131], v[176:179]\n"
      "ds_read_b128 a[228:231], v213 offset:34816\n"
      "v_mov_b32 v141, 0x0\n"
      "v_mfma_f32_16x16x32_bf16 v[176:179], a[36:39], a[132:135], v[176:179]\n"
      "ds_read_b128 a[232:235], v212 offset:36864\n"
      "v_mov_b32 v142, 0x0\n"
      "v_mfma_f32_16x16x32_bf16 v[180:183], a[40:43], a[128:131], v[180:183]\n"
      "s_add_u32 m0, s23, 53248\n"
      "ds_read_b128 a[236:239], v213 offset:36864\n"
      "v_mov_b32 v143, 0x0\n"
      "v_mfma_f32_16x16x32_bf16 v[180:183], a[44:47], a[132:135], v[180:183]\n"
      "buffer_load_dwordx4 v211, s[44:47], s75 offen lds\n"
      "v_mfma_f32_16x16x32_bf16 v[184:187], a[48:51], a[128:131], v[184:187]\n"
      "s_add_u32 s75, s75, 131072\n"
      "ds_read_b128 a[240:243], v212 offset:38912\n"
      "v_mov_b32 v144, 0x0\n"
      "v_mfma_f32_16x16x32_bf16 v[184:187], a[52:55], a[132:135], v[184:187]\n"
      "ds_read_b128 a[244:247], v213 offset:38912\n"
      "v_mov_b32 v145, 0x0\n"
      "v_mfma_f32_16x16x32_bf16 v[188:191], a[56:59], a[128:131], v[188:191]\n"
      "s_add_u32 m0, s23, 57344\n"
      "ds_read_b128 a[248:251], v214 offset:8192\n"
      "v_mov_b32 v146, 0x0\n"
      "v_mfma_f32_16x16x32_bf16 v[188:191], a[60:63], a[132:135], v[188:191]\n"
      "buffer_load_dwordx4 v211, s[44:47], s75 offen lds\n"
      "v_mfma_f32_16x16x32_bf16 v[0:3], a[0:3], a[144:147], v[0:3]\n"
      "s_add_u32 s75, s75, 131072\n"
      "ds_read_b128 a[252:255], v215 offset:8192\n"
      "v_mov_b32 v147, 0x0\n"
      "v_mfma_f32_16x16x32_bf16 v[0:3], a[4:7], a[148:151], v[0:3]\n"
      "ds_read_b64_tr_b16 a[64:65], v216 offset:16384\n"
      "v_mov_b32 v148, 0x0\n"
      "v_mfma_f32_16x16x32_bf16 v[4:7], a[8:11], a[144:147], v[4:7]\n"
      "ds_read_b64_tr_b16 a[66:67], v216 offset:16640\n"
      "v_mov_b32 v149, 0x0\n"
      "v_mfma_f32_16x16x32_bf16 v[4:7], a[12:15], a[148:151], v[4:7]\n"
      "s_add_u32 m0, s23, 61440\n"
      "ds_read_b64_tr_b16 a[68:69], v216 offset:24576\n"
      "v_mov_b32 v150, 0x0\n"
      "v_mfma_f32_16x16x32_bf16 v[8:11], a[16:19], a[144:147], v[8:11]\n"
      "buffer_load_dwordx4 v211, s[44:47], s75 offen lds\n"
      "v_mfma_f32_16x16x32_bf16 v[8:11], a[20:23], a[148:151], v[8:11]\n"
      "ds_read_b64_tr_b16 a[70:71], v216 offset:24832\n"
      "v_mov_b32 v151, 0x0\n"
      "v_mfma_f32_16x16x32_bf16 v[12:15], a[24:27], a[144:147], v[12:15]\n"
      "ds_read_b64_tr_b16 a[72:73], v217 offset:16384\n"
      "v_mov_b32 v152, 0x0\n"
      "v_mfma_f32_16x16x32_bf16 v[12:15], a[28:31], a[148:151], v[12:15]\n"
      "s_add_u32 m0, s23, 151552\n"
      "ds_read_b64_tr_b16 a[74:75], v217 offset:16640\n"
      "v_mov_b32 v153, 0x0\n"
      "v_mfma_f32_16x16x32_bf16 v[16:19], a[32:35], a[144:147], v[16:19]\n"
      "buffer_load_dwordx4 v210, s[40:43], s74 offen lds\n"
      "v_mfma_f32_16x16x32_bf16 v[16:19], a[36:39], a[148:151], v[16:19]\n"
      "ds_read_b64_tr_b16 a[76:77], v217 offset:24576\n"
      "v_mov_b32 v154, 0x0\n"
      "v_mfma_f32_16x16x32_bf16 v[20:23], a[40:43], a[144:147], v[20:23]\n"
      "ds_read_b64_tr_b16 a[78:79], v217 offset:24832\n"
      "v_mov_b32 v155, 0x0\n"
      "v_mfma_f32_16x16x32_bf16 v[20:23], a[44:47], a[148:151], v[20:23]\n"
      "ds_read_b64_tr_b16 a[80:81], v216 offset:16896\n"
      "v_mov_b32 v156, 0x0\n"
      "v_mfma_f32_16x16x32_bf16 v[24:27], a[48:51], a[144:147], v[24:27]\n"
      "s_waitcnt lgkmcnt(11)\n"
      "s_add_u32 m0, s84, 32768\n"
      "ds_read_b64_tr_b16 a[82:83], v216 offset:17152\n"
      "v_mfma_f32_16x16x32_bf16 v[24:27], a[52:55], a[148:151], v[24:27]\n"
      "buffer_load_dwordx4 v208, s[40:43], s73 offen lds\n"
      "v_mfma_f32_16x16x32_bf16 v[28:31], a[56:59], a[144:147], v[28:31]\n"
      "s_add_u32 s73, s73, 8192\n"
      "ds_read_b64_tr_b16 a[84:85], v216 offset:25088\n"
      "v_mov_b32 v157, 0x0\n"
      "v_mfma_f32_16x16x32_bf16 v[28:31], a[60:63], a[148:151], v[28:31]\n"
      "ds_read_b64_tr_b16 a[86:87], v216 offset:25344\n"
      "v_mov_b32 v158, 0x0\n"
      "v_mfma_f32_16x16x32_bf16 v[32:35], a[0:3], a[160:163], v[32:35]\n"
      "s_add_u32 m0, s84, 33792\n"
      "ds_read_b64_tr_b16 a[88:89], v217 offset:16896\n"
      "v_mov_b32 v159, 0x0\n"
      "v_mfma_f32_16x16x32_bf16 v[32:35], a[4:7], a[164:167], v[32:35]\n"
      "buffer_load_dwordx4 v209, s[40:43], s73 offen lds\n"
      "v_mfma_f32_16x16x32_bf16 v[36:39], a[8:11], a[160:163], v[36:39]\n"
      "s_add_u32 s73, s73, 8192\n"
      "ds_read_b64_tr_b16 a[90:91], v217 offset:17152\n"
      "v_mfma_f32_16x16x32_bf16 v[36:39], a[12:15], a[164:167], v[36:39]\n"
      "s_and_b32 s73, s73, 0xffff\n"
      "ds_read_b64_tr_b16 a[92:93], v217 offset:25088\n"
      "v_mfma_f32_16x16x32_bf16 v[40:43], a[16:19], a[160:163], v[40:43]\n"
      "ds_read_b64_tr_b16 a[94:95], v217 offset:25344\n"
      "v_mfma_f32_16x16x32_bf16 v[40:43], a[20:23], a[164:167], v[40:43]\n"
      "s_add_u32 m0, s84, 34816\n"
      "ds_read_b64_tr_b16 a[96:97], v218 offset:16384\n"
      "v_mfma_f32_16x16x32_bf16 v[44:47], a[24:27], a[160:163], v[44:47]\n"
      "buffer_load_dwordx4 v208, s[40:43], s73 offen lds\n"
      "v_mfma_f32_16x16x32_bf16 v[44:47], a[28:31], a[164:167], v[44:47]\n"
      "s_add_u32 s73, s73, 8192\n"
      "ds_read_b64_tr_b16 a[98:99], v218 offset:16640\n"
      "v_mfma_f32_16x16x32_bf16 v[48:51], a[32:35], a[160:163], v[48:51]\n"
      "ds_read_b64_tr_b16 a[100:101], v218 offset:24576\n"
      "v_mfma_f32_16x16x32_bf16 v[48:51], a[36:39], a[164:167], v[48:51]\n"
      "s_add_u32 m0, s84, 35840\n"
      "ds_read_b64_tr_b16 a[102:103], v218 offset:24832\n"
      "v_mfma_f32_16x16x32_bf16 v[52:55], a[40:43], a[160:163], v[52:55]\n"
      "buffer_load_dwordx4 v209, s[40:43], s73 offen lds\n"
      "v_mfma_f32_16x16x32_bf16 v[52:55], a[44:47], a[164:167], v[52:55]\n"
      "s_add_u32 s73, s73, 8192\n"
      "ds_read_b64_tr_b16 a[104:105], v219 offset:16384\n"
      "v_mfma_f32_16x16x32_bf16 v[56:59], a[48:51], a[160:163], v[56:59]\n"
      "s_and_b32 s73, s73, 0xffff\n"
      "ds_read_b64_tr_b16 a[106:107], v219 offset:16640\n"
      "v_mfma_f32_16x16x32_bf16 v[56:59], a[52:55], a[164:167], v[56:59]\n"
      "ds_read_b64_tr_b16 a[108:109], v219 offset:24576\n"
      "v_mfma_f32_16x16x32_bf16 v[60:63], a[56:59], a[160:163], v[60:63]\n"
      "s_add_u32 m0, s84, 36864\n"
      "ds_read_b64_tr_b16 a[110:111], v219 offset:24832\n"
      "v_mfma_f32_16x16x32_bf16 v[60:63], a[60:63], a[164:167], v[60:63]\n"
      "buffer_load_dwordx4 v208, s[40:43], s73 offen lds\n"
      "v_mfma_f32_16x16x32_bf16 v[64:67], a[0:3], a[176:179], v[64:67]\n"
      "s_add_u32 s73, s73, 8192\n"
      "ds_read_b64_tr_b16 a[112:113], v218 offset:16896\n"
      "v_mfma_f32_16x16x32_bf16 v[64:67], a[4:7], a[180:183], v[64:67]\n"
      "ds_read_b64_tr_b16 a[114:115], v218 offset:17152\n"
      "v_mfma_f32_16x16x32_bf16 v[68:71], a[8:11], a[176:179], v[68:71]\n"
      "s_add_u32 m0, s84, 37888\n"
      "ds_read_b64_tr_b16 a[116:117], v218 offset:25088\n"
      "v_mfma_f32_16x16x32_bf16 v[68:71], a[12:15], a[180:183], v[68:71]\n"
      "buffer_load_dwordx4 v209, s[40:43], s73 offen lds\n"
      "v_mfma_f32_16x16x32_bf16 v[72:75], a[16:19], a[176:179], v[72:75]\n"
      "s_add_u32 s73, s73, 8192\n"
      "ds_read_b64_tr_b16 a[118:119], v218 offset:25344\n"
      "v_mfma_f32_16x16x32_bf16 v[72:75], a[20:23], a[180:183], v[72:75]\n"
      "s_and_b32 s73, s73, 0xffff\n"
      "ds_read_b64_tr_b16 a[120:121], v219 offset:16896\n"
      "v_mfma_f32_16x16x32_bf16 v[76:79], a[24:27], a[176:179], v[76:79]\n"
      "ds_read_b64_tr_b16 a[122:123], v219 offset:17152\n"
      "v_mfma_f32_16x16x32_bf16 v[76:79], a[28:31], a[180:183], v[76:79]\n"
      "s_add_u32 m0, s84, 38912\n"
      "ds_read_b64_tr_b16 a[124:125], v219 offset:25088\n"
      "v_mfma_f32_16x16x32_bf16 v[80:83], a[32:35], a[176:179], v[80:83]\n"
      "buffer_load_dwordx4 v208, s[40:43], s73 offen lds\n"
      "v_mfma_f32_16x16x32_bf16 v[80:83], a[36:39], a[180:183], v[80:83]\n"
      "s_add_u32 s73, s73, 8192\n"
      "ds_read_b64_tr_b16 a[126:127], v219 offset:25344\n"
      "v_mfma_f32_16x16x32_bf16 v[84:87], a[40:43], a[176:179], v[84:87]\n"
      "v_mfma_f32_16x16x32_bf16 v[84:87], a[44:47], a[180:183], v[84:87]\n"
      "s_add_u32 m0, s84, 39936\n"
      "v_mfma_f32_16x16x32_bf16 v[88:91], a[48:51], a[176:179], v[88:91]\n"
      "buffer_load_dwordx4 v209, s[40:43], s73 offen lds\n"
      "v_mfma_f32_16x16x32_bf16 v[88:91], a[52:55], a[180:183], v[88:91]\n"
      "v_mfma_f32_16x16x32_bf16 v[92:95], a[56:59], a[176:179], v[92:95]\n"
      "v_mfma_f32_16x16x32_bf16 v[92:95], a[60:63], a[180:183], v[92:95]\n"
      "s_waitcnt lgkmcnt(0)\n"
      "v_mfma_f32_16x16x32_bf16 v[192:195], a[64:67], a[248:251], v[192:195]\n"
      "s_add_u32 m0, s23, 0\n"
      "v_mfma_f32_16x16x32_bf16 v[192:195], a[68:71], a[252:255], v[192:195]\n"
      "global_load_lds_dwordx4 v236, s[24:25] offset:0 nt\n"
      "v_mfma_f32_16x16x32_bf16 v[196:199], a[72:75], a[248:251], v[196:199]\n"
      "v_mfma_f32_16x16x32_bf16 v[196:199], a[76:79], a[252:255], v[196:199]\n"
      "v_mfma_f32_16x16x32_bf16 v[200:203], a[80:83], a[248:251], v[200:203]\n"
      "v_mfma_f32_16x16x32_bf16 v[200:203], a[84:87], a[252:255], v[200:203]\n"
      "v_mfma_f32_16x16x32_bf16 v[204:207], a[88:91], a[248:251], v[204:207]\n"
      "v_mfma_f32_16x16x32_bf16 v[204:207], a[92:95], a[252:255], v[204:207]\n"
      "v_mfma_f32_16x16x32_bf16 v[160:163], a[64:67], a[216:219], v[160:163]\n"
      "v_mfma_f32_16x16x32_bf16 v[160:163], a[68:71], a[220:223], v[160:163]\n"
      "v_mfma_f32_16x16x32_bf16 v[164:167], a[72:75], a[216:219], v[164:167]\n"
      "v_mfma_f32_16x16x32_bf16 v[164:167], a[76:79], a[220:223], v[164:167]\n"
      "s_waitcnt vmcnt(14) lgkmcnt(0)\n"
      "v_mfma_f32_16x16x32_bf16 v[168:171], a[80:83], a[216:219], v[168:171]\n"
      "s_barrier\n"
      "ds_read_b128 a[128:131], v212 offset:0\n"
      "v_mfma_f32_16x16x32_bf16 v[168:171], a[84:87], a[220:223], v[168:171]\n"
      "s_add_u32 m0, s23, 3072\n"
      "ds_read_b128 a[132:135], v213 offset:0\n"
      "v_mfma_f32_16x16x32_bf16 v[172:175], a[88:91], a[216:219], v[172:175]\n"
      "global_load_lds_dwordx4 v236, s[24:25] offset:1024 nt\n"
      "v_mfma_f32_16x16x32_bf16 v[172:175], a[92:95], a[220:223], v[172:175]\n"
      "ds_read_b128 a[144:147], v212 offset:2048\n"
      "v_mfma_f32_16x16x32_bf16 v[176:179], a[96:99], a[216:219], v[176:179]\n"
      "ds_read_b128 a[148:151], v213 offset:2048\n"
      "v_mfma_f32_16x16x32_bf16 v[176:179], a[100:103], a[220:223], v[176:179]\n"
      "ds_read_b128 a[160:163], v212 offset:4096\n"
      "v_mfma_f32_16x16x32_bf16 v[180:183], a[104:107], a[216:219], v[180:183]\n"
      "ds_read_b128 a[164:167], v213 offset:4096\n"
      "v_mfma_f32_16x16x32_bf16 v[180:183], a[108:111], a[220:223], v[180:183]\n"
      "ds_read_b128 a[176:179], v212 offset:6144\n"
      "v_mfma_f32_16x16x32_bf16 v[184:187], a[112:115], a[216:219], v[184:187]\n"
      "ds_read_b128 a[180:183], v213 offset:6144\n"
      "v_mfma_f32_16x16x32_bf16 v[184:187], a[116:119], a[220:223], v[184:187]\n"
      "ds_read_b128 a[192:195], v214 offset:0\n"
      "v_mfma_f32_16x16x32_bf16 v[188:191], a[120:123], a[216:219], v[188:191]\n"
      "ds_read_b128 a[196:199], v215 offset:0\n"
      "v_mfma_f32_16x16x32_bf16 v[188:191], a[124:127], a[220:223], v[188:191]\n"
      "ds_read_b64_tr_b16 a[0:1], v216 offset:32768\n"
      "v_mfma_f32_16x16x32_bf16 v[0:3], a[64:67], a[224:227], v[0:3]\n"
      "ds_read_b64_tr_b16 a[2:3], v216 offset:33024\n"
      "v_mfma_f32_16x16x32_bf16 v[0:3], a[68:71], a[228:231], v[0:3]\n"
      "ds_read_b64_tr_b16 a[4:5], v216 offset:40960\n"
      "v_mfma_f32_16x16x32_bf16 v[4:7], a[72:75], a[224:227], v[4:7]\n"
      "s_add_u32 m0, s23, 6144\n"
      "ds_read_b64_tr_b16 a[6:7], v216 offset:41216\n"
      "v_mfma_f32_16x16x32_bf16 v[4:7], a[76:79], a[228:231], v[4:7]\n"
      "global_load_lds_dwordx4 v236, s[24:25] offset:2048 nt\n"
      "v_mfma_f32_16x16x32_bf16 v[8:11], a[80:83], a[224:227], v[8:11]\n"
      "ds_read_b64_tr_b16 a[8:9], v217 offset:32768\n"
      "v_mfma_f32_16x16x32_bf16 v[8:11], a[84:87], a[228:231], v[8:11]\n"
      "ds_read_b64_tr_b16 a[10:11], v217 offset:33024\n"
      "v_mfma_f32_16x16x32_bf16 v[12:15], a[88:91], a[224:227], v[12:15]\n"
      "ds_read_b64_tr_b16 a[12:13], v217 offset:40960\n"
      "v_mfma_f32_16x16x32_bf16 v[12:15], a[92:95], a[228:231], v[12:15]\n"
      "ds_read_b64_tr_b16 a[14:15], v217 offset:41216\n"
      "v_mfma_f32_16x16x32_bf16 v[16:19], a[96:99], a[224:227], v[16:19]\n"
      "ds_read_b64_tr_b16 a[16:17], v216 offset:33280\n"
      "v_mfma_f32_16x16x32_bf16 v[16:19], a[100:103], a[228:231], v[16:19]\n"
      "ds_read_b64_tr_b16 a[18:19], v216 offset:33536\n"
      "v_mfma_f32_16x16x32_bf16 v[20:23], a[104:107], a[224:227], v[20:23]\n"
      "ds_read_b64_tr_b16 a[20:21], v216 offset:41472\n"
      "v_mfma_f32_16x16x32_bf16 v[20:23], a[108:111], a[228:231], v[20:23]\n"
      "ds_read_b64_tr_b16 a[22:23], v216 offset:41728\n"
      "v_mfma_f32_16x16x32_bf16 v[24:27], a[112:115], a[224:227], v[24:27]\n"
      "ds_read_b64_tr_b16 a[24:25], v217 offset:33280\n"
      "v_mfma_f32_16x16x32_bf16 v[24:27], a[116:119], a[228:231], v[24:27]\n"
      "ds_read_b64_tr_b16 a[26:27], v217 offset:33536\n"
      "v_mfma_f32_16x16x32_bf16 v[28:31], a[120:123], a[224:227], v[28:31]\n"
      "ds_read_b64_tr_b16 a[28:29], v217 offset:41472\n"
      "v_mfma_f32_16x16x32_bf16 v[28:31], a[124:127], a[228:231], v[28:31]\n"
      "s_add_u32 m0, s23, 9216\n"
      "ds_read_b64_tr_b16 a[30:31], v217 offset:41728\n"
      "v_mfma_f32_16x16x32_bf16 v[32:35], a[64:67], a[232:235], v[32:35]\n"
      "global_load_lds_dwordx4 v236, s[24:25] offset:3072 nt\n"
      "v_mfma_f32_16x16x32_bf16 v[32:35], a[68:71], a[236:239], v[32:35]\n"
      "v_add_u32 v236, 0x1000, v236\n"
      "v_mfma_f32_16x16x32_bf16 v[36:39], a[72:75], a[232:235], v[36:39]\n"
      "s_xor_b32 s56, s56, 0x18000\n"
      "ds_read_b64_tr_b16 a[32:33], v218 offset:32768\n"
      "v_mfma_f32_16x16x32_bf16 v[36:39], a[76:79], a[236:239], v[36:39]\n"
      "ds_read_b64_tr_b16 a[34:35], v218 offset:33024\n"
      "v_mfma_f32_16x16x32_bf16 v[40:43], a[80:83], a[232:235], v[40:43]\n"
      "ds_read_b64_tr_b16 a[36:37], v218 offset:40960\n"
      "v_mfma_f32_16x16x32_bf16 v[40:43], a[84:87], a[236:239], v[40:43]\n"
      "ds_read_b64_tr_b16 a[38:39], v218 offset:41216\n"
      "v_mfma_f32_16x16x32_bf16 v[44:47], a[88:91], a[232:235], v[44:47]\n"
      "ds_read_b64_tr_b16 a[40:41], v219 offset:32768\n"
      "v_mfma_f32_16x16x32_bf16 v[44:47], a[92:95], a[236:239], v[44:47]\n"
      "ds_read_b64_tr_b16 a[42:43], v219 offset:33024\n"
      "v_mfma_f32_16x16x32_bf16 v[48:51], a[96:99], a[232:235], v[48:51]\n"
      "ds_read_b64_tr_b16 a[44:45], v219 offset:40960\n"
      "v_mfma_f32_16x16x32_bf16 v[48:51], a[100:103], a[236:239], v[48:51]\n"
      "ds_read_b64_tr_b16 a[46:47], v219 offset:41216\n"
      "v_mfma_f32_16x16x32_bf16 v[52:55], a[104:107], a[232:235], v[52:55]\n"
      "ds_read_b64_tr_b16 a[48:49], v218 offset:33280\n"
      "v_mfma_f32_16x16x32_bf16 v[52:55], a[108:111], a[236:239], v[52:55]\n"
      "ds_read_b64_tr_b16 a[50:51], v218 offset:33536\n"
      "v_mfma_f32_16x16x32_bf16 v[56:59], a[112:115], a[232:235], v[56:59]\n"
      "s_add_u32 m0, s23, 65536\n"
      "ds_read_b64_tr_b16 a[52:53], v218 offset:41472\n"
      "v_mfma_f32_16x16x32_bf16 v[56:59], a[116:119], a[236:239], v[56:59]\n"
      "global_load_lds_dwordx4 v237, s[28:29] offset:0 nt\n"
      "v_mfma_f32_16x16x32_bf16 v[60:63], a[120:123], a[232:235], v[60:63]\n"
      "ds_read_b64_tr_b16 a[54:55], v218 offset:41728\n"
      "v_mfma_f32_16x16x32_bf16 v[60:63], a[124:127], a[236:239], v[60:63]\n"
      "ds_read_b64_tr_b16 a[56:57], v219 offset:33280\n"
      "v_mfma_f32_16x16x32_bf16 v[64:67], a[64:67], a[240:243], v[64:67]\n"
      "ds_read_b64_tr_b16 a[58:59], v219 offset:33536\n"
      "v_mfma_f32_16x16x32_bf16 v[64:67], a[68:71], a[244:247], v[64:67]\n"
      "ds_read_b64_tr_b16 a[60:61], v219 offset:41472\n"
      "v_mfma_f32_16x16x32_bf16 v[68:71], a[72:75], a[240:243], v[68:71]\n"
      "ds_read_b64_tr_b16 a[62:63], v219 offset:41728\n"
      "v_mfma_f32_16x16x32_bf16 v[68:71], a[76:79], a[244:247], v[68:71]\n"
      "v_mfma_f32_16x16x32_bf16 v[72:75], a[80:83], a[240:243], v[72:75]\n"
      "v_mfma_f32_16x16x32_bf16 v[72:75], a[84:87], a[244:247], v[72:75]\n"
      "v_mfma_f32_16x16x32_bf16 v[76:79], a[88:91], a[240:243], v[76:79]\n"
      "v_mfma_f32_16x16x32_bf16 v[76:79], a[92:95], a[244:247], v[76:79]\n"
      "v_mfma_f32_16x16x32_bf16 v[80:83], a[96:99], a[240:243], v[80:83]\n"
      "v_mfma_f32_16x16x32_bf16 v[80:83], a[100:103], a[244:247], v[80:83]\n"
      "s_add_u32 m0, s23, 68608\n"
      "v_mfma_f32_16x16x32_bf16 v[84:87], a[104:107], a[240:243], v[84:87]\n"
      "global_load_lds_dwordx4 v237, s[28:29] offset:1024 nt\n"
      "v_mfma_f32_16x16x32_bf16 v[84:87], a[108:111], a[244:247], v[84:87]\n"
      "v_mfma_f32_16x16x32_bf16 v[88:91], a[112:115], a[240:243], v[88:91]\n"
      "v_mfma_f32_16x16x32_bf16 v[88:91], a[116:119], a[244:247], v[88:91]\n"
      "v_mfma_f32_16x16x32_bf16 v[92:95], a[120:123], a[240:243], v[92:95]\n"
      "v_mfma_f32_16x16x32_bf16 v[92:95], a[124:127], a[244:247], v[92:95]\n"
      "s_waitcnt lgkmcnt(0)\n"
      "v_mfma_f32_16x16x32_bf16 v[192:195], a[0:3], a[192:195], v[192:195]\n"
      "v_mfma_f32_16x16x32_bf16 v[192:195], a[4:7], a[196:199], v[192:195]\n"
      "s_waitcnt vmcnt(6) lgkmcnt(0)\n"
      "v_mfma_f32_16x16x32_bf16 v[196:199], a[8:11], a[192:195], v[196:199]\n"
      "s_barrier\n"
      "ds_read_b128 a[248:251], v214 offset:4096\n"
      "v_mfma_f32_16x16x32_bf16 v[196:199], a[12:15], a[196:199], v[196:199]\n"
      "ds_read_b128 a[252:255], v215 offset:4096\n"
      "v_mfma_f32_16x16x32_bf16 v[200:203], a[16:19], a[192:195], v[200:203]\n"
      "ds_read_b64_tr_b16 a[64:65], v216 offset:49152\n"
      "v_mfma_f32_16x16x32_bf16 v[200:203], a[20:23], a[196:199], v[200:203]\n"
      "ds_read_b64_tr_b16 a[66:67], v216 offset:49408\n"
      "v_mfma_f32_16x16x32_bf16 v[204:207], a[24:27], a[192:195], v[204:207]\n"
      "s_add_u32 m0, s23, 71680\n"
      "ds_read_b64_tr_b16 a[68:69], v216 offset:57344\n"
      "v_mfma_f32_16x16x32_bf16 v[204:207], a[28:31], a[196:199], v[204:207]\n"
      "global_load_lds_dwordx4 v237, s[28:29] offset:2048 nt\n"
      "v_mfma_f32_16x16x32_bf16 v[160:163], a[0:3], a[128:131], v[160:163]\n"
      "ds_read_b64_tr_b16 a[70:71], v216 offset:57600\n"
      "v_mfma_f32_16x16x32_bf16 v[160:163], a[4:7], a[132:135], v[160:163]\n"
      "ds_read_b64_tr_b16 a[72:73], v217 offset:49152\n"
      "v_mfma_f32_16x16x32_bf16 v[164:167], a[8:11], a[128:131], v[164:167]\n"
      "ds_read_b64_tr_b16 a[74:75], v217 offset:49408\n"
      "v_mfma_f32_16x16x32_bf16 v[164:167], a[12:15], a[132:135], v[164:167]\n"
      "ds_read_b64_tr_b16 a[76:77], v217 offset:57344\n"
      "v_mfma_f32_16x16x32_bf16 v[168:171], a[16:19], a[128:131], v[168:171]\n"
      "ds_read_b64_tr_b16 a[78:79], v217 offset:57600\n"
      "v_mfma_f32_16x16x32_bf16 v[168:171], a[20:23], a[132:135], v[168:171]\n"
      "ds_read_b128 a[216:219], v212 offset:32768\n"
      "v_mfma_f32_16x16x32_bf16 v[172:175], a[24:27], a[128:131], v[172:175]\n"
      "ds_read_b128 a[220:223], v213 offset:32768\n"
      "v_mfma_f32_16x16x32_bf16 v[172:175], a[28:31], a[132:135], v[172:175]\n"
      "ds_read_b64_tr_b16 a[80:81], v216 offset:49664\n"
      "v_mfma_f32_16x16x32_bf16 v[176:179], a[32:35], a[128:131], v[176:179]\n"
      "ds_read_b64_tr_b16 a[82:83], v216 offset:49920\n"
      "v_mfma_f32_16x16x32_bf16 v[176:179], a[36:39], a[132:135], v[176:179]\n"
      "ds_read_b64_tr_b16 a[84:85], v216 offset:57856\n"
      "v_mfma_f32_16x16x32_bf16 v[180:183], a[40:43], a[128:131], v[180:183]\n"
      "ds_read_b64_tr_b16 a[86:87], v216 offset:58112\n"
      "v_mfma_f32_16x16x32_bf16 v[180:183], a[44:47], a[132:135], v[180:183]\n"
      "s_add_u32 m0, s23, 74752\n"
      "ds_read_b64_tr_b16 a[88:89], v217 offset:49664\n"
      "v_mfma_f32_16x16x32_bf16 v[184:187], a[48:51], a[128:131], v[184:187]\n"
      "global_load_lds_dwordx4 v237, s[28:29] offset:3072 nt\n"
      "v_mfma_f32_16x16x32_bf16 v[184:187], a[52:55], a[132:135], v[184:187]\n"
      "v_add_u32 v237, 0x1000, v237\n"
      "v_mfma_f32_16x16x32_bf16 v[188:191], a[56:59], a[128:131], v[188:191]\n"
      "s_xor_b32 s57, s57, 0x18000\n"
      "ds_read_b64_tr_b16 a[90:91], v217 offset:49920\n"
      "v_mfma_f32_16x16x32_bf16 v[188:191], a[60:63], a[132:135], v[188:191]\n"
      "s_waitcnt lgkmcnt(3)\n"
      "ds_read_b64_tr_b16 a[92:93], v217 offset:57856\n"
      "v_mfma_f32_16x16x32_bf16 v[192:195], a[64:67], a[248:251], v[192:195]\n"
      "ds_read_b64_tr_b16 a[94:95], v217 offset:58112\n"
      "v_mfma_f32_16x16x32_bf16 v[192:195], a[68:71], a[252:255], v[192:195]\n"
      "ds_read_b64_tr_b16 a[96:97], v218 offset:49152\n"
      "v_mfma_f32_16x16x32_bf16 v[196:199], a[72:75], a[248:251], v[196:199]\n"
      "ds_read_b64_tr_b16 a[98:99], v218 offset:49408\n"
      "v_mfma_f32_16x16x32_bf16 v[196:199], a[76:79], a[252:255], v[196:199]\n"
      "ds_read_b64_tr_b16 a[100:101], v218 offset:57344\n"
      "v_mfma_f32_16x16x32_bf16 v[200:203], a[80:83], a[248:251], v[200:203]\n"
      "s_waitcnt lgkmcnt(5)\n"
      "ds_read_b64_tr_b16 a[102:103], v218 offset:57600\n"
      "v_mfma_f32_16x16x32_bf16 v[200:203], a[84:87], a[252:255], v[200:203]\n"
      "ds_read_b64_tr_b16 a[104:105], v219 offset:49152\n"
      "v_mfma_f32_16x16x32_bf16 v[204:207], a[88:91], a[248:251], v[204:207]\n"
      "s_waitcnt lgkmcnt(5)\n"
      "ds_read_b64_tr_b16 a[106:107], v219 offset:49408\n"
      "v_mul_f32 v192, v248, v192\n"
      "v_mfma_f32_16x16x32_bf16 v[204:207], a[92:95], a[252:255], v[204:207]\n"
      "ds_read_b64_tr_b16 a[108:109], v219 offset:57344\n"
      "v_mul_f32 v193, v248, v193\n"
      "v_mfma_f32_16x16x32_bf16 v[160:163], a[64:67], a[216:219], v[160:163]\n"
      "s_add_u32 m0, s23, 16384\n"
      "ds_read_b64_tr_b16 a[110:111], v219 offset:57600\n"
      "v_mul_f32 v194, v248, v194\n"
      "v_mfma_f32_16x16x32_bf16 v[160:163], a[68:71], a[220:223], v[160:163]\n"
      "global_load_lds_dwordx4 v236, s[24:25] offset:0 nt\n"
      "v_mul_f32 v195, v248, v195\n"
      "v_mfma_f32_16x16x32_bf16 v[164:167], a[72:75], a[216:219], v[164:167]\n"
      "ds_read_b64_tr_b16 a[112:113], v218 offset:49664\n"
      "v_cvt_pk_bf16_f32 v192, v192, v193\n"
      "v_mfma_f32_16x16x32_bf16 v[164:167], a[76:79], a[220:223], v[164:167]\n"
      "ds_read_b64_tr_b16 a[114:115], v218 offset:49920\n"
      "v_cvt_pk_bf16_f32 v193, v194, v195\n"
      "v_mfma_f32_16x16x32_bf16 v[168:171], a[80:83], a[216:219], v[168:171]\n"
      "ds_read_b64_tr_b16 a[116:117], v218 offset:57856\n"
      "v_mul_f32 v196, v248, v196\n"
      "v_mfma_f32_16x16x32_bf16 v[168:171], a[84:87], a[220:223], v[168:171]\n"
      "ds_read_b64_tr_b16 a[118:119], v218 offset:58112\n"
      "ds_write_b64 v244, v[192:193] offset:0\n"
      "v_mfma_f32_16x16x32_bf16 v[172:175], a[88:91], a[216:219], v[172:175]\n"
      "ds_read_b64_tr_b16 a[120:121], v219 offset:49664\n"
      "v_mul_f32 v197, v248, v197\n"
      "v_mfma_f32_16x16x32_bf16 v[172:175], a[92:95], a[220:223], v[172:175]\n"
      "s_waitcnt lgkmcnt(6)\n"
      "ds_read_b64_tr_b16 a[122:123], v219 offset:49920\n"
      "v_mul_f32 v198, v248, v198\n"
      "v_mfma_f32_16x16x32_bf16 v[176:179], a[96:99], a[216:219], v[176:179]\n"
      "ds_read_b64_tr_b16 a[124:125], v219 offset:57856\n"
      "v_mul_f32 v199, v248, v199\n"
      "v_mfma_f32_16x16x32_bf16 v[176:179], a[100:103], a[220:223], v[176:179]\n"
      "ds_read_b64_tr_b16 a[126:127], v219 offset:58112\n"
      "v_cvt_pk_bf16_f32 v196, v196, v197\n"
      "v_mfma_f32_16x16x32_bf16 v[180:183], a[104:107], a[216:219], v[180:183]\n"
      "ds_read_b128 a[224:227], v212 offset:34816\n"
      "v_cvt_pk_bf16_f32 v197, v198, v199\n"
      "v_mfma_f32_16x16x32_bf16 v[180:183], a[108:111], a[220:223], v[180:183]\n"
      "s_waitcnt lgkmcnt(5)\n"
      "ds_read_b128 a[228:231], v213 offset:34816\n"
      "v_mul_f32 v200, v248, v200\n"
      "v_mfma_f32_16x16x32_bf16 v[184:187], a[112:115], a[216:219], v[184:187]\n"
      "s_waitcnt lgkmcnt(4)\n"
      "ds_read_b128 a[232:235], v212 offset:36864\n"
      "ds_write_b64 v244, v[196:197] offset:512\n"
      "v_mfma_f32_16x16x32_bf16 v[184:187], a[116:119], a[220:223], v[184:187]\n"
      "s_waitcnt lgkmcnt(4)\n"
      "s_add_u32 m0, s23, 19456\n"
      "ds_read_b128 a[236:239], v213 offset:36864\n"
      "v_mul_f32 v201, v248, v201\n"
      "v_mfma_f32_16x16x32_bf16 v[188:191], a[120:123], a[216:219], v[188:191]\n"
      "global_load_lds_dwordx4 v236, s[24:25] offset:1024 nt\n"
      "v_mul_f32 v202, v248, v202\n"
      "v_mfma_f32_16x16x32_bf16 v[188:191], a[124:127], a[220:223], v[188:191]\n"
      "ds_read_b128 a[240:243], v212 offset:38912\n"
      "v_mul_f32 v203, v248, v203\n"
      "v_mfma_f32_16x16x32_bf16 v[0:3], a[0:3], a[144:147], v[0:3]\n"
      "ds_read_b128 a[244:247], v213 offset:38912\n"
      "v_cvt_pk_bf16_f32 v200, v200, v201\n"
      "v_mfma_f32_16x16x32_bf16 v[0:3], a[4:7], a[148:151], v[0:3]\n"
      "v_cvt_pk_bf16_f32 v201, v202, v203\n"
      "v_mul_f32 v204, v248, v204\n"
      "v_mfma_f32_16x16x32_bf16 v[4:7], a[8:11], a[144:147], v[4:7]\n"
      "ds_write_b64 v244, v[200:201] offset:1024\n"
      "v_mul_f32 v205, v248, v205\n"
      "v_mfma_f32_16x16x32_bf16 v[4:7], a[12:15], a[148:151], v[4:7]\n"
      "v_mul_f32 v206, v248, v206\n"
      "v_mul_f32 v207, v248, v207\n"
      "v_mfma_f32_16x16x32_bf16 v[8:11], a[16:19], a[144:147], v[8:11]\n"
      "v_cvt_pk_bf16_f32 v204, v204, v205\n"
      "v_cvt_pk_bf16_f32 v205, v206, v207\n"
      "v_mfma_f32_16x16x32_bf16 v[8:11], a[20:23], a[148:151], v[8:11]\n"
      "ds_write_b64 v244, v[204:205] offset:1536\n"
      "v_mul_f32 v160, v248, v160\n"
      "v_mfma_f32_16x16x32_bf16 v[12:15], a[24:27], a[144:147], v[12:15]\n"
      "v_mul_f32 v161, v248, v161\n"
      "v_mul_f32 v162, v248, v162\n"
      "v_mfma_f32_16x16x32_bf16 v[12:15], a[28:31], a[148:151], v[12:15]\n"
      "v_mul_f32 v163, v248, v163\n"
      "v_cvt_pk_bf16_f32 v160, v160, v161\n"
      "v_mfma_f32_16x16x32_bf16 v[16:19], a[32:35], a[144:147], v[16:19]\n"
      "v_cvt_pk_bf16_f32 v161, v162, v163\n"
      "v_mul_f32 v164, v248, v164\n"
      "v_mfma_f32_16x16x32_bf16 v[16:19], a[36:39], a[148:151], v[16:19]\n"
      "ds_write_b64 v241, v[160:161] offset:0\n"
      "v_mul_f32 v165, v248, v165\n"
      "v_mfma_f32_16x16x32_bf16 v[20:23], a[40:43], a[144:147], v[20:23]\n"
      "s_add_u32 m0, s23, 22528\n"
      "v_mul_f32 v166, v248, v166\n"
      "v_mul_f32 v167, v248, v167\n"
      "v_mfma_f32_16x16x32_bf16 v[20:23], a[44:47], a[148:151], v[20:23]\n"
      "global_load_lds_dwordx4 v236, s[24:25] offset:2048 nt\n"
      "v_cvt_pk_bf16_f32 v164, v164, v165\n"
      "v_mfma_f32_16x16x32_bf16 v[24:27], a[48:51], a[144:147], v[24:27]\n"
      "v_cvt_pk_bf16_f32 v165, v166, v167\n"
      "v_mul_f32 v168, v248, v168\n"
      "v_mfma_f32_16x16x32_bf16 v[24:27], a[52:55], a[148:151], v[24:27]\n"
      "ds_write_b64 v241, v[164:165] offset:512\n"
      "v_mul_f32 v169, v248, v169\n"
      "v_mfma_f32_16x16x32_bf16 v[28:31], a[56:59], a[144:147], v[28:31]\n"
      "v_mul_f32 v170, v248, v170\n"
      "v_mul_f32 v171, v248, v171\n"
      "v_mfma_f32_16x16x32_bf16 v[28:31], a[60:63], a[148:151], v[28:31]\n"
      "s_waitcnt lgkmcnt(1)\n"
      "v_cvt_pk_bf16_f32 v168, v168, v169\n"
      "v_cvt_pk_bf16_f32 v169, v170, v171\n"
      "v_mfma_f32_16x16x32_bf16 v[0:3], a[64:67], a[224:227], v[0:3]\n"
      "v_mul_f32 v172, v248, v172\n"
      "v_mfma_f32_16x16x32_bf16 v[0:3], a[68:71], a[228:231], v[0:3]\n"
      "ds_write_b64 v241, v[168:169] offset:1024\n"
      "v_mul_f32 v173, v248, v173\n"
      "v_mfma_f32_16x16x32_bf16 v[4:7], a[72:75], a[224:227], v[4:7]\n"
      "v_mul_f32 v174, v248, v174\n"
      "v_mul_f32 v175, v248, v175\n"
      "v_mfma_f32_16x16x32_bf16 v[4:7], a[76:79], a[228:231], v[4:7]\n"
      "v_cvt_pk_bf16_f32 v172, v172, v173\n"
      "v_cvt_pk_bf16_f32 v173, v174, v175\n"
      "v_mfma_f32_16x16x32_bf16 v[8:11], a[80:83], a[224:227], v[8:11]\n"
      "v_mul_f32 v176, v248, v176\n"
      "v_mfma_f32_16x16x32_bf16 v[8:11], a[84:87], a[228:231], v[8:11]\n"
      "s_waitcnt vmcnt(7) lgkmcnt(0)\n"
      "ds_write_b64 v241, v[172:173] offset:1536\n"
      "v_mul_f32 v177, v248, v177\n"
      "v_mfma_f32_16x16x32_bf16 v[12:15], a[88:91], a[224:227], v[12:15]\n"
      "s_barrier\n"
      "v_mul_f32 v178, v248, v178\n"
      "v_mul_f32 v179, v248, v179\n"
      "v_mfma_f32_16x16x32_bf16 v[12:15], a[92:95], a[228:231], v[12:15]\n"
      "s_add_u32 m0, s23, 25600\n"
      "ds_read_b128 a[192:195], v247 offset:0\n"
      "v_cvt_pk_bf16_f32 v176, v176, v177\n"
      "v_mfma_f32_16x16x32_bf16 v[16:19], a[96:99], a[224:227], v[16:19]\n"
      "global_load_lds_dwordx4 v236, s[24:25] offset:3072 nt\n"
      "v_cvt_pk_bf16_f32 v177, v178, v179\n"
      "v_mfma_f32_16x16x32_bf16 v[16:19], a[100:103], a[228:231], v[16:19]\n"
      "ds_read_b128 a[196:199], v247 offset:1024\n"
      "v_add_u32 v236, 0x1000, v236\n"
      "v_mfma_f32_16x16x32_bf16 v[20:23], a[104:107], a[224:227], v[20:23]\n"
      "v_mul_f32 v180, v248, v180\n"
      "v_mfma_f32_16x16x32_bf16 v[20:23], a[108:111], a[228:231], v[20:23]\n"
      "ds_read_b128 a[200:203], v247 offset:2048\n"
      "ds_write_b64 v242, v[176:177] offset:0\n"
      "v_mfma_f32_16x16x32_bf16 v[24:27], a[112:115], a[224:227], v[24:27]\n"
      "v_mul_f32 v181, v248, v181\n"
      "v_mul_f32 v182, v248, v182\n"
      "v_mfma_f32_16x16x32_bf16 v[24:27], a[116:119], a[228:231], v[24:27]\n"
      "ds_read_b128 a[204:207], v247 offset:3072\n"
      "v_mul_f32 v183, v248, v183\n"
      "v_mfma_f32_16x16x32_bf16 v[28:31], a[120:123], a[224:227], v[28:31]\n"
      "v_cvt_pk_bf16_f32 v180, v180, v181\n"
      "v_cvt_pk_bf16_f32 v181, v182, v183\n"
      "v_mfma_f32_16x16x32_bf16 v[28:31], a[124:127], a[228:231], v[28:31]\n"
      "v_mul_f32 v184, v248, v184\n"
      "v_mfma_f32_16x16x32_bf16 v[32:35], a[0:3], a[160:163], v[32:35]\n"
      "ds_write_b64 v242, v[180:181] offset:512\n"
      "v_mul_f32 v185, v248, v185\n"
      "v_mfma_f32_16x16x32_bf16 v[32:35], a[4:7], a[164:167], v[32:35]\n"
      "v_mul_f32 v186, v248, v186\n"
      "v_mul_f32 v187, v248, v187\n"
      "v_mfma_f32_16x16x32_bf16 v[36:39], a[8:11], a[160:163], v[36:39]\n"
      "v_cvt_pk_bf16_f32 v184, v184, v185\n"
      "v_cvt_pk_bf16_f32 v185, v186, v187\n"
      "v_mfma_f32_16x16x32_bf16 v[36:39], a[12:15], a[164:167], v[36:39]\n"
      "v_mul_f32 v188, v248, v188\n"
      "v_mfma_f32_16x16x32_bf16 v[40:43], a[16:19], a[160:163], v[40:43]\n"
      "s_add_u32 m0, s23, 81920\n"
      "ds_write_b64 v242, v[184:185] offset:1024\n"
      "v_mul_f32 v189, v248, v189\n"
      "v_mfma_f32_16x16x32_bf16 v[40:43], a[20:23], a[164:167], v[40:43]\n"
      "global_load_lds_dwordx4 v237, s[28:29] offset:0 nt\n"
      "v_mul_f32 v190, v248, v190\n"
      "v_mfma_f32_16x16x32_bf16 v[44:47], a[24:27], a[160:163], v[44:47]\n"
      "v_mul_f32 v191, v248, v191\n"
      "v_cvt_pk_bf16_f32 v188, v188, v189\n"
      "v_mfma_f32_16x16x32_bf16 v[44:47], a[28:31], a[164:167], v[44:47]\n"
      "v_cvt_pk_bf16_f32 v189, v190, v191\n"
      "v_mfma_f32_16x16x32_bf16 v[48:51], a[32:35], a[160:163], v[48:51]\n"
      "ds_write_b64 v242, v[188:189] offset:1536\n"
      "ds_read_b128 a[128:131], v243 offset:0\n"
      "v_mfma_f32_16x16x32_bf16 v[48:51], a[36:39], a[164:167], v[48:51]\n"
      "ds_read_b128 a[132:135], v243 offset:1024\n"
      "ds_read_b128 a[136:139], v243 offset:2048\n"
      "v_mfma_f32_16x16x32_bf16 v[52:55], a[40:43], a[160:163], v[52:55]\n"
      "ds_read_b128 a[140:143], v243 offset:3072\n"
      "v_mul_f32 v0, v248, v0\n"
      "v_mfma_f32_16x16x32_bf16 v[52:55], a[44:47], a[164:167], v[52:55]\n"
      "v_mul_f32 v1, v248, v1\n"
      "v_mul_f32 v2, v248, v2\n"
      "v_mfma_f32_16x16x32_bf16 v[56:59], a[48:51], a[160:163], v[56:59]\n"
      "v_mul_f32 v3, v248, v3\n"
      "v_cvt_pk_bf16_f32 v0, v0, v1\n"
      "v_mfma_f32_16x16x32_bf16 v[56:59], a[52:55], a[164:167], v[56:59]\n"
      "v_cvt_pk_bf16_f32 v1, v2, v3\n"
      "v_mul_f32 v4, v248, v4\n"
      "v_mfma_f32_16x16x32_bf16 v[60:63], a[56:59], a[160:163], v[60:63]\n"
      "ds_write_b64 v241, v[0:1] offset:0\n"
      "v_mul_f32 v5, v248, v5\n"
      "v_mfma_f32_16x16x32_bf16 v[60:63], a[60:63], a[164:167], v[60:63]\n"
      "v_mul_f32 v6, v248, v6\n"
      "v_mul_f32 v7, v248, v7\n"
      "v_mfma_f32_16x16x32_bf16 v[64:67], a[0:3], a[176:179], v[64:67]\n"
      "v_cvt_pk_bf16_f32 v4, v4, v5\n"
      "v_cvt_pk_bf16_f32 v5, v6, v7\n"
      "v_mfma_f32_16x16x32_bf16 v[64:67], a[4:7], a[180:183], v[64:67]\n"
      "s_add_u32 m0, s23, 84992\n"
      "v_mul_f32 v8, v248, v8\n"
      "v_mfma_f32_16x16x32_bf16 v[68:71], a[8:11], a[176:179], v[68:71]\n"
      "global_load_lds_dwordx4 v237, s[28:29] offset:1024 nt\n"
      "ds_write_b64 v241, v[4:5] offset:512\n"
      "v_mfma_f32_16x16x32_bf16 v[68:71], a[12:15], a[180:183], v[68:71]\n"
      "v_mul_f32 v9, v248, v9\n"
      "v_mul_f32 v10, v248, v10\n"
      "v_mfma_f32_16x16x32_bf16 v[72:75], a[16:19], a[176:179], v[72:75]\n"
      "ds_read_b128 a[0:3], v228 offset:0\n"
      "v_mul_f32 v11, v248, v11\n"
      "v_mfma_f32_16x16x32_bf16 v[72:75], a[20:23], a[180:183], v[72:75]\n"
      "v_cvt_pk_bf16_f32 v8, v8, v9\n"
      "v_cvt_pk_bf16_f32 v9, v10, v11\n"
      "v_mfma_f32_16x16x32_bf16 v[76:79], a[24:27], a[176:179], v[76:79]\n"
      "v_mul_f32 v12, v248, v12\n"
      "v_mfma_f32_16x16x32_bf16 v[76:79], a[28:31], a[180:183], v[76:79]\n"
      "ds_read_b128 a[4:7], v229 offset:256\n"
      "ds_write_b64 v241, v[8:9] offset:1024\n"
      "v_mfma_f32_16x16x32_bf16 v[80:83], a[32:35], a[176:179], v[80:83]\n"
      "v_mul_f32 v13, v248, v13\n"
      "v_mul_f32 v14, v248, v14\n"
      "v_mfma_f32_16x16x32_bf16 v[80:83], a[36:39], a[180:183], v[80:83]\n"
      "v_mul_f32 v15, v248, v15\n"
      "v_cvt_pk_bf16_f32 v12, v12, v13\n"
      "v_mfma_f32_16x16x32_bf16 v[84:87], a[40:43], a[176:179], v[84:87]\n"
      "ds_read_b128 a[8:11], v228 offset:512\n"
      "v_cvt_pk_bf16_f32 v13, v14, v15\n"
      "v_mfma_f32_16x16x32_bf16 v[84:87], a[44:47], a[180:183], v[84:87]\n"
      "v_mul_f32 v16, v248, v16\n"
      "v_mfma_f32_16x16x32_bf16 v[88:91], a[48:51], a[176:179], v[88:91]\n"
      "ds_write_b64 v241, v[12:13] offset:1536\n"
      "v_mul_f32 v17, v248, v17\n"
      "v_mfma_f32_16x16x32_bf16 v[88:91], a[52:55], a[180:183], v[88:91]\n"
      "ds_read_b128 a[12:15], v229 offset:768\n"
      "v_mul_f32 v18, v248, v18\n"
      "v_mfma_f32_16x16x32_bf16 v[92:95], a[56:59], a[176:179], v[92:95]\n"
      "s_add_u32 m0, s23, 88064\n"
      "v_mul_f32 v19, v248, v19\n"
      "v_cvt_pk_bf16_f32 v16, v16, v17\n"
      "v_mfma_f32_16x16x32_bf16 v[92:95], a[60:63], a[180:183], v[92:95]\n"
      "global_load_lds_dwordx4 v237, s[28:29] offset:2048 nt\n"
      "v_cvt_pk_bf16_f32 v17, v18, v19\n"
      "v_mfma_f32_16x16x32_bf16 v[32:35], a[64:67], a[232:235], v[32:35]\n"
      "ds_read_b128 a[28:31], v229 offset:4864\n"
      "v_mul_f32 v20, v248, v20\n"
      "v_mfma_f32_16x16x32_bf16 v[32:35], a[68:71], a[236:239], v[32:35]\n"
      "ds_write_b64 v242, v[16:17] offset:0\n"
      "v_mul_f32 v21, v248, v21\n"
      "v_mfma_f32_16x16x32_bf16 v[36:39], a[72:75], a[232:235], v[36:39]\n"
      "v_mul_f32 v22, v248, v22\n"
      "v_mul_f32 v23, v248, v23\n"
      "v_mfma_f32_16x16x32_bf16 v[36:39], a[76:79], a[236:239], v[36:39]\n"
      "ds_read_b128 a[24:27], v228 offset:4608\n"
      "v_cvt_pk_bf16_f32 v20, v20, v21\n"
      "v_mfma_f32_16x16x32_bf16 v[40:43], a[80:83], a[232:235], v[40:43]\n"
      "v_cvt_pk_bf16_f32 v21, v22, v23\n"
      "v_mul_f32 v24, v248, v24\n"
      "v_mfma_f32_16x16x32_bf16 v[40:43], a[84:87], a[236:239], v[40:43]\n"
      "ds_write_b64 v242, v[20:21] offset:512\n"
      "v_mul_f32 v25, v248, v25\n"
      "v_mfma_f32_16x16x32_bf16 v[44:47], a[88:91], a[232:235], v[44:47]\n"
      "ds_read_b128 a[20:23], v229 offset:4352\n"
      "v_mul_f32 v26, v248, v26\n"
      "v_mfma_f32_16x16x32_bf16 v[44:47], a[92:95], a[236:239], v[44:47]\n"
      "v_mul_f32 v27, v248, v27\n"
      "v_cvt_pk_bf16_f32 v24, v24, v25\n"
      "v_mfma_f32_16x16x32_bf16 v[48:51], a[96:99], a[232:235], v[48:51]\n"
      "v_cvt_pk_bf16_f32 v25, v26, v27\n"
      "v_mul_f32 v28, v248, v28\n"
      "v_mfma_f32_16x16x32_bf16 v[48:51], a[100:103], a[236:239], v[48:51]\n"
      "ds_read_b128 a[16:19], v228 offset:4096\n"
      "ds_write_b64 v242, v[24:25] offset:1024\n"
      "v_mfma_f32_16x16x32_bf16 v[52:55], a[104:107], a[232:235], v[52:55]\n"
      "v_mul_f32 v29, v248, v29\n"
      "v_mul_f32 v30, v248, v30\n"
      "v_mfma_f32_16x16x32_bf16 v[52:55], a[108:111], a[236:239], v[52:55]\n"
      "s_add_u32 m0, s23, 91136\n"
      "v_mul_f32 v31, v248, v31\n"
      "v_cvt_pk_bf16_f32 v28, v28, v29\n"
      "v_mfma_f32_16x16x32_bf16 v[56:59], a[112:115], a[232:235], v[56:59]\n"
      "global_load_lds_dwordx4 v237, s[28:29] offset:3072 nt\n"
      "ds_read_b128 a[32:35], v230 offset:0\n"
      "v_mfma_f32_16x16x32_bf16 v[56:59], a[116:119], a[236:239], v[56:59]\n"
      "v_add_u32 v237, 0x1000, v237\n"
      "v_cvt_pk_bf16_f32 v29, v30, v31\n"
      "v_mfma_f32_16x16x32_bf16 v[60:63], a[120:123], a[232:235], v[60:63]\n"
      "ds_read_b128 v[0:3], v245 offset:0\n"
      "ds_write_b64 v242, v[28:29] offset:1536\n"
      "v_mfma_f32_16x16x32_bf16 v[60:63], a[124:127], a[236:239], v[60:63]\n"
      "ds_read_b128 a[36:39], v231 offset:256\n"
      "ds_read_b128 a[144:147], v243 offset:0\n"
      "v_mfma_f32_16x16x32_bf16 v[64:67], a[64:67], a[240:243], v[64:67]\n"
      "ds_read_b128 a[148:151], v243 offset:1024\n"
      "ds_read_b128 a[152:155], v243 offset:2048\n"
      "v_mfma_f32_16x16x32_bf16 v[64:67], a[68:71], a[244:247], v[64:67]\n"
      "ds_read_b128 v[4:7], v246 offset:256\n"
      "ds_read_b128 a[156:159], v243 offset:3072\n"
      "v_mfma_f32_16x16x32_bf16 v[68:71], a[72:75], a[240:243], v[68:71]\n"
      "ds_read_b128 a[40:43], v230 offset:512\n"
      "v_mul_f32 v32, v248, v32\n"
      "v_mfma_f32_16x16x32_bf16 v[68:71], a[76:79], a[244:247], v[68:71]\n"
      "v_mul_f32 v33, v248, v33\n"
      "v_mul_f32 v34, v248, v34\n"
      "v_mfma_f32_16x16x32_bf16 v[72:75], a[80:83], a[240:243], v[72:75]\n"
      "ds_read_b128 v[8:11], v245 offset:512\n"
      "v_mul_f32 v35, v248, v35\n"
      "v_mfma_f32_16x16x32_bf16 v[72:75], a[84:87], a[244:247], v[72:75]\n"
      "ds_read_b128 a[44:47], v231 offset:768\n"
      "v_cvt_pk_bf16_f32 v32, v32, v33\n"
      "v_mfma_f32_16x16x32_bf16 v[76:79], a[88:91], a[240:243], v[76:79]\n"
      "v_cvt_pk_bf16_f32 v33, v34, v35\n"
      "v_mul_f32 v36, v248, v36\n"
      "v_mfma_f32_16x16x32_bf16 v[76:79], a[92:95], a[244:247], v[76:79]\n"
      "ds_read_b128 v[12:15], v246 offset:768\n"
      "ds_write_b64 v241, v[32:33] offset:0\n"
      "v_mfma_f32_16x16x32_bf16 v[80:83], a[96:99], a[240:243], v[80:83]\n"
      "ds_read_b128 a[60:63], v231 offset:4864\n"
      "v_mul_f32 v37, v248, v37\n"
      "v_mfma_f32_16x16x32_bf16 v[80:83], a[100:103], a[244:247], v[80:83]\n"
      "v_mul_f32 v38, v248, v38\n"
      "v_mul_f32 v39, v248, v39\n"
      "v_mfma_f32_16x16x32_bf16 v[84:87], a[104:107], a[240:243], v[84:87]\n"
      "v_cvt_pk_bf16_f32 v36, v36, v37\n"
      "v_cvt_pk_bf16_f32 v37, v38, v39\n"
      "v_mfma_f32_16x16x32_bf16 v[84:87], a[108:111], a[244:247], v[84:87]\n"
      "ds_read_b128 a[56:59], v230 offset:4608\n"
      "v_mul_f32 v40, v248, v40\n"
      "v_mfma_f32_16x16x32_bf16 v[88:91], a[112:115], a[240:243], v[88:91]\n"
      "ds_write_b64 v241, v[36:37] offset:512\n"
      "v_mul_f32 v41, v248, v41\n"
      "v_mfma_f32_16x16x32_bf16 v[88:91], a[116:119], a[244:247], v[88:91]\n"
      "v_mul_f32 v42, v248, v42\n"
      "v_mul_f32 v43, v248, v43\n"
      "v_mfma_f32_16x16x32_bf16 v[92:95], a[120:123], a[240:243], v[92:95]\n"
      "ds_read_b128 a[52:55], v231 offset:4352\n"
      "v_cvt_pk_bf16_f32 v40, v40, v41\n"
      "v_mfma_f32_16x16x32_bf16 v[92:95], a[124:127], a[244:247], v[92:95]\n"
      "s_waitcnt lgkmcnt(3)\n"
      "v_cvt_pk_bf16_f32 v41, v42, v43\n"
      "v_mul_f32 v44, v248, v44\n"
      "v_mfma_f32_16x16x32_bf16 v[208:211], v[0:3], a[128:131], 0\n"
      "ds_write_b64 v241, v[40:41] offset:1024\n"
      "v_mul_f32 v45, v248, v45\n"
      "v_mfma_f32_16x16x32_bf16 v[208:211], v[4:7], a[132:135], v[208:211]\n"
      "ds_read_b128 a[48:51], v230 offset:4096\n"
      "v_mul_f32 v46, v248, v46\n"
      "v_mfma_f32_16x16x32_bf16 v[208:211], v[8:11], a[136:139], v[208:211]\n"
      "v_mul_f32 v47, v248, v47\n"
      "v_cvt_pk_bf16_f32 v44, v44, v45\n"
      "v_mfma_f32_16x16x32_bf16 v[208:211], v[12:15], a[140:143], v[208:211]\n"
      "v_cvt_pk_bf16_f32 v45, v46, v47\n"
      "v_mfma_f32_16x16x32_bf16 v[212:215], v[0:3], a[144:147], 0\n"
      "ds_write_b64 v241, v[44:45] offset:1536\n"
      "v_mfma_f32_16x16x32_bf16 v[212:215], v[4:7], a[148:151], v[212:215]\n"
      "v_mfma_f32_16x16x32_bf16 v[212:215], v[8:11], a[152:155], v[212:215]\n"
      "v_mfma_f32_16x16x32_bf16 v[212:215], v[12:15], a[156:159], v[212:215]\n"
      "s_nop 1\n"
      "v_max3_f32 v208, v208, v209, v210\n"
      "v_max_f32 v208, v208, v211\n"
      "v_mov_b32 v209, v208\n"
      "s_nop 1\n"
      "v_permlane32_swap_b32 v208, v209\n"
      "v_mfma_f32_16x16x32_bf16 v[224:227], v[0:3], a[192:195], 0\n"
      "v_max_f32 v208, v208, v209\n"
      "v_mov_b32 v209, v208\n"
      "s_nop 1\n"
      "v_permlane16_swap_b32 v208, v209\n"
      "v_mfma_f32_16x16x32_bf16 v[224:227], v[4:7], a[196:199], v[224:227]\n"
      "v_max_f32 v208, v208, v209\n"
      "v_sub_f32 v211, 0xc2880000, v208\n"
      "v_sub_f32 v210, 0xc2880000, v208\n"
      "v_sub_f32 v209, 0xc2880000, v208\n"
      "v_mfma_f32_16x16x32_bf16 v[224:227], v[8:11], a[200:203], v[224:227]\n"
      "v_sub_f32 v208, 0xc2880000, v208\n"
      "v_mfma_f32_16x16x32_bf16 v[224:227], v[12:15], a[204:207], v[224:227]\n"
      "s_waitcnt lgkmcnt(0)\n"
      "s_mov_b32 s58, 0\n"
      "v_mfma_f32_16x16x32_bf16 v[160:163], a[0:3], a[128:131], v[208:211]\n"
      "v_mfma_f32_16x16x32_bf16 v[160:163], a[4:7], a[132:135], v[160:163]\n"
      "v_max3_f32 v212, v212, v213, v214\n"
      "v_max_f32 v212, v212, v215\n"
      "v_mfma_f32_16x16x32_bf16 v[160:163], a[8:11], a[136:139], v[160:163]\n"
      "s_add_u32 m0, s23, 32768\n"
      "v_mov_b32 v213, v212\n"
      "v_mfma_f32_16x16x32_bf16 v[160:163], a[12:15], a[140:143], v[160:163]\n"
      "global_load_lds_dwordx4 v236, s[24:25] offset:0 nt\n"
      "v_mfma_f32_16x16x32_bf16 v[164:167], a[28:31], a[140:143], v[208:211]\n"
      "v_permlane32_swap_b32 v212, v213\n"
      "v_mfma_f32_16x16x32_bf16 v[164:167], a[24:27], a[136:139], v[164:167]\n"
      "v_max_f32 v212, v212, v213\n"
      "v_mov_b32 v213, v212\n"
      "v_mfma_f32_16x16x32_bf16 v[164:167], a[20:23], a[132:135], v[164:167]\n"
      "s_nop 0\n"
      "v_permlane16_swap_b32 v212, v213\n"
      "v_mfma_f32_16x16x32_bf16 v[164:167], a[16:19], a[128:131], v[164:167]\n"
      "v_max_f32 v212, v212, v213\n"
      "v_sub_f32 v215, 0xc2880000, v212\n"
      "v_mfma_f32_16x16x32_bf16 v[168:171], a[32:35], a[128:131], v[208:211]\n"
      "v_sub_f32 v214, 0xc2880000, v212\n"
      "v_sub_f32 v213, 0xc2880000, v212\n"
      "v_mfma_f32_16x16x32_bf16 v[168:171], a[36:39], a[132:135], v[168:171]\n"
      "v_sub_f32 v212, 0xc2880000, v212\n"
      "v_mul_f32 v48, v248, v48\n"
      "v_mfma_f32_16x16x32_bf16 v[168:171], a[40:43], a[136:139], v[168:171]\n"
      "s_waitcnt vmcnt(9)\n"
      "v_mul_f32 v49, v248, v49\n"
      "v_mul_f32 v50, v248, v50\n"
      "v_mfma_f32_16x16x32_bf16 v[168:171], a[44:47], a[140:143], v[168:171]\n"
      "s_barrier\n"
      "v_mul_f32 v51, v248, v51\n"
      "v_cvt_pk_bf16_f32 v48, v48, v49\n"
      "v_mfma_f32_16x16x32_bf16 v[172:175], a[60:63], a[140:143], v[208:211]\n"
      "s_add_u32 m0, s23, 98304\n"
      "v_cvt_pk_bf16_f32 v49, v50, v51\n"
      "v_mfma_f32_16x16x32_bf16 v[172:175], a[56:59], a[136:139], v[172:175]\n"
      "global_load_lds_dwordx4 v237, s[28:29] offset:0 nt\n"
      "ds_write_b64 v242, v[48:49] offset:0\n"
      "v_mfma_f32_16x16x32_bf16 v[172:175], a[52:55], a[132:135], v[172:175]\n"
      "v_mfma_f32_16x16x32_bf16 v[172:175], a[48:51], a[128:131], v[172:175]\n"
      "v_pk_mul_f32 v[52:53], v[52:53], v[248:249]\n"
      "v_pk_mul_f32 v[54:55], v[54:55], v[248:249]\n"
      "v_cvt_pk_bf16_f32 v52, v52, v53\n"
      "v_cvt_pk_bf16_f32 v53, v54, v55\n"
      "ds_write_b64 v242, v[52:53] offset:512\n"
      "v_pk_mul_f32 v[56:57], v[56:57], v[248:249]\n"
      "v_pk_mul_f32 v[58:59], v[58:59], v[248:249]\n"
      "v_cvt_pk_bf16_f32 v56, v56, v57\n"
      "v_cvt_pk_bf16_f32 v57, v58, v59\n"
      "ds_write_b64 v242, v[56:57] offset:1024\n"
      "v_pk_mul_f32 v[60:61], v[60:61], v[248:249]\n"
      "v_pk_mul_f32 v[62:63], v[62:63], v[248:249]\n"
      "v_cvt_pk_bf16_f32 v60, v60, v61\n"
      "v_cvt_pk_bf16_f32 v61, v62, v63\n"
      "ds_write_b64 v242, v[60:61] offset:1536\n"
      "ds_read_b128 a[160:163], v243 offset:0\n"
      "ds_read_b128 a[164:167], v243 offset:1024\n"
      "ds_read_b128 a[168:171], v243 offset:2048\n"
      "ds_read_b128 a[172:175], v243 offset:3072\n"
      "v_pk_mul_f32 v[64:65], v[64:65], v[248:249]\n"
      "v_pk_mul_f32 v[66:67], v[66:67], v[248:249]\n"
      "v_cvt_pk_bf16_f32 v64, v64, v65\n"
      "v_cvt_pk_bf16_f32 v65, v66, v67\n"
      "ds_write_b64 v241, v[64:65] offset:0\n"
      "v_pk_mul_f32 v[68:69], v[68:69], v[248:249]\n"
      "v_pk_mul_f32 v[70:71], v[70:71], v[248:249]\n"
      "v_cvt_pk_bf16_f32 v68, v68, v69\n"
      "v_cvt_pk_bf16_f32 v69, v70, v71\n"
      "ds_read_b64_tr_b16 a[64:65], v232 offset:0\n"
      "ds_write_b64 v241, v[68:69] offset:512\n"
      "v_pk_mul_f32 v[72:73], v[72:73], v[248:249]\n"
      "v_pk_mul_f32 v[74:75], v[74:75], v[248:249]\n"
      "v_cvt_pk_bf16_f32 v72, v72, v73\n"
      "v_cvt_pk_bf16_f32 v73, v74, v75\n"
      "ds_write_b64 v241, v[72:73] offset:1024\n"
      "v_pk_mul_f32 v[76:77], v[76:77], v[248:249]\n"
      "v_pk_mul_f32 v[78:79], v[78:79], v[248:249]\n"
      "v_cvt_pk_bf16_f32 v76, v76, v77\n"
      "v_cvt_pk_bf16_f32 v77, v78, v79\n"
      "s_add_u32 m0, s23, 35840\n"
      "ds_write_b64 v241, v[76:77] offset:1536\n"
      "v_pk_mul_f32 v[80:81], v[80:81], v[248:249]\n"
      "v_pk_mul_f32 v[82:83], v[82:83], v[248:249]\n"
      "v_cvt_pk_bf16_f32 v80, v80, v81\n"
      "v_cvt_pk_bf16_f32 v81, v82, v83\n"
      "global_load_lds_dwordx4 v236, s[24:25] offset:1024 nt\n"
      "ds_read_b64_tr_b16 a[66:67], v232 offset:4096\n"
      "ds_read_b64_tr_b16 a[68:69], v233 offset:0\n"
      "ds_write_b64 v242, v[80:81] offset:0\n"
      "v_pk_mul_f32 v[84:85], v[84:85], v[248:249]\n"
      "v_pk_mul_f32 v[86:87], v[86:87], v[248:249]\n"
      "v_cvt_pk_bf16_f32 v84, v84, v85\n"
      "v_cvt_pk_bf16_f32 v85, v86, v87\n"
      "ds_write_b64 v242, v[84:85] offset:512\n"
      "v_exp_f32 v160, v160\n"
      "v_pk_mul_f32 v[88:89], v[88:89], v[248:249]\n"
      "v_pk_mul_f32 v[90:91], v[90:91], v[248:249]\n"
      "v_cvt_pk_bf16_f32 v88, v88, v89\n"
      "v_cvt_pk_bf16_f32 v89, v90, v91\n"
      "ds_read_b64_tr_b16 a[70:71], v233 offset:4096\n"
      "ds_read_b64_tr_b16 a[72:73], v232 offset:256\n"
      "ds_write_b64 v242, v[88:89] offset:1024\n"
      "v_pk_mul_f32 v[92:93], v[92:93], v[248:249]\n"
      "v_pk_mul_f32 v[94:95], v[94:95], v[248:249]\n"
      "v_cvt_pk_bf16_f32 v92, v92, v93\n"
      "v_cvt_pk_bf16_f32 v93, v94, v95\n"
      "ds_write_b64 v242, v[92:93] offset:1536\n"
      "ds_read_b128 a[176:179], v243 offset:0\n"
      "ds_read_b128 a[180:183], v243 offset:1024\n"
      "ds_read_b128 a[184:187], v243 offset:2048\n"
      "ds_read_b128 a[188:191], v243 offset:3072\n"
      "v_exp_f32 v161, v161\n"
      "s_waitcnt lgkmcnt(13)\n"
      "ds_read_b64_tr_b16 a[74:75], v232 offset:4352\n"
      "v_mfma_f32_16x16x32_bf16 v[216:219], v[0:3], a[160:163], 0\n"
      "v_exp_f32 v162, v162\n"
      "v_mfma_f32_16x16x32_bf16 v[216:219], v[4:7], a[164:167], v[216:219]\n"
      "ds_read_b64_tr_b16 a[76:77], v233 offset:256\n"
      "v_mfma_f32_16x16x32_bf16 v[216:219], v[8:11], a[168:171], v[216:219]\n"
      "v_exp_f32 v163, v163\n"
      "v_mfma_f32_16x16x32_bf16 v[216:219], v[12:15], a[172:175], v[216:219]\n"
      "v_cvt_pk_bf16_f32 v192, v160, v161\n"
      "v_mfma_f32_16x16x32_bf16 v[176:179], a[0:3], a[144:147], v[212:215]\n"
      "v_exp_f32 v164, v164\n"
      "v_bfe_i32 v247, v255, 0, 1\n"
      "v_mfma_f32_16x16x32_bf16 v[176:179], a[4:7], a[148:151], v[176:179]\n"
      "ds_read_b64_tr_b16 a[78:79], v233 offset:4352\n"
      "v_cvt_pk_bf16_f32 v193, v162, v163\n"
      "v_and_b32 v247, 0xaaaaaaaa, v247\n"
      "v_mfma_f32_16x16x32_bf16 v[176:179], a[8:11], a[152:155], v[176:179]\n"
      "v_exp_f32 v165, v165\n"
      "v_or_b32 v247, 0x44444444, v247\n"
      "v_mfma_f32_16x16x32_bf16 v[176:179], a[12:15], a[156:159], v[176:179]\n"
      "v_exp_f32 v166, v166\n"
      "v_accvgpr_write_b32 a216, 0\n"
      "v_mfma_f32_16x16x32_bf16 v[180:183], a[28:31], a[156:159], v[212:215]\n"
      "s_add_u32 m0, s23, 101376\n"
      "v_cvt_pk_bf16_f32 v194, v164, v165\n"
      "v_accvgpr_write_b32 a217, 0\n"
      "v_mfma_f32_16x16x32_bf16 v[180:183], a[24:27], a[152:155], v[180:183]\n"
      "global_load_lds_dwordx4 v237, s[28:29] offset:1024 nt\n"
      "v_accvgpr_write_b32 a218, 0\n"
      "v_mfma_f32_16x16x32_bf16 v[180:183], a[20:23], a[148:151], v[180:183]\n"
      "v_exp_f32 v167, v167\n"
      "v_accvgpr_write_b32 a219, 0\n"
      "v_mfma_f32_16x16x32_bf16 v[180:183], a[16:19], a[144:147], v[180:183]\n"
      "ds_read_b64_tr_b16 a[80:81], v232 offset:512\n"
      "v_cvt_pk_bf16_f32 v195, v166, v167\n"
      "v_accvgpr_write_b32 a220, 0\n"
      "v_mfma_f32_16x16x32_bf16 v[184:187], a[32:35], a[144:147], v[212:215]\n"
      "v_exp_f32 v168, v168\n"
      "v_accvgpr_write_b32 a221, 0\n"
      "v_mfma_f32_16x16x32_bf16 v[184:187], a[36:39], a[148:151], v[184:187]\n"
      "ds_read_b64_tr_b16 a[82:83], v232 offset:4608\n"
      "ds_read_b64_tr_b16 a[84:85], v233 offset:512\n"
      "v_accvgpr_write_b32 a222, 0\n"
      "v_mfma_f32_16x16x32_bf16 v[184:187], a[40:43], a[152:155], v[184:187]\n"
      "s_add_u32 m0, s23, 38912\n"
      "v_exp_f32 v169, v169\n"
      "v_accvgpr_write_b32 a223, 0\n"
      "v_mfma_f32_16x16x32_bf16 v[184:187], a[44:47], a[156:159], v[184:187]\n"
      "global_load_lds_dwordx4 v236, s[24:25] offset:2048 nt\n"
      "ds_read_b64_tr_b16 a[86:87], v233 offset:4608\n"
      "v_cvt_pk_bf16_f32 v196, v168, v169\n"
      "v_mfma_f32_16x16x32_bf16 v[188:191], a[60:63], a[156:159], v[212:215]\n"
      "v_exp_f32 v170, v170\n"
      "v_mfma_f32_16x16x32_bf16 v[188:191], a[56:59], a[152:155], v[188:191]\n"
      "ds_read_b64_tr_b16 a[88:89], v232 offset:768\n"
      "ds_read_b64_tr_b16 a[90:91], v232 offset:4864\n"
      "v_mfma_f32_16x16x32_bf16 v[188:191], a[52:55], a[148:151], v[188:191]\n"
      "v_exp_f32 v171, v171\n"
      "v_mfma_f32_16x16x32_bf16 v[188:191], a[48:51], a[144:147], v[188:191]\n"
      "s_waitcnt lgkmcnt(3)\n"
      "ds_read_b64_tr_b16 a[92:93], v233 offset:768\n"
      "v_cvt_pk_bf16_f32 v197, v170, v171\n"
      "v_mfma_f32_16x16x32_bf16 v[220:223], v[0:3], a[176:179], 0\n"
      "v_exp_f32 v172, v172\n"
      "v_mfma_f32_16x16x32_bf16 v[220:223], v[4:7], a[180:183], v[220:223]\n"
      "ds_read_b64_tr_b16 a[96:97], v234 offset:0\n"
      "ds_read_b64_tr_b16 a[98:99], v234 offset:4096\n"
      "v_mfma_f32_16x16x32_bf16 v[220:223], v[8:11], a[184:187], v[220:223]\n"
      "v_exp_f32 v173, v173\n"
      "v_mfma_f32_16x16x32_bf16 v[220:223], v[12:15], a[188:191], v[220:223]\n"
      "ds_read_b64_tr_b16 a[100:101], v235 offset:0\n"
      "v_cvt_pk_bf16_f32 v198, v172, v173\n"
      "v_exp_f32 v174, v174\n"
      "v_max3_f32 v216, v216, v217, v218\n"
      "v_max_f32 v216, v216, v219\n"
      "v_mov_b32 v217, v216\n"
      "s_nop 1\n"
      "v_permlane32_swap_b32 v216, v217\n"
      "v_max_f32 v216, v216, v217\n"
      "v_mov_b32 v217, v216\n"
      "s_nop 1\n"
      "v_permlane16_swap_b32 v216, v217\n"
      "v_max_f32 v216, v216, v217\n"
      "v_sub_f32 v219, 0xc2880000, v216\n"
      "v_sub_f32 v218, 0xc2880000, v216\n"
      "v_sub_f32 v217, 0xc2880000, v216\n"
      "v_sub_f32 v216, 0xc2880000, v216\n"
      "ds_read_b64_tr_b16 a[102:103], v235 offset:4096\n"
      "ds_read_b64_tr_b16 a[94:95], v233 offset:4864\n"
      "v_exp_f32 v175, v175\n"
      "s_add_u32 m0, s23, 104448\n"
      "v_cvt_pk_bf16_f32 v199, v174, v175\n"
      "v_mfma_f32_16x16x32_bf16 v[160:163], a[0:3], a[160:163], v[216:219]\n"
      "global_load_lds_dwordx4 v237, s[28:29] offset:2048 nt\n"
      "v_mfma_f32_16x16x32_bf16 v[160:163], a[4:7], a[164:167], v[160:163]\n"
      "v_exp_f32 v176, v176\n"
      "v_mfma_f32_16x16x32_bf16 v[160:163], a[8:11], a[168:171], v[160:163]\n"
      "ds_read_b64_tr_b16 a[104:105], v234 offset:256\n"
      "ds_read_b64_tr_b16 a[106:107], v234 offset:4352\n"
      "v_mfma_f32_16x16x32_bf16 v[160:163], a[12:15], a[172:175], v[160:163]\n"
      "s_lshl_b32 s20, s61, 13\n"
      "v_exp_f32 v177, v177\n"
      "v_mfma_f32_16x16x32_bf16 v[164:167], a[28:31], a[172:175], v[216:219]\n"
      "s_add_u32 s20, s20, 65536\n"
      "ds_read_b64_tr_b16 a[108:109], v235 offset:256\n"
      "ds_read_b64_tr_b16 a[110:111], v235 offset:4352\n"
      "v_mfma_f32_16x16x32_bf16 v[164:167], a[24:27], a[168:171], v[164:167]\n"
      "s_lshl_b32 s21, s22, 9\n"
      "v_exp_f32 v178, v178\n"
      "v_mfma_f32_16x16x32_bf16 v[164:167], a[20:23], a[164:167], v[164:167]\n"
      "s_add_u32 m0, s23, 41984\n"
      "ds_read_b64_tr_b16 a[112:113], v234 offset:512\n"
      "v_cvt_pk_bf16_f32 v200, v176, v177\n"
      "v_mfma_f32_16x16x32_bf16 v[164:167], a[16:19], a[160:163], v[164:167]\n"
      "s_sub_u32 s20, s20, s21\n"
      "global_load_lds_dwordx4 v236, s[24:25] offset:3072 nt\n"
      "v_exp_f32 v179, v179\n"
      "v_mfma_f32_16x16x32_bf16 v[168:171], a[32:35], a[160:163], v[216:219]\n"
      "ds_read_b64_tr_b16 a[114:115], v234 offset:4608\n"
      "v_cvt_pk_bf16_f32 v201, v178, v179\n"
      "v_add_u32 v236, 0x1000, v236\n"
      "v_mfma_f32_16x16x32_bf16 v[168:171], a[36:39], a[164:167], v[168:171]\n"
      "v_exp_f32 v180, v180\n"
      "v_mfma_f32_16x16x32_bf16 v[168:171], a[40:43], a[168:171], v[168:171]\n"
      "ds_read_b64_tr_b16 a[116:117], v235 offset:512\n"
      "v_max3_f32 v220, v220, v221, v222\n"
      "v_mfma_f32_16x16x32_bf16 v[168:171], a[44:47], a[172:175], v[168:171]\n"
      "v_exp_f32 v181, v181\n"
      "v_mfma_f32_16x16x32_bf16 v[172:175], a[60:63], a[172:175], v[216:219]\n"
      "ds_read_b64_tr_b16 a[118:119], v235 offset:4608\n"
      "v_cvt_pk_bf16_f32 v202, v180, v181\n"
      "v_mfma_f32_16x16x32_bf16 v[172:175], a[56:59], a[168:171], v[172:175]\n"
      "v_exp_f32 v182, v182\n"
      "v_bfe_u32 v249, v255, 4, 1\n"
      "v_mfma_f32_16x16x32_bf16 v[172:175], a[52:55], a[164:167], v[172:175]\n"
      "ds_read_b64_tr_b16 a[120:121], v234 offset:768\n"
      "ds_read_b64_tr_b16 a[122:123], v234 offset:4864\n"
      "v_mul_u32_u24 v249, 0x300, v249\n"
      "v_mfma_f32_16x16x32_bf16 v[172:175], a[48:51], a[160:163], v[172:175]\n"
      "v_exp_f32 v183, v183\n"
      "v_subrev_u32 v241, s20, v234\n"
      "v_mfma_f32_16x16x32_bf16 v[0:3], v[192:195], a[64:67], 0\n"
      "s_waitcnt lgkmcnt(3)\n"
      "v_exp_f32 v184, v184\n"
      "v_mfma_f32_16x16x32_bf16 v[0:3], v[196:199], a[96:99], v[0:3]\n"
      "s_add_u32 m0, s23, 107520\n"
      "ds_read_b64_tr_b16 a[124:125], v235 offset:768\n"
      "v_max_f32 v220, v220, v223\n"
      "v_mfma_f32_16x16x32_bf16 v[4:7], v[192:195], a[68:71], 0\n"
      "global_load_lds_dwordx4 v237, s[28:29] offset:3072 nt\n"
      "v_mfma_f32_16x16x32_bf16 v[4:7], v[196:199], a[100:103], v[4:7]\n"
      "v_exp_f32 v185, v185\n"
      "v_mfma_f32_16x16x32_bf16 v[8:11], v[192:195], a[72:75], 0\n"
      "ds_read_b64_tr_b16 a[126:127], v235 offset:4864\n"
      "v_cvt_pk_bf16_f32 v203, v182, v183\n"
      "v_mfma_f32_16x16x32_bf16 v[8:11], v[196:199], a[104:107], v[8:11]\n"
      "v_exp_f32 v186, v186\n"
      "v_mfma_f32_16x16x32_bf16 v[12:15], v[192:195], a[76:79], 0\n"
      "v_cvt_pk_bf16_f32 v204, v184, v185\n"
      "v_add_u32 v237, 0x1000, v237\n"
      "v_mov_b32 v221, v220\n"
      "v_mfma_f32_16x16x32_bf16 v[12:15], v[196:199], a[108:111], v[12:15]\n"
      "v_exp_f32 v187, v187\n"
      "v_permlane32_swap_b32 v220, v221\n"
      "v_mfma_f32_16x16x32_bf16 v[16:19], v[192:195], a[80:83], 0\n"
      "v_cvt_pk_bf16_f32 v205, v186, v187\n"
      "v_max_f32 v220, v220, v221\n"
      "v_mfma_f32_16x16x32_bf16 v[16:19], v[196:199], a[112:115], v[16:19]\n"
      "v_exp_f32 v188, v188\n"
      "v_mov_b32 v221, v220\n"
      "v_mfma_f32_16x16x32_bf16 v[20:23], v[192:195], a[84:87], 0\n"
      "s_waitcnt lgkmcnt(0)\n"
      "buffer_load_dword v254, v253, s[36:39], 0 offen sc1\n"
      "v_permlane16_swap_b32 v220, v221\n"
      "v_mfma_f32_16x16x32_bf16 v[20:23], v[196:199], a[116:119], v[20:23]\n"
      "v_exp_f32 v189, v189\n"
      "v_max_f32 v220, v220, v221\n"
      "v_mfma_f32_16x16x32_bf16 v[24:27], v[192:195], a[88:91], 0\n"
      "v_cvt_pk_bf16_f32 v206, v188, v189\n"
      "v_sub_f32 v223, 0xc2880000, v220\n"
      "v_mfma_f32_16x16x32_bf16 v[24:27], v[196:199], a[120:123], v[24:27]\n"
      "v_exp_f32 v190, v190\n"
      "v_sub_f32 v222, 0xc2880000, v220\n"
      "v_mfma_f32_16x16x32_bf16 v[28:31], v[192:195], a[92:95], 0\n"
      "v_sub_f32 v221, 0xc2880000, v220\n"
      "v_sub_f32 v220, 0xc2880000, v220\n"
      "v_mfma_f32_16x16x32_bf16 v[28:31], v[196:199], a[124:127], v[28:31]\n"
      "v_exp_f32 v191, v191\n"
      "v_smfmac_f32_16x16x64_bf16 a[216:219], a[208:211], v[192:199], v247\n"
      "s_memrealtime s[82:83]\n"
      "s_waitcnt vmcnt(9) lgkmcnt(0)\n"
      "v_cvt_pk_bf16_f32 v207, v190, v191\n"
      "v_mfma_f32_16x16x32_bf16 v[176:179], a[0:3], a[176:179], v[220:223]\n"
      "s_barrier\n"
      "v_max3_f32 v224, v224, v225, v226\n"
      "v_max_f32 v224, v224, v227\n"
      "v_mfma_f32_16x16x32_bf16 v[176:179], a[4:7], a[180:183], v[176:179]\n"
      "v_exp_f32 v160, v160\n"
      "v_mfma_f32_16x16x32_bf16 v[176:179], a[8:11], a[184:187], v[176:179]\n"
      "ds_read_b128 a[0:3], v228 offset:16384\n"
      "v_mov_b32 v225, v224\n"
      "v_mfma_f32_16x16x32_bf16 v[176:179], a[12:15], a[188:191], v[176:179]\n"
      "v_exp_f32 v161, v161\n"
      "v_mfma_f32_16x16x32_bf16 v[180:183], a[28:31], a[188:191], v[220:223]\n"
      "v_exp_f32 v162, v162\n"
      "v_mfma_f32_16x16x32_bf16 v[180:183], a[24:27], a[184:187], v[180:183]\n"
      "s_add_u32 m0, s23, 49152\n"
      "v_cvt_pk_bf16_f32 v192, v160, v161\n"
      "v_permlane32_swap_b32 v224, v225\n"
      "v_mfma_f32_16x16x32_bf16 v[180:183], a[20:23], a[180:183], v[180:183]\n"
      "global_load_lds_dwordx4 v236, s[24:25] offset:0 nt\n"
      "v_mfma_f32_16x16x32_bf16 v[180:183], a[16:19], a[176:179], v[180:183]\n"
      "v_exp_f32 v163, v163\n"
      "v_mfma_f32_16x16x32_bf16 v[184:187], a[32:35], a[176:179], v[220:223]\n"
      "ds_read_b128 a[4:7], v229 offset:16640\n"
      "v_cvt_pk_bf16_f32 v193, v162, v163\n"
      "v_mfma_f32_16x16x32_bf16 v[184:187], a[36:39], a[180:183], v[184:187]\n"
      "v_exp_f32 v164, v164\n"
      "v_mfma_f32_16x16x32_bf16 v[184:187], a[40:43], a[184:187], v[184:187]\n"
      "s_add_u32 s34, s23, 131072\n"
      "s_mov_b32 s78, 0\n"
      "ds_read_b128 a[8:11], v228 offset:16896\n"
      "v_max_f32 v224, v224, v225\n"
      "v_mfma_f32_16x16x32_bf16 v[184:187], a[44:47], a[188:191], v[184:187]\n"
      "s_mov_b32 s35, 0x10000\n"
      "v_exp_f32 v165, v165\n"
      "v_mfma_f32_16x16x32_bf16 v[188:191], a[60:63], a[188:191], v[220:223]\n"
      "s_mov_b32 s81, 140\n"
      "v_cvt_pk_bf16_f32 v194, v164, v165\n"
      "v_mov_b32 v225, v224\n"
      "v_mfma_f32_16x16x32_bf16 v[188:191], a[56:59], a[184:187], v[188:191]\n"
      "v_exp_f32 v166, v166\n"
      "v_mfma_f32_16x16x32_bf16 v[188:191], a[52:55], a[180:183], v[188:191]\n"
      "s_mov_b32 s83, s82\n"
      "ds_read_b128 a[12:15], v229 offset:17152\n"
      "v_permlane16_swap_b32 v224, v225\n"
      "v_mfma_f32_16x16x32_bf16 v[188:191], a[48:51], a[176:179], v[188:191]\n"
      "v_exp_f32 v167, v167\n"
      "v_mfma_f32_16x16x32_bf16 v[32:35], v[200:203], a[64:67], 0\n"
      "v_exp_f32 v168, v168\n"
      "v_mfma_f32_16x16x32_bf16 v[32:35], v[204:207], a[96:99], v[32:35]\n"
      "s_add_u32 m0, s23, 52224\n"
      "v_cvt_pk_bf16_f32 v195, v166, v167\n"
      "v_max_f32 v224, v224, v225\n"
      "v_mfma_f32_16x16x32_bf16 v[36:39], v[200:203], a[68:71], 0\n"
      "global_load_lds_dwordx4 v236, s[24:25] offset:1024 nt\n"
      "v_mfma_f32_16x16x32_bf16 v[36:39], v[204:207], a[100:103], v[36:39]\n"
      "v_exp_f32 v169, v169\n"
      "v_mfma_f32_16x16x32_bf16 v[40:43], v[200:203], a[72:75], 0\n"
      "ds_read_b128 a[28:31], v229 offset:21248\n"
      "v_cvt_pk_bf16_f32 v196, v168, v169\n"
      "v_mfma_f32_16x16x32_bf16 v[40:43], v[204:207], a[104:107], v[40:43]\n"
      "v_exp_f32 v170, v170\n"
      "v_mfma_f32_16x16x32_bf16 v[44:47], v[200:203], a[76:79], 0\n"
      "ds_read_b128 a[24:27], v228 offset:20992\n"
      "v_sub_f32 v227, 0xc2880000, v224\n"
      "v_mfma_f32_16x16x32_bf16 v[44:47], v[204:207], a[108:111], v[44:47]\n"
      "v_exp_f32 v171, v171\n"
      "v_mfma_f32_16x16x32_bf16 v[48:51], v[200:203], a[80:83], 0\n"
      "v_cvt_pk_bf16_f32 v197, v170, v171\n"
      "v_sub_f32 v226, 0xc2880000, v224\n"
      "v_mfma_f32_16x16x32_bf16 v[48:51], v[204:207], a[112:115], v[48:51]\n"
      "v_exp_f32 v172, v172\n"
      "v_mfma_f32_16x16x32_bf16 v[52:55], v[200:203], a[84:87], 0\n"
      "ds_read_b128 a[20:23], v229 offset:20736\n"
      "v_sub_f32 v225, 0xc2880000, v224\n"
      "v_mfma_f32_16x16x32_bf16 v[52:55], v[204:207], a[116:119], v[52:55]\n"
      "v_exp_f32 v173, v173\n"
      "v_mfma_f32_16x16x32_bf16 v[56:59], v[200:203], a[88:91], 0\n"
      "v_exp_f32 v174, v174\n"
      "v_mfma_f32_16x16x32_bf16 v[56:59], v[204:207], a[120:123], v[56:59]\n"
      "s_add_u32 m0, s23, 55296\n"
      "v_cvt_pk_bf16_f32 v198, v172, v173\n"
      "v_sub_f32 v224, 0xc2880000, v224\n"
      "v_mfma_f32_16x16x32_bf16 v[60:63], v[200:203], a[92:95], 0\n"
      "global_load_lds_dwordx4 v236, s[24:25] offset:2048 nt\n"
      "v_mfma_f32_16x16x32_bf16 v[60:63], v[204:207], a[124:127], v[60:63]\n"
      "v_exp_f32 v175, v175\n"
      "v_smfmac_f32_16x16x64_bf16 a[216:219], a[212:215], v[200:207], v247\n"
      "v_cvt_pk_bf16_f32 v199, v174, v175\n"
      "v_mfma_f32_16x16x32_bf16 v[160:163], a[32:35], a[192:195], v[224:227]\n"
      "v_exp_f32 v176, v176\n"
      "v_mfma_f32_16x16x32_bf16 v[160:163], a[36:39], a[196:199], v[160:163]\n"
      "ds_read_b128 a[16:19], v228 offset:20480\n"
      "ds_read_b128 a[32:35], v230 offset:16384\n"
      "v_mfma_f32_16x16x32_bf16 v[160:163], a[40:43], a[200:203], v[160:163]\n"
      "v_exp_f32 v177, v177\n"
      "v_mfma_f32_16x16x32_bf16 v[160:163], a[44:47], a[204:207], v[160:163]\n"
      "ds_read_b128 a[36:39], v231 offset:16640\n"
      "v_cvt_pk_bf16_f32 v200, v176, v177\n"
      "v_mfma_f32_16x16x32_bf16 v[164:167], a[60:63], a[204:207], v[224:227]\n"
      "v_exp_f32 v178, v178\n"
      "v_mfma_f32_16x16x32_bf16 v[164:167], a[56:59], a[200:203], v[164:167]\n"
      "v_sub_u32 v241, v241, v249\n"
      "v_add_u32 v243, 0x10000, v241\n"
      "v_mfma_f32_16x16x32_bf16 v[164:167], a[52:55], a[196:199], v[164:167]\n"
      "v_exp_f32 v179, v179\n"
      "v_mfma_f32_16x16x32_bf16 v[164:167], a[48:51], a[192:195], v[164:167]\n"
      "s_mov_b32 m0, s34\n"
      "ds_read_b128 a[40:43], v230 offset:16896\n"
      "v_cvt_pk_bf16_f32 v201, v178, v179\n"
      "v_mfma_f32_16x16x32_bf16 v[64:67], v[192:195], a[64:67], 0\n"
      "buffer_load_dwordx4 v250, s[48:51], s93 offen lds\n"
      "v_add_u32 v245, 0x20000, v241\n"
      "v_subrev_u32 v242, s20, v235\n"
      "v_mfma_f32_16x16x32_bf16 v[64:67], v[196:199], a[96:99], v[64:67]\n"
      "s_add_u32 s93, s93, s35\n"
      "v_exp_f32 v180, v180\n"
      "v_mfma_f32_16x16x32_bf16 v[68:71], v[192:195], a[68:71], 0\n"
      "s_sub_u32 s35, 4096, s35\n"
      "v_exp_f32 v181, v181\n"
      "v_mfma_f32_16x16x32_bf16 v[68:71], v[196:199], a[100:103], v[68:71]\n"
      "s_add_u32 m0, s23, 58368\n"
      "s_add_u32 s34, s34, 4096\n"
      "v_cvt_pk_bf16_f32 v202, v180, v181\n"
      "v_sub_u32 v242, v242, v249\n"
      "v_mfma_f32_16x16x32_bf16 v[72:75], v[192:195], a[72:75], 0\n"
      "global_load_lds_dwordx4 v236, s[24:25] offset:3072 nt\n"
      "v_mfma_f32_16x16x32_bf16 v[72:75], v[196:199], a[104:107], v[72:75]\n"
      "v_exp_f32 v182, v182\n"
      "v_mfma_f32_16x16x32_bf16 v[76:79], v[192:195], a[76:79], 0\n"
      "ds_read_b128 a[44:47], v231 offset:17152\n"
      "ds_read_b128 a[60:63], v231 offset:21248\n"
      "v_mfma_f32_16x16x32_bf16 v[76:79], v[196:199], a[108:111], v[76:79]\n"
      "v_exp_f32 v183, v183\n"
      "v_mfma_f32_16x16x32_bf16 v[80:83], v[192:195], a[80:83], 0\n"
      "ds_read_b128 a[56:59], v230 offset:20992\n"
      "v_cvt_pk_bf16_f32 v203, v182, v183\n"
      "v_mfma_f32_16x16x32_bf16 v[80:83], v[196:199], a[112:115], v[80:83]\n"
      "v_exp_f32 v184, v184\n"
      "v_mfma_f32_16x16x32_bf16 v[84:87], v[192:195], a[84:87], 0\n"
      "ds_read_b128 a[52:55], v231 offset:20736\n"
      "v_add_u32 v236, s56, v236\n"
      "v_add_u32 v244, 0x10000, v242\n"
      "v_mfma_f32_16x16x32_bf16 v[84:87], v[196:199], a[116:119], v[84:87]\n"
      "v_exp_f32 v185, v185\n"
      "v_add_u32 v246, 0x20000, v242\n"
      "v_mfma_f32_16x16x32_bf16 v[88:91], v[192:195], a[88:91], 0\n"
      "v_cvt_pk_bf16_f32 v204, v184, v185\n"
      "v_lshlrev_b32 v248, 4, v255\n"
      "v_mfma_f32_16x16x32_bf16 v[88:91], v[196:199], a[120:123], v[88:91]\n"
      "v_exp_f32 v186, v186\n"
      "v_mov_b32 v249, 0x7ffffff0\n"
      "v_mfma_f32_16x16x32_bf16 v[92:95], v[192:195], a[92:95], 0\n"
      "ds_read_b128 a[48:51], v230 offset:20480\n"
      "v_writelane_b32 v249, 0, 0\n"
      "v_mfma_f32_16x16x32_bf16 v[92:95], v[196:199], a[124:127], v[92:95]\n"
      "v_exp_f32 v187, v187\n"
      "v_smfmac_f32_16x16x64_bf16 a[220:223], a[208:211], v[192:199], v247\n"
      "s_waitcnt lgkmcnt(1)\n"
      "v_cvt_pk_bf16_f32 v205, v186, v187\n"
      "v_mfma_f32_16x16x32_bf16 v[176:179], a[0:3], a[128:131], v[208:211]\n"
      "v_exp_f32 v188, v188\n"
      "v_mfma_f32_16x16x32_bf16 v[176:179], a[4:7], a[132:135], v[176:179]\n"
      ".p2align 6\n"
      "LOOP_%=:\n"
      "v_mfma_f32_16x16x32_bf16 v[176:179], a[8:11], a[136:139], v[176:179]\n"
      "v_exp_f32 v189, v189\n"
      "v_mfma_f32_16x16x32_bf16 v[176:179], a[12:15], a[140:143], v[176:179]\n"
      "v_cvt_pk_bf16_f32 v206, v188, v189\n"
      "v_mfma_f32_16x16x32_bf16 v[180:183], a[28:31], a[140:143], v[208:211]\n"
      "v_exp_f32 v190, v190\n"
      "v_mfma_f32_16x16x32_bf16 v[180:183], a[24:27], a[136:139], v[180:183]\n"
      "v_mfma_f32_16x16x32_bf16 v[180:183], a[20:23], a[132:135], v[180:183]\n"
      "v_exp_f32 v191, v191\n"
      "v_mfma_f32_16x16x32_bf16 v[180:183], a[16:19], a[128:131], v[180:183]\n"
      "s_waitcnt lgkmcnt(0)\n"
      "v_cvt_pk_bf16_f32 v207, v190, v191\n"
      "v_mfma_f32_16x16x32_bf16 v[184:187], a[32:35], a[128:131], v[208:211]\n"
      "s_memrealtime s[76:77]\n"
      "v_exp_f32 v160, v160\n"
      "v_mfma_f32_16x16x32_bf16 v[184:187], a[36:39], a[132:135], v[184:187]\n"
      "v_add_f32 v240, v240, v160\n"
      "v_mfma_f32_16x16x32_bf16 v[184:187], a[40:43], a[136:139], v[184:187]\n"
      "v_exp_f32 v161, v161\n"
      "v_mfma_f32_16x16x32_bf16 v[184:187], a[44:47], a[140:143], v[184:187]\n"
      "v_exp_f32 v162, v162\n"
      "v_mfma_f32_16x16x32_bf16 v[188:191], a[60:63], a[140:143], v[208:211]\n"
      "s_add_u32 m0, s23, 114688\n"
      "v_add_f32 v240, v240, v161\n"
      "v_mfma_f32_16x16x32_bf16 v[188:191], a[56:59], a[136:139], v[188:191]\n"
      "global_load_lds_dwordx4 v237, s[28:29] offset:0 nt\n"
      "v_mfma_f32_16x16x32_bf16 v[188:191], a[52:55], a[132:135], v[188:191]\n"
      "v_exp_f32 v163, v163\n"
      "v_mfma_f32_16x16x32_bf16 v[188:191], a[48:51], a[128:131], v[188:191]\n"
      "v_cvt_pk_bf16_f32 v192, v160, v161\n"
      "v_add_f32 v240, v240, v162\n"
      "v_mfma_f32_16x16x32_bf16 v[96:99], v[200:203], a[64:67], v[96:99]\n"
      "s_waitcnt lgkmcnt(0)\n"
      "v_exp_f32 v164, v164\n"
      "v_mfma_f32_16x16x32_bf16 v[96:99], v[204:207], a[96:99], v[96:99]\n"
      "s_sub_u32 s79, s76, s78\n"
      "v_add_f32 v240, v240, v164\n"
      "v_add_f32 v240, v240, v163\n"
      "v_mfma_f32_16x16x32_bf16 v[100:103], v[200:203], a[68:71], v[100:103]\n"
      "s_cbranch_scc1 SPINp0_%=\n"
      "BACKp0_%=:\n"
      "v_exp_f32 v165, v165\n"
      "v_mfma_f32_16x16x32_bf16 v[100:103], v[204:207], a[100:103], v[100:103]\n"
      "s_sub_u32 s76, s76, 4\n"
      "v_add_f32 v240, v240, v165\n"
      "v_cvt_pk_bf16_f32 v194, v164, v165\n"
      "v_mfma_f32_16x16x32_bf16 v[104:107], v[200:203], a[72:75], v[104:107]\n"
      "s_max_u32 s78, s78, s76\n"
      "v_exp_f32 v166, v166\n"
      "v_mfma_f32_16x16x32_bf16 v[104:107], v[204:207], a[104:107], v[104:107]\n"
      "s_sub_u32 s81, s81, 3\n"
      "ds_read_b64_tr_b16 a[64:65], v232 offset:16384\n"
      "v_add_f32 v240, v240, v166\n"
      "v_mfma_f32_16x16x32_bf16 v[108:111], v[200:203], a[76:79], v[108:111]\n"
      "s_max_u32 s81, s81, 140\n"
      "v_exp_f32 v167, v167\n"
      "v_mfma_f32_16x16x32_bf16 v[108:111], v[204:207], a[108:111], v[108:111]\n"
      "s_sub_u32 s79, s76, s83\n"
      "v_add_f32 v240, v240, v167\n"
      "v_cvt_pk_bf16_f32 v195, v166, v167\n"
      "v_mfma_f32_16x16x32_bf16 v[112:115], v[200:203], a[80:83], v[112:115]\n"
      "s_cmp_gt_u32 s79, 340\n"
      "ds_read_b64_tr_b16 a[66:67], v232 offset:20480\n"
      "ds_read_b64_tr_b16 a[68:69], v233 offset:16384\n"
      "v_mfma_f32_16x16x32_bf16 v[112:115], v[204:207], a[112:115], v[112:115]\n"
      "s_cselect_b32 s81, 162, s81\n"
      "v_exp_f32 v176, v176\n"
      "v_mfma_f32_16x16x32_bf16 v[116:119], v[200:203], a[84:87], v[116:119]\n"
      "s_mov_b32 s83, s82\n"
      "ds_read_b64_tr_b16 a[70:71], v233 offset:20480\n"
      "ds_read_b64_tr_b16 a[72:73], v232 offset:16640\n"
      "v_mfma_f32_16x16x32_bf16 v[116:119], v[204:207], a[116:119], v[116:119]\n"
      "s_mov_b32 s82, s76\n"
      "v_exp_f32 v177, v177\n"
      "v_mfma_f32_16x16x32_bf16 v[120:123], v[200:203], a[88:91], v[120:123]\n"
      "s_add_u32 s78, s78, s81\n"
      "ds_read_b64_tr_b16 a[74:75], v232 offset:20736\n"
      "v_cvt_pk_bf16_f32 v193, v162, v163\n"
      "v_mfma_f32_16x16x32_bf16 v[120:123], v[204:207], a[120:123], v[120:123]\n"
      "v_exp_f32 v178, v178\n"
      "v_mfma_f32_16x16x32_bf16 v[124:127], v[200:203], a[92:95], v[124:127]\n"
      "ds_read_b64_tr_b16 a[76:77], v233 offset:16640\n"
      "v_mfma_f32_16x16x32_bf16 v[124:127], v[204:207], a[124:127], v[124:127]\n"
      "v_exp_f32 v179, v179\n"
      "v_smfmac_f32_16x16x64_bf16 a[220:223], a[212:215], v[200:207], v247\n"
      "v_cvt_pk_bf16_f32 v200, v176, v177\n"
      "v_mfma_f32_16x16x32_bf16 v[160:163], a[0:3], a[144:147], v[212:215]\n"
      "v_exp_f32 v180, v180\n"
      "v_mfma_f32_16x16x32_bf16 v[160:163], a[4:7], a[148:151], v[160:163]\n"
      "ds_read_b64_tr_b16 a[78:79], v233 offset:20736\n"
      "v_cvt_pk_bf16_f32 v201, v178, v179\n"
      "v_mfma_f32_16x16x32_bf16 v[160:163], a[8:11], a[152:155], v[160:163]\n"
      "v_exp_f32 v181, v181\n"
      "v_mfma_f32_16x16x32_bf16 v[160:163], a[12:15], a[156:159], v[160:163]\n"
      "v_exp_f32 v182, v182\n"
      "v_mfma_f32_16x16x32_bf16 v[164:167], a[28:31], a[156:159], v[212:215]\n"
      "s_add_u32 m0, s23, 117760\n"
      "v_cvt_pk_bf16_f32 v202, v180, v181\n"
      "v_mfma_f32_16x16x32_bf16 v[164:167], a[24:27], a[152:155], v[164:167]\n"
      "global_load_lds_dwordx4 v237, s[28:29] offset:1024 nt\n"
      "v_mfma_f32_16x16x32_bf16 v[164:167], a[20:23], a[148:151], v[164:167]\n"
      "v_exp_f32 v183, v183\n"
      "v_mfma_f32_16x16x32_bf16 v[164:167], a[16:19], a[144:147], v[164:167]\n"
      "ds_read_b64_tr_b16 a[80:81], v232 offset:16896\n"
      "v_cvt_pk_bf16_f32 v203, v182, v183\n"
      "v_mfma_f32_16x16x32_bf16 v[168:171], a[32:35], a[144:147], v[212:215]\n"
      "v_exp_f32 v184, v184\n"
      "v_mfma_f32_16x16x32_bf16 v[168:171], a[36:39], a[148:151], v[168:171]\n"
      "ds_read_b64_tr_b16 a[82:83], v232 offset:20992\n"
      "ds_read_b64_tr_b16 a[84:85], v233 offset:16896\n"
      "v_mfma_f32_16x16x32_bf16 v[168:171], a[40:43], a[152:155], v[168:171]\n"
      "v_exp_f32 v185, v185\n"
      "v_mfma_f32_16x16x32_bf16 v[168:171], a[44:47], a[156:159], v[168:171]\n"
      "ds_read_b64_tr_b16 a[86:87], v233 offset:20992\n"
      "v_cvt_pk_bf16_f32 v204, v184, v185\n"
      "v_mfma_f32_16x16x32_bf16 v[172:175], a[60:63], a[156:159], v[212:215]\n"
      "v_exp_f32 v186, v186\n"
      "v_mfma_f32_16x16x32_bf16 v[172:175], a[56:59], a[152:155], v[172:175]\n"
      "ds_read_b64_tr_b16 a[88:89], v232 offset:17152\n"
      "ds_read_b64_tr_b16 a[90:91], v232 offset:21248\n"
      "v_mfma_f32_16x16x32_bf16 v[172:175], a[52:55], a[148:151], v[172:175]\n"
      "v_exp_f32 v187, v187\n"
      "v_mfma_f32_16x16x32_bf16 v[172:175], a[48:51], a[144:147], v[172:175]\n"
      "ds_read_b64_tr_b16 a[92:93], v233 offset:17152\n"
      "v_cvt_pk_bf16_f32 v205, v186, v187\n"
      "v_mfma_f32_16x16x32_bf16 v[128:131], v[192:195], a[96:99], v[128:131]\n"
      "v_exp_f32 v188, v188\n"
      "v_mfma_f32_16x16x32_bf16 v[132:135], v[192:195], a[100:103], v[132:135]\n"
      "ds_read_b64_tr_b16 a[96:97], v234 offset:16384\n"
      "ds_read_b64_tr_b16 a[98:99], v234 offset:20480\n"
      "v_mfma_f32_16x16x32_bf16 v[136:139], v[192:195], a[104:107], v[136:139]\n"
      "v_exp_f32 v189, v189\n"
      "v_mfma_f32_16x16x32_bf16 v[140:143], v[192:195], a[108:111], v[140:143]\n"
      "ds_read_b64_tr_b16 a[100:101], v235 offset:16384\n"
      "v_cvt_pk_bf16_f32 v206, v188, v189\n"
      "v_mfma_f32_16x16x32_bf16 v[144:147], v[192:195], a[112:115], v[144:147]\n"
      "v_exp_f32 v190, v190\n"
      "v_mfma_f32_16x16x32_bf16 v[148:151], v[192:195], a[116:119], v[148:151]\n"
      "ds_read_b64_tr_b16 a[102:103], v235 offset:20480\n"
      "ds_read_b64_tr_b16 a[94:95], v233 offset:21248\n"
      "v_mfma_f32_16x16x32_bf16 v[152:155], v[192:195], a[120:123], v[152:155]\n"
      "v_exp_f32 v191, v191\n"
      "v_mfma_f32_16x16x32_bf16 v[156:159], v[192:195], a[124:127], v[156:159]\n"
      "s_add_u32 m0, s23, 120832\n"
      "v_cvt_pk_bf16_f32 v207, v190, v191\n"
      "v_mfma_f32_16x16x32_bf16 v[176:179], a[0:3], a[160:163], v[216:219]\n"
      "global_load_lds_dwordx4 v237, s[28:29] offset:2048 nt\n"
      "v_mfma_f32_16x16x32_bf16 v[176:179], a[4:7], a[164:167], v[176:179]\n"
      "v_exp_f32 v160, v160\n"
      "v_mfma_f32_16x16x32_bf16 v[176:179], a[8:11], a[168:171], v[176:179]\n"
      "ds_read_b64_tr_b16 a[104:105], v234 offset:16640\n"
      "ds_read_b64_tr_b16 a[106:107], v234 offset:20736\n"
      "v_mfma_f32_16x16x32_bf16 v[176:179], a[12:15], a[172:175], v[176:179]\n"
      "v_exp_f32 v161, v161\n"
      "v_mfma_f32_16x16x32_bf16 v[180:183], a[28:31], a[172:175], v[216:219]\n"
      "ds_read_b64_tr_b16 a[108:109], v235 offset:16640\n"
      "ds_read_b64_tr_b16 a[110:111], v235 offset:20736\n"
      "v_mfma_f32_16x16x32_bf16 v[180:183], a[24:27], a[168:171], v[180:183]\n"
      "v_exp_f32 v162, v162\n"
      "v_mfma_f32_16x16x32_bf16 v[180:183], a[20:23], a[164:167], v[180:183]\n"
      "ds_read_b64_tr_b16 a[112:113], v234 offset:16896\n"
      "v_cvt_pk_bf16_f32 v192, v160, v161\n"
      "v_mfma_f32_16x16x32_bf16 v[180:183], a[16:19], a[160:163], v[180:183]\n"
      "v_exp_f32 v163, v163\n"
      "v_mfma_f32_16x16x32_bf16 v[184:187], a[32:35], a[160:163], v[216:219]\n"
      "ds_read_b64_tr_b16 a[114:115], v234 offset:20992\n"
      "v_cvt_pk_bf16_f32 v193, v162, v163\n"
      "v_mfma_f32_16x16x32_bf16 v[184:187], a[36:39], a[164:167], v[184:187]\n"
      "v_exp_f32 v164, v164\n"
      "v_mfma_f32_16x16x32_bf16 v[184:187], a[40:43], a[168:171], v[184:187]\n"
      "ds_read_b64_tr_b16 a[116:117], v235 offset:16896\n"
      "v_mfma_f32_16x16x32_bf16 v[184:187], a[44:47], a[172:175], v[184:187]\n"
      "v_exp_f32 v165, v165\n"
      "v_mfma_f32_16x16x32_bf16 v[188:191], a[60:63], a[172:175], v[216:219]\n"
      "ds_read_b64_tr_b16 a[118:119], v235 offset:20992\n"
      "v_cvt_pk_bf16_f32 v194, v164, v165\n"
      "v_mfma_f32_16x16x32_bf16 v[188:191], a[56:59], a[168:171], v[188:191]\n"
      "v_exp_f32 v166, v166\n"
      "v_mfma_f32_16x16x32_bf16 v[188:191], a[52:55], a[164:167], v[188:191]\n"
      "ds_read_b64_tr_b16 a[120:121], v234 offset:17152\n"
      "ds_read_b64_tr_b16 a[122:123], v234 offset:21248\n"
      "v_mfma_f32_16x16x32_bf16 v[188:191], a[48:51], a[160:163], v[188:191]\n"
      "s_waitcnt lgkmcnt(4)\n"
      "v_exp_f32 v167, v167\n"
      "v_mfma_f32_16x16x32_bf16 v[0:3], v[200:203], a[64:67], v[0:3]\n"
      "v_exp_f32 v168, v168\n"
      "v_mfma_f32_16x16x32_bf16 v[0:3], v[204:207], a[96:99], v[0:3]\n"
      "s_add_u32 m0, s23, 123904\n"
      "ds_read_b64_tr_b16 a[124:125], v235 offset:17152\n"
      "v_mfma_f32_16x16x32_bf16 v[4:7], v[200:203], a[68:71], v[4:7]\n"
      "global_load_lds_dwordx4 v237, s[28:29] offset:3072 nt\n"
      "v_mfma_f32_16x16x32_bf16 v[4:7], v[204:207], a[100:103], v[4:7]\n"
      "v_exp_f32 v169, v169\n"
      "v_mfma_f32_16x16x32_bf16 v[8:11], v[200:203], a[72:75], v[8:11]\n"
      "ds_read_b64_tr_b16 a[126:127], v235 offset:21248\n"
      "v_cvt_pk_bf16_f32 v195, v166, v167\n"
      "v_mfma_f32_16x16x32_bf16 v[8:11], v[204:207], a[104:107], v[8:11]\n"
      "v_exp_f32 v170, v170\n"
      "v_mfma_f32_16x16x32_bf16 v[12:15], v[200:203], a[76:79], v[12:15]\n"
      "v_cvt_pk_bf16_f32 v196, v168, v169\n"
      "v_add_u32 v237, s57, v237\n"
      "v_mfma_f32_16x16x32_bf16 v[12:15], v[204:207], a[108:111], v[12:15]\n"
      "v_exp_f32 v171, v171\n"
      "v_mfma_f32_16x16x32_bf16 v[16:19], v[200:203], a[80:83], v[16:19]\n"
      "v_cvt_pk_bf16_f32 v197, v170, v171\n"
      "v_mfma_f32_16x16x32_bf16 v[16:19], v[204:207], a[112:115], v[16:19]\n"
      "v_exp_f32 v172, v172\n"
      "v_mfma_f32_16x16x32_bf16 v[20:23], v[200:203], a[84:87], v[20:23]\n"
      "s_waitcnt lgkmcnt(0)\n"
      "v_mfma_f32_16x16x32_bf16 v[20:23], v[204:207], a[116:119], v[20:23]\n"
      "v_exp_f32 v173, v173\n"
      "v_mfma_f32_16x16x32_bf16 v[24:27], v[200:203], a[88:91], v[24:27]\n"
      "v_cvt_pk_bf16_f32 v198, v172, v173\n"
      "v_mfma_f32_16x16x32_bf16 v[24:27], v[204:207], a[120:123], v[24:27]\n"
      "v_exp_f32 v174, v174\n"
      "v_mfma_f32_16x16x32_bf16 v[28:31], v[200:203], a[92:95], v[28:31]\n"
      "v_mfma_f32_16x16x32_bf16 v[28:31], v[204:207], a[124:127], v[28:31]\n"
      "v_exp_f32 v175, v175\n"
      "v_smfmac_f32_16x16x64_bf16 a[216:219], a[208:211], v[200:207], v247\n"
      "s_waitcnt vmcnt(8) lgkmcnt(0)\n"
      "v_cvt_pk_bf16_f32 v199, v174, v175\n"
      "v_mfma_f32_16x16x32_bf16 v[160:163], a[0:3], a[176:179], v[220:223]\n"
      "s_barrier\n"
      "v_mfma_f32_16x16x32_bf16 v[160:163], a[4:7], a[180:183], v[160:163]\n"
      "v_exp_f32 v176, v176\n"
      "v_mfma_f32_16x16x32_bf16 v[160:163], a[8:11], a[184:187], v[160:163]\n"
      "ds_read_b128 a[0:3], v228 offset:32768\n"
      "v_mfma_f32_16x16x32_bf16 v[160:163], a[12:15], a[188:191], v[160:163]\n"
      "v_exp_f32 v177, v177\n"
      "v_mfma_f32_16x16x32_bf16 v[164:167], a[28:31], a[188:191], v[220:223]\n"
      "v_exp_f32 v178, v178\n"
      "v_mfma_f32_16x16x32_bf16 v[164:167], a[24:27], a[184:187], v[164:167]\n"
      "s_add_u32 m0, s23, 0\n"
      "v_cvt_pk_bf16_f32 v200, v176, v177\n"
      "v_mfma_f32_16x16x32_bf16 v[164:167], a[20:23], a[180:183], v[164:167]\n"
      "global_load_lds_dwordx4 v236, s[24:25] offset:0 nt\n"
      "v_mfma_f32_16x16x32_bf16 v[164:167], a[16:19], a[176:179], v[164:167]\n"
      "v_exp_f32 v179, v179\n"
      "v_mfma_f32_16x16x32_bf16 v[168:171], a[32:35], a[176:179], v[220:223]\n"
      "ds_read_b128 a[4:7], v229 offset:33024\n"
      "v_cvt_pk_bf16_f32 v201, v178, v179\n"
      "v_mfma_f32_16x16x32_bf16 v[168:171], a[36:39], a[180:183], v[168:171]\n"
      "v_exp_f32 v180, v180\n"
      "v_mfma_f32_16x16x32_bf16 v[168:171], a[40:43], a[184:187], v[168:171]\n"
      "ds_read_b128 a[8:11], v228 offset:33280\n"
      "v_mfma_f32_16x16x32_bf16 v[168:171], a[44:47], a[188:191], v[168:171]\n"
      "v_exp_f32 v181, v181\n"
      "v_mfma_f32_16x16x32_bf16 v[172:175], a[60:63], a[188:191], v[220:223]\n"
      "v_cvt_pk_bf16_f32 v202, v180, v181\n"
      "v_mfma_f32_16x16x32_bf16 v[172:175], a[56:59], a[184:187], v[172:175]\n"
      "v_exp_f32 v182, v182\n"
      "v_mfma_f32_16x16x32_bf16 v[172:175], a[52:55], a[180:183], v[172:175]\n"
      "ds_read_b128 a[12:15], v229 offset:33536\n"
      "v_mfma_f32_16x16x32_bf16 v[172:175], a[48:51], a[176:179], v[172:175]\n"
      "v_exp_f32 v183, v183\n"
      "v_mfma_f32_16x16x32_bf16 v[32:35], v[192:195], a[64:67], v[32:35]\n"
      "v_exp_f32 v184, v184\n"
      "v_mfma_f32_16x16x32_bf16 v[32:35], v[196:199], a[96:99], v[32:35]\n"
      "s_add_u32 m0, s23, 3072\n"
      "v_cvt_pk_bf16_f32 v203, v182, v183\n"
      "v_mfma_f32_16x16x32_bf16 v[36:39], v[192:195], a[68:71], v[36:39]\n"
      "global_load_lds_dwordx4 v236, s[24:25] offset:1024 nt\n"
      "v_mfma_f32_16x16x32_bf16 v[36:39], v[196:199], a[100:103], v[36:39]\n"
      "v_exp_f32 v185, v185\n"
      "v_mfma_f32_16x16x32_bf16 v[40:43], v[192:195], a[72:75], v[40:43]\n"
      "ds_read_b128 a[28:31], v229 offset:37632\n"
      "v_cvt_pk_bf16_f32 v204, v184, v185\n"
      "v_mfma_f32_16x16x32_bf16 v[40:43], v[196:199], a[104:107], v[40:43]\n"
      "v_exp_f32 v186, v186\n"
      "v_mfma_f32_16x16x32_bf16 v[44:47], v[192:195], a[76:79], v[44:47]\n"
      "ds_read_b128 a[24:27], v228 offset:37376\n"
      "v_mfma_f32_16x16x32_bf16 v[44:47], v[196:199], a[108:111], v[44:47]\n"
      "v_exp_f32 v187, v187\n"
      "v_mfma_f32_16x16x32_bf16 v[48:51], v[192:195], a[80:83], v[48:51]\n"
      "v_cvt_pk_bf16_f32 v205, v186, v187\n"
      "v_mfma_f32_16x16x32_bf16 v[48:51], v[196:199], a[112:115], v[48:51]\n"
      "v_exp_f32 v188, v188\n"
      "v_mfma_f32_16x16x32_bf16 v[52:55], v[192:195], a[84:87], v[52:55]\n"
      "ds_read_b128 a[20:23], v229 offset:37120\n"
      "v_mfma_f32_16x16x32_bf16 v[52:55], v[196:199], a[116:119], v[52:55]\n"
      "v_exp_f32 v189, v189\n"
      "v_mfma_f32_16x16x32_bf16 v[56:59], v[192:195], a[88:91], v[56:59]\n"
      "v_exp_f32 v190, v190\n"
      "v_mfma_f32_16x16x32_bf16 v[56:59], v[196:199], a[120:123], v[56:59]\n"
      "s_add_u32 m0, s23, 6144\n"
      "v_cvt_pk_bf16_f32 v206, v188, v189\n"
      "v_mfma_f32_16x16x32_bf16 v[60:63], v[192:195], a[92:95], v[60:63]\n"
      "global_load_lds_dwordx4 v236, s[24:25] offset:2048 nt\n"
      "v_mfma_f32_16x16x32_bf16 v[60:63], v[196:199], a[124:127], v[60:63]\n"
      "v_exp_f32 v191, v191\n"
      "v_smfmac_f32_16x16x64_bf16 a[216:219], a[212:215], v[192:199], v247\n"
      "v_cvt_pk_bf16_f32 v207, v190, v191\n"
      "v_mfma_f32_16x16x32_bf16 v[176:179], a[32:35], a[192:195], v[224:227]\n"
      "v_exp_f32 v160, v160\n"
      "v_mfma_f32_16x16x32_bf16 v[176:179], a[36:39], a[196:199], v[176:179]\n"
      "ds_read_b128 a[16:19], v228 offset:36864\n"
      "ds_read_b128 a[32:35], v230 offset:32768\n"
      "v_mfma_f32_16x16x32_bf16 v[176:179], a[40:43], a[200:203], v[176:179]\n"
      "v_exp_f32 v161, v161\n"
      "v_mfma_f32_16x16x32_bf16 v[176:179], a[44:47], a[204:207], v[176:179]\n"
      "ds_read_b128 a[36:39], v231 offset:33024\n"
      "v_cvt_pk_bf16_f32 v192, v160, v161\n"
      "v_mfma_f32_16x16x32_bf16 v[180:183], a[60:63], a[204:207], v[224:227]\n"
      "v_exp_f32 v162, v162\n"
      "v_mfma_f32_16x16x32_bf16 v[180:183], a[56:59], a[200:203], v[180:183]\n"
      "v_mfma_f32_16x16x32_bf16 v[180:183], a[52:55], a[196:199], v[180:183]\n"
      "v_exp_f32 v163, v163\n"
      "v_mfma_f32_16x16x32_bf16 v[180:183], a[48:51], a[192:195], v[180:183]\n"
      "ds_read_b128 a[40:43], v230 offset:33280\n"
      "v_cvt_pk_bf16_f32 v193, v162, v163\n"
      "v_mfma_f32_16x16x32_bf16 v[64:67], v[200:203], a[64:67], v[64:67]\n"
      "v_mfma_f32_16x16x32_bf16 v[64:67], v[204:207], a[96:99], v[64:67]\n"
      "v_exp_f32 v164, v164\n"
      "v_mfma_f32_16x16x32_bf16 v[68:71], v[200:203], a[68:71], v[68:71]\n"
      "v_exp_f32 v165, v165\n"
      "v_mfma_f32_16x16x32_bf16 v[68:71], v[204:207], a[100:103], v[68:71]\n"
      "s_add_u32 m0, s23, 9216\n"
      "v_cvt_pk_bf16_f32 v194, v164, v165\n"
      "v_mfma_f32_16x16x32_bf16 v[72:75], v[200:203], a[72:75], v[72:75]\n"
      "global_load_lds_dwordx4 v236, s[24:25] offset:3072 nt\n"
      "v_mfma_f32_16x16x32_bf16 v[72:75], v[204:207], a[104:107], v[72:75]\n"
      "s_xor_b32 s56, s56, 0x18000\n"
      "v_exp_f32 v166, v166\n"
      "v_mfma_f32_16x16x32_bf16 v[76:79], v[200:203], a[76:79], v[76:79]\n"
      "ds_read_b128 a[44:47], v231 offset:33536\n"
      "ds_read_b128 a[60:63], v231 offset:37632\n"
      "v_mfma_f32_16x16x32_bf16 v[76:79], v[204:207], a[108:111], v[76:79]\n"
      "v_exp_f32 v167, v167\n"
      "v_mfma_f32_16x16x32_bf16 v[80:83], v[200:203], a[80:83], v[80:83]\n"
      "ds_read_b128 a[56:59], v230 offset:37376\n"
      "v_cvt_pk_bf16_f32 v195, v166, v167\n"
      "v_mfma_f32_16x16x32_bf16 v[80:83], v[204:207], a[112:115], v[80:83]\n"
      "v_exp_f32 v168, v168\n"
      "v_mfma_f32_16x16x32_bf16 v[84:87], v[200:203], a[84:87], v[84:87]\n"
      "ds_read_b128 a[52:55], v231 offset:37120\n"
      "v_add_u32 v236, 0x1000, v236\n"
      "v_mfma_f32_16x16x32_bf16 v[84:87], v[204:207], a[116:119], v[84:87]\n"
      "v_exp_f32 v169, v169\n"
      "v_mfma_f32_16x16x32_bf16 v[88:91], v[200:203], a[88:91], v[88:91]\n"
      "v_cvt_pk_bf16_f32 v196, v168, v169\n"
      "v_mfma_f32_16x16x32_bf16 v[88:91], v[204:207], a[120:123], v[88:91]\n"
      "v_exp_f32 v170, v170\n"
      "v_mfma_f32_16x16x32_bf16 v[92:95], v[200:203], a[92:95], v[92:95]\n"
      "ds_read_b128 a[48:51], v230 offset:36864\n"
      "v_mfma_f32_16x16x32_bf16 v[92:95], v[204:207], a[124:127], v[92:95]\n"
      "v_exp_f32 v171, v171\n"
      "v_smfmac_f32_16x16x64_bf16 a[220:223], a[208:211], v[200:207], v247\n"
      "s_waitcnt lgkmcnt(1)\n"
      "v_cvt_pk_bf16_f32 v197, v170, v171\n"
      "v_mfma_f32_16x16x32_bf16 v[160:163], a[0:3], a[128:131], v[208:211]\n"
      "v_exp_f32 v172, v172\n"
      "v_mfma_f32_16x16x32_bf16 v[160:163], a[4:7], a[132:135], v[160:163]\n"
      "v_mfma_f32_16x16x32_bf16 v[160:163], a[8:11], a[136:139], v[160:163]\n"
      "v_exp_f32 v173, v173\n"
      "v_mfma_f32_16x16x32_bf16 v[160:163], a[12:15], a[140:143], v[160:163]\n"
      "v_cvt_pk_bf16_f32 v198, v172, v173\n"
      "v_mfma_f32_16x16x32_bf16 v[164:167], a[28:31], a[140:143], v[208:211]\n"
      "v_exp_f32 v174, v174\n"
      "v_mfma_f32_16x16x32_bf16 v[164:167], a[24:27], a[136:139], v[164:167]\n"
      "v_mfma_f32_16x16x32_bf16 v[164:167], a[20:23], a[132:135], v[164:167]\n"
      "v_exp_f32 v175, v175\n"
      "v_mfma_f32_16x16x32_bf16 v[164:167], a[16:19], a[128:131], v[164:167]\n"
      "s_waitcnt lgkmcnt(0)\n"
      "v_cvt_pk_bf16_f32 v199, v174, v175\n"
      "v_mfma_f32_16x16x32_bf16 v[168:171], a[32:35], a[128:131], v[208:211]\n"
      "s_memrealtime s[76:77]\n"
      "v_exp_f32 v176, v176\n"
      "v_mfma_f32_16x16x32_bf16 v[168:171], a[36:39], a[132:135], v[168:171]\n"
      "v_add_f32 v240, v240, v176\n"
      "v_mfma_f32_16x16x32_bf16 v[168:171], a[40:43], a[136:139], v[168:171]\n"
      "v_exp_f32 v177, v177\n"
      "v_mfma_f32_16x16x32_bf16 v[168:171], a[44:47], a[140:143], v[168:171]\n"
      "v_exp_f32 v178, v178\n"
      "v_mfma_f32_16x16x32_bf16 v[172:175], a[60:63], a[140:143], v[208:211]\n"
      "s_add_u32 m0, s23, 65536\n"
      "v_add_f32 v240, v240, v177\n"
      "v_mfma_f32_16x16x32_bf16 v[172:175], a[56:59], a[136:139], v[172:175]\n"
      "global_load_lds_dwordx4 v237, s[28:29] offset:0 nt\n"
      "v_mfma_f32_16x16x32_bf16 v[172:175], a[52:55], a[132:135], v[172:175]\n"
      "v_exp_f32 v179, v179\n"
      "v_mfma_f32_16x16x32_bf16 v[172:175], a[48:51], a[128:131], v[172:175]\n"
      "v_cvt_pk_bf16_f32 v200, v176, v177\n"
      "v_add_f32 v240, v240, v178\n"
      "v_mfma_f32_16x16x32_bf16 v[96:99], v[192:195], a[64:67], v[96:99]\n"
      "s_waitcnt lgkmcnt(0)\n"
      "v_exp_f32 v180, v180\n"
      "v_mfma_f32_16x16x32_bf16 v[96:99], v[196:199], a[96:99], v[96:99]\n"
      "s_sub_u32 s79, s76, s78\n"
      "v_add_f32 v240, v240, v180\n"
      "v_add_f32 v240, v240, v179\n"
      "v_mfma_f32_16x16x32_bf16 v[100:103], v[192:195], a[68:71], v[100:103]\n"
      "s_cbranch_scc1 SPINp1_%=\n"
      "BACKp1_%=:\n"
      "v_exp_f32 v181, v181\n"
      "v_mfma_f32_16x16x32_bf16 v[100:103], v[196:199], a[100:103], v[100:103]\n"
      "s_sub_u32 s76, s76, 4\n"
      "v_add_f32 v240, v240, v181\n"
      "v_cvt_pk_bf16_f32 v202, v180, v181\n"
      "v_mfma_f32_16x16x32_bf16 v[104:107], v[192:195], a[72:75], v[104:107]\n"
      "s_max_u32 s78, s78, s76\n"
      "v_exp_f32 v182, v182\n"
      "v_mfma_f32_16x16x32_bf16 v[104:107], v[196:199], a[104:107], v[104:107]\n"
      "s_sub_u32 s81, s81, 3\n"
      "ds_read_b64_tr_b16 a[64:65], v232 offset:32768\n"
      "v_add_f32 v240, v240, v182\n"
      "v_mfma_f32_16x16x32_bf16 v[108:111], v[192:195], a[76:79], v[108:111]\n"
      "s_max_u32 s81, s81, 140\n"
      "v_exp_f32 v183, v183\n"
      "v_mfma_f32_16x16x32_bf16 v[108:111], v[196:199], a[108:111], v[108:111]\n"
      "s_sub_u32 s79, s76, s83\n"
      "v_add_f32 v240, v240, v183\n"
      "v_cvt_pk_bf16_f32 v203, v182, v183\n"
      "v_mfma_f32_16x16x32_bf16 v[112:115], v[192:195], a[80:83], v[112:115]\n"
      "s_cmp_gt_u32 s79, 340\n"
      "ds_read_b64_tr_b16 a[66:67], v232 offset:36864\n"
      "ds_read_b64_tr_b16 a[68:69], v233 offset:32768\n"
      "v_mfma_f32_16x16x32_bf16 v[112:115], v[196:199], a[112:115], v[112:115]\n"
      "s_cselect_b32 s81, 162, s81\n"
      "v_exp_f32 v160, v160\n"
      "v_mfma_f32_16x16x32_bf16 v[116:119], v[192:195], a[84:87], v[116:119]\n"
      "s_mov_b32 s83, s82\n"
      "ds_read_b64_tr_b16 a[70:71], v233 offset:36864\n"
      "ds_read_b64_tr_b16 a[72:73], v232 offset:33024\n"
      "v_mfma_f32_16x16x32_bf16 v[116:119], v[196:199], a[116:119], v[116:119]\n"
      "s_mov_b32 s82, s76\n"
      "v_exp_f32 v161, v161\n"
      "v_mfma_f32_16x16x32_bf16 v[120:123], v[192:195], a[88:91], v[120:123]\n"
      "s_add_u32 s78, s78, s81\n"
      "ds_read_b64_tr_b16 a[74:75], v232 offset:37120\n"
      "v_cvt_pk_bf16_f32 v201, v178, v179\n"
      "v_mfma_f32_16x16x32_bf16 v[120:123], v[196:199], a[120:123], v[120:123]\n"
      "v_exp_f32 v162, v162\n"
      "v_mfma_f32_16x16x32_bf16 v[124:127], v[192:195], a[92:95], v[124:127]\n"
      "ds_read_b64_tr_b16 a[76:77], v233 offset:33024\n"
      "v_mfma_f32_16x16x32_bf16 v[124:127], v[196:199], a[124:127], v[124:127]\n"
      "v_exp_f32 v163, v163\n"
      "v_smfmac_f32_16x16x64_bf16 a[220:223], a[212:215], v[192:199], v247\n"
      "v_cvt_pk_bf16_f32 v192, v160, v161\n"
      "v_mfma_f32_16x16x32_bf16 v[176:179], a[0:3], a[144:147], v[212:215]\n"
      "v_exp_f32 v164, v164\n"
      "v_mfma_f32_16x16x32_bf16 v[176:179], a[4:7], a[148:151], v[176:179]\n"
      "ds_read_b64_tr_b16 a[78:79], v233 offset:37120\n"
      "v_cvt_pk_bf16_f32 v193, v162, v163\n"
      "v_mfma_f32_16x16x32_bf16 v[176:179], a[8:11], a[152:155], v[176:179]\n"
      "v_exp_f32 v165, v165\n"
      "v_mfma_f32_16x16x32_bf16 v[176:179], a[12:15], a[156:159], v[176:179]\n"
      "v_exp_f32 v166, v166\n"
      "v_mfma_f32_16x16x32_bf16 v[180:183], a[28:31], a[156:159], v[212:215]\n"
      "s_add_u32 m0, s23, 68608\n"
      "v_cvt_pk_bf16_f32 v194, v164, v165\n"
      "v_mfma_f32_16x16x32_bf16 v[180:183], a[24:27], a[152:155], v[180:183]\n"
      "global_load_lds_dwordx4 v237, s[28:29] offset:1024 nt\n"
      "v_mfma_f32_16x16x32_bf16 v[180:183], a[20:23], a[148:151], v[180:183]\n"
      "v_exp_f32 v167, v167\n"
      "v_mfma_f32_16x16x32_bf16 v[180:183], a[16:19], a[144:147], v[180:183]\n"
      "ds_read_b64_tr_b16 a[80:81], v232 offset:33280\n"
      "v_cvt_pk_bf16_f32 v195, v166, v167\n"
      "v_mfma_f32_16x16x32_bf16 v[184:187], a[32:35], a[144:147], v[212:215]\n"
      "v_exp_f32 v168, v168\n"
      "v_mfma_f32_16x16x32_bf16 v[184:187], a[36:39], a[148:151], v[184:187]\n"
      "ds_read_b64_tr_b16 a[82:83], v232 offset:37376\n"
      "ds_read_b64_tr_b16 a[84:85], v233 offset:33280\n"
      "v_mfma_f32_16x16x32_bf16 v[184:187], a[40:43], a[152:155], v[184:187]\n"
      "v_exp_f32 v169, v169\n"
      "v_mfma_f32_16x16x32_bf16 v[184:187], a[44:47], a[156:159], v[184:187]\n"
      "ds_read_b64_tr_b16 a[86:87], v233 offset:37376\n"
      "v_cvt_pk_bf16_f32 v196, v168, v169\n"
      "v_mfma_f32_16x16x32_bf16 v[188:191], a[60:63], a[156:159], v[212:215]\n"
      "v_exp_f32 v170, v170\n"
      "v_mfma_f32_16x16x32_bf16 v[188:191], a[56:59], a[152:155], v[188:191]\n"
      "ds_read_b64_tr_b16 a[88:89], v232 offset:33536\n"
      "ds_read_b64_tr_b16 a[90:91], v232 offset:37632\n"
      "v_mfma_f32_16x16x32_bf16 v[188:191], a[52:55], a[148:151], v[188:191]\n"
      "v_exp_f32 v171, v171\n"
      "v_mfma_f32_16x16x32_bf16 v[188:191], a[48:51], a[144:147], v[188:191]\n"
      "ds_read_b64_tr_b16 a[92:93], v233 offset:33536\n"
      "v_cvt_pk_bf16_f32 v197, v170, v171\n"
      "v_mfma_f32_16x16x32_bf16 v[128:131], v[200:203], a[96:99], v[128:131]\n"
      "v_exp_f32 v172, v172\n"
      "v_mfma_f32_16x16x32_bf16 v[132:135], v[200:203], a[100:103], v[132:135]\n"
      "ds_read_b64_tr_b16 a[96:97], v234 offset:32768\n"
      "ds_read_b64_tr_b16 a[98:99], v234 offset:36864\n"
      "v_mfma_f32_16x16x32_bf16 v[136:139], v[200:203], a[104:107], v[136:139]\n"
      "v_exp_f32 v173, v173\n"
      "v_mfma_f32_16x16x32_bf16 v[140:143], v[200:203], a[108:111], v[140:143]\n"
      "ds_read_b64_tr_b16 a[100:101], v235 offset:32768\n"
      "v_cvt_pk_bf16_f32 v198, v172, v173\n"
      "v_mfma_f32_16x16x32_bf16 v[144:147], v[200:203], a[112:115], v[144:147]\n"
      "v_exp_f32 v174, v174\n"
      "v_mfma_f32_16x16x32_bf16 v[148:151], v[200:203], a[116:119], v[148:151]\n"
      "ds_read_b64_tr_b16 a[102:103], v235 offset:36864\n"
      "ds_read_b64_tr_b16 a[94:95], v233 offset:37632\n"
      "v_mfma_f32_16x16x32_bf16 v[152:155], v[200:203], a[120:123], v[152:155]\n"
      "v_exp_f32 v175, v175\n"
      "v_mfma_f32_16x16x32_bf16 v[156:159], v[200:203], a[124:127], v[156:159]\n"
      "s_add_u32 m0, s23, 71680\n"
      "v_cvt_pk_bf16_f32 v199, v174, v175\n"
      "v_mfma_f32_16x16x32_bf16 v[160:163], a[0:3], a[160:163], v[216:219]\n"
      "global_load_lds_dwordx4 v237, s[28:29] offset:2048 nt\n"
      "v_mfma_f32_16x16x32_bf16 v[160:163], a[4:7], a[164:167], v[160:163]\n"
      "v_exp_f32 v176, v176\n"
      "v_mfma_f32_16x16x32_bf16 v[160:163], a[8:11], a[168:171], v[160:163]\n"
      "ds_read_b64_tr_b16 a[104:105], v234 offset:33024\n"
      "ds_read_b64_tr_b16 a[106:107], v234 offset:37120\n"
      "v_mfma_f32_16x16x32_bf16 v[160:163], a[12:15], a[172:175], v[160:163]\n"
      "v_exp_f32 v177, v177\n"
      "v_mfma_f32_16x16x32_bf16 v[164:167], a[28:31], a[172:175], v[216:219]\n"
      "ds_read_b64_tr_b16 a[108:109], v235 offset:33024\n"
      "ds_read_b64_tr_b16 a[110:111], v235 offset:37120\n"
      "v_mfma_f32_16x16x32_bf16 v[164:167], a[24:27], a[168:171], v[164:167]\n"
      "v_exp_f32 v178, v178\n"
      "v_mfma_f32_16x16x32_bf16 v[164:167], a[20:23], a[164:167], v[164:167]\n"
      "ds_read_b64_tr_b16 a[112:113], v234 offset:33280\n"
      "v_cvt_pk_bf16_f32 v200, v176, v177\n"
      "v_mfma_f32_16x16x32_bf16 v[164:167], a[16:19], a[160:163], v[164:167]\n"
      "v_exp_f32 v179, v179\n"
      "v_mfma_f32_16x16x32_bf16 v[168:171], a[32:35], a[160:163], v[216:219]\n"
      "ds_read_b64_tr_b16 a[114:115], v234 offset:37376\n"
      "v_cvt_pk_bf16_f32 v201, v178, v179\n"
      "v_mfma_f32_16x16x32_bf16 v[168:171], a[36:39], a[164:167], v[168:171]\n"
      "v_exp_f32 v180, v180\n"
      "v_mfma_f32_16x16x32_bf16 v[168:171], a[40:43], a[168:171], v[168:171]\n"
      "ds_read_b64_tr_b16 a[116:117], v235 offset:33280\n"
      "v_mfma_f32_16x16x32_bf16 v[168:171], a[44:47], a[172:175], v[168:171]\n"
      "v_exp_f32 v181, v181\n"
      "v_mfma_f32_16x16x32_bf16 v[172:175], a[60:63], a[172:175], v[216:219]\n"
      "ds_read_b64_tr_b16 a[118:119], v235 offset:37376\n"
      "v_cvt_pk_bf16_f32 v202, v180, v181\n"
      "v_mfma_f32_16x16x32_bf16 v[172:175], a[56:59], a[168:171], v[172:175]\n"
      "v_exp_f32 v182, v182\n"
      "v_mfma_f32_16x16x32_bf16 v[172:175], a[52:55], a[164:167], v[172:175]\n"
      "ds_read_b64_tr_b16 a[120:121], v234 offset:33536\n"
      "ds_read_b64_tr_b16 a[122:123], v234 offset:37632\n"
      "v_mfma_f32_16x16x32_bf16 v[172:175], a[48:51], a[160:163], v[172:175]\n"
      "s_waitcnt lgkmcnt(4)\n"
      "v_exp_f32 v183, v183\n"
      "v_mfma_f32_16x16x32_bf16 v[0:3], v[192:195], a[64:67], v[0:3]\n"
      "v_exp_f32 v184, v184\n"
      "v_mfma_f32_16x16x32_bf16 v[0:3], v[196:199], a[96:99], v[0:3]\n"
      "s_add_u32 m0, s23, 74752\n"
      "ds_read_b64_tr_b16 a[124:125], v235 offset:33536\n"
      "v_mfma_f32_16x16x32_bf16 v[4:7], v[192:195], a[68:71], v[4:7]\n"
      "global_load_lds_dwordx4 v237, s[28:29] offset:3072 nt\n"
      "v_mfma_f32_16x16x32_bf16 v[4:7], v[196:199], a[100:103], v[4:7]\n"
      "s_xor_b32 s57, s57, 0x18000\n"
      "v_exp_f32 v185, v185\n"
      "v_mfma_f32_16x16x32_bf16 v[8:11], v[192:195], a[72:75], v[8:11]\n"
      "ds_read_b64_tr_b16 a[126:127], v235 offset:37632\n"
      "v_cvt_pk_bf16_f32 v203, v182, v183\n"
      "v_mfma_f32_16x16x32_bf16 v[8:11], v[196:199], a[104:107], v[8:11]\n"
      "v_exp_f32 v186, v186\n"
      "v_mfma_f32_16x16x32_bf16 v[12:15], v[192:195], a[76:79], v[12:15]\n"
      "v_cvt_pk_bf16_f32 v204, v184, v185\n"
      "v_add_u32 v237, 0x1000, v237\n"
      "v_mfma_f32_16x16x32_bf16 v[12:15], v[196:199], a[108:111], v[12:15]\n"
      "v_exp_f32 v187, v187\n"
      "v_mfma_f32_16x16x32_bf16 v[16:19], v[192:195], a[80:83], v[16:19]\n"
      "s_waitcnt lgkmcnt(1)\n"
      "v_cvt_pk_bf16_f32 v205, v186, v187\n"
      "v_mfma_f32_16x16x32_bf16 v[16:19], v[196:199], a[112:115], v[16:19]\n"
      "s_mov_b32 m0, s34\n"
      "v_exp_f32 v188, v188\n"
      "v_mfma_f32_16x16x32_bf16 v[20:23], v[192:195], a[84:87], v[20:23]\n"
      "buffer_load_dwordx4 v250, s[48:51], s93 offen lds\n"
      "v_mfma_f32_16x16x32_bf16 v[20:23], v[196:199], a[116:119], v[20:23]\n"
      "s_add_u32 s93, s93, s35\n"
      "v_exp_f32 v189, v189\n"
      "v_mfma_f32_16x16x32_bf16 v[24:27], v[192:195], a[88:91], v[24:27]\n"
      "s_sub_u32 s35, 4096, s35\n"
      "v_cvt_pk_bf16_f32 v206, v188, v189\n"
      "v_mfma_f32_16x16x32_bf16 v[24:27], v[196:199], a[120:123], v[24:27]\n"
      "s_add_u32 s34, s34, 4096\n"
      "v_exp_f32 v190, v190\n"
      "v_mfma_f32_16x16x32_bf16 v[28:31], v[192:195], a[92:95], v[28:31]\n"
      "s_waitcnt lgkmcnt(0)\n"
      "v_mfma_f32_16x16x32_bf16 v[28:31], v[196:199], a[124:127], v[28:31]\n"
      "v_exp_f32 v191, v191\n"
      "v_smfmac_f32_16x16x64_bf16 a[216:219], a[208:211], v[192:199], v247\n"
      "s_waitcnt vmcnt(9) lgkmcnt(0)\n"
      "v_cvt_pk_bf16_f32 v207, v190, v191\n"
      "v_mfma_f32_16x16x32_bf16 v[176:179], a[0:3], a[176:179], v[220:223]\n"
      "s_barrier\n"
      "v_mfma_f32_16x16x32_bf16 v[176:179], a[4:7], a[180:183], v[176:179]\n"
      "v_exp_f32 v160, v160\n"
      "v_mfma_f32_16x16x32_bf16 v[176:179], a[8:11], a[184:187], v[176:179]\n"
      "ds_read_b128 a[0:3], v228 offset:49152\n"
      "v_mfma_f32_16x16x32_bf16 v[176:179], a[12:15], a[188:191], v[176:179]\n"
      "v_exp_f32 v161, v161\n"
      "v_mfma_f32_16x16x32_bf16 v[180:183], a[28:31], a[188:191], v[220:223]\n"
      "v_exp_f32 v162, v162\n"
      "v_mfma_f32_16x16x32_bf16 v[180:183], a[24:27], a[184:187], v[180:183]\n"
      "s_add_u32 m0, s23, 16384\n"
      "v_cvt_pk_bf16_f32 v192, v160, v161\n"
      "v_mfma_f32_16x16x32_bf16 v[180:183], a[20:23], a[180:183], v[180:183]\n"
      "global_load_lds_dwordx4 v236, s[24:25] offset:0 nt\n"
      "v_mfma_f32_16x16x32_bf16 v[180:183], a[16:19], a[176:179], v[180:183]\n"
      "v_exp_f32 v163, v163\n"
      "v_mfma_f32_16x16x32_bf16 v[184:187], a[32:35], a[176:179], v[220:223]\n"
      "ds_read_b128 a[4:7], v229 offset:49408\n"
      "v_cvt_pk_bf16_f32 v193, v162, v163\n"
      "v_mfma_f32_16x16x32_bf16 v[184:187], a[36:39], a[180:183], v[184:187]\n"
      "v_exp_f32 v164, v164\n"
      "v_mfma_f32_16x16x32_bf16 v[184:187], a[40:43], a[184:187], v[184:187]\n"
      "ds_read_b128 a[8:11], v228 offset:49664\n"
      "v_mfma_f32_16x16x32_bf16 v[184:187], a[44:47], a[188:191], v[184:187]\n"
      "v_exp_f32 v165, v165\n"
      "v_mfma_f32_16x16x32_bf16 v[188:191], a[60:63], a[188:191], v[220:223]\n"
      "v_cvt_pk_bf16_f32 v194, v164, v165\n"
      "v_mfma_f32_16x16x32_bf16 v[188:191], a[56:59], a[184:187], v[188:191]\n"
      "v_exp_f32 v166, v166\n"
      "v_mfma_f32_16x16x32_bf16 v[188:191], a[52:55], a[180:183], v[188:191]\n"
      "ds_read_b128 a[12:15], v229 offset:49920\n"
      "v_mfma_f32_16x16x32_bf16 v[188:191], a[48:51], a[176:179], v[188:191]\n"
      "v_exp_f32 v167, v167\n"
      "v_mfma_f32_16x16x32_bf16 v[32:35], v[200:203], a[64:67], v[32:35]\n"
      "v_exp_f32 v168, v168\n"
      "v_mfma_f32_16x16x32_bf16 v[32:35], v[204:207], a[96:99], v[32:35]\n"
      "s_add_u32 m0, s23, 19456\n"
      "v_cvt_pk_bf16_f32 v195, v166, v167\n"
      "v_mfma_f32_16x16x32_bf16 v[36:39], v[200:203], a[68:71], v[36:39]\n"
      "global_load_lds_dwordx4 v236, s[24:25] offset:1024 nt\n"
      "v_mfma_f32_16x16x32_bf16 v[36:39], v[204:207], a[100:103], v[36:39]\n"
      "v_exp_f32 v169, v169\n"
      "v_mfma_f32_16x16x32_bf16 v[40:43], v[200:203], a[72:75], v[40:43]\n"
      "ds_read_b128 a[28:31], v229 offset:54016\n"
      "v_cvt_pk_bf16_f32 v196, v168, v169\n"
      "v_mfma_f32_16x16x32_bf16 v[40:43], v[204:207], a[104:107], v[40:43]\n"
      "v_exp_f32 v170, v170\n"
      "v_mfma_f32_16x16x32_bf16 v[44:47], v[200:203], a[76:79], v[44:47]\n"
      "ds_read_b128 a[24:27], v228 offset:53760\n"
      "v_mfma_f32_16x16x32_bf16 v[44:47], v[204:207], a[108:111], v[44:47]\n"
      "v_exp_f32 v171, v171\n"
      "v_mfma_f32_16x16x32_bf16 v[48:51], v[200:203], a[80:83], v[48:51]\n"
      "v_cvt_pk_bf16_f32 v197, v170, v171\n"
      "v_mfma_f32_16x16x32_bf16 v[48:51], v[204:207], a[112:115], v[48:51]\n"
      "v_exp_f32 v172, v172\n"
      "v_mfma_f32_16x16x32_bf16 v[52:55], v[200:203], a[84:87], v[52:55]\n"
      "ds_read_b128 a[20:23], v229 offset:53504\n"
      "v_mfma_f32_16x16x32_bf16 v[52:55], v[204:207], a[116:119], v[52:55]\n"
      "v_exp_f32 v173, v173\n"
      "v_mfma_f32_16x16x32_bf16 v[56:59], v[200:203], a[88:91], v[56:59]\n"
      "v_exp_f32 v174, v174\n"
      "v_mfma_f32_16x16x32_bf16 v[56:59], v[204:207], a[120:123], v[56:59]\n"
      "s_add_u32 m0, s23, 22528\n"
      "v_cvt_pk_bf16_f32 v198, v172, v173\n"
      "v_mfma_f32_16x16x32_bf16 v[60:63], v[200:203], a[92:95], v[60:63]\n"
      "global_load_lds_dwordx4 v236, s[24:25] offset:2048 nt\n"
      "v_mfma_f32_16x16x32_bf16 v[60:63], v[204:207], a[124:127], v[60:63]\n"
      "v_exp_f32 v175, v175\n"
      "v_smfmac_f32_16x16x64_bf16 a[216:219], a[212:215], v[200:207], v247\n"
      "v_cvt_pk_bf16_f32 v199, v174, v175\n"
      "v_mfma_f32_16x16x32_bf16 v[160:163], a[32:35], a[192:195], v[224:227]\n"
      "v_exp_f32 v176, v176\n"
      "v_mfma_f32_16x16x32_bf16 v[160:163], a[36:39], a[196:199], v[160:163]\n"
      "ds_read_b128 a[16:19], v228 offset:53248\n"
      "ds_read_b128 a[32:35], v230 offset:49152\n"
      "v_mfma_f32_16x16x32_bf16 v[160:163], a[40:43], a[200:203], v[160:163]\n"
      "v_exp_f32 v177, v177\n"
      "v_mfma_f32_16x16x32_bf16 v[160:163], a[44:47], a[204:207], v[160:163]\n"
      "ds_read_b128 a[36:39], v231 offset:49408\n"
      "v_cvt_pk_bf16_f32 v200, v176, v177\n"
      "v_mfma_f32_16x16x32_bf16 v[164:167], a[60:63], a[204:207], v[224:227]\n"
      "v_exp_f32 v178, v178\n"
      "v_mfma_f32_16x16x32_bf16 v[164:167], a[56:59], a[200:203], v[164:167]\n"
      "v_mfma_f32_16x16x32_bf16 v[164:167], a[52:55], a[196:199], v[164:167]\n"
      "v_exp_f32 v179, v179\n"
      "v_mfma_f32_16x16x32_bf16 v[164:167], a[48:51], a[192:195], v[164:167]\n"
      "ds_read_b128 a[40:43], v230 offset:49664\n"
      "v_cvt_pk_bf16_f32 v201, v178, v179\n"
      "v_mfma_f32_16x16x32_bf16 v[64:67], v[192:195], a[64:67], v[64:67]\n"
      "v_mfma_f32_16x16x32_bf16 v[64:67], v[196:199], a[96:99], v[64:67]\n"
      "v_exp_f32 v180, v180\n"
      "v_mfma_f32_16x16x32_bf16 v[68:71], v[192:195], a[68:71], v[68:71]\n"
      "v_exp_f32 v181, v181\n"
      "v_mfma_f32_16x16x32_bf16 v[68:71], v[196:199], a[100:103], v[68:71]\n"
      "s_add_u32 m0, s23, 25600\n"
      "v_cvt_pk_bf16_f32 v202, v180, v181\n"
      "v_mfma_f32_16x16x32_bf16 v[72:75], v[192:195], a[72:75], v[72:75]\n"
      "global_load_lds_dwordx4 v236, s[24:25] offset:3072 nt\n"
      "v_mfma_f32_16x16x32_bf16 v[72:75], v[196:199], a[104:107], v[72:75]\n"
      "v_exp_f32 v182, v182\n"
      "v_mfma_f32_16x16x32_bf16 v[76:79], v[192:195], a[76:79], v[76:79]\n"
      "ds_read_b128 a[44:47], v231 offset:49920\n"
      "ds_read_b128 a[60:63], v231 offset:54016\n"
      "v_mfma_f32_16x16x32_bf16 v[76:79], v[196:199], a[108:111], v[76:79]\n"
      "v_exp_f32 v183, v183\n"
      "v_mfma_f32_16x16x32_bf16 v[80:83], v[192:195], a[80:83], v[80:83]\n"
      "ds_read_b128 a[56:59], v230 offset:53760\n"
      "v_cvt_pk_bf16_f32 v203, v182, v183\n"
      "v_mfma_f32_16x16x32_bf16 v[80:83], v[196:199], a[112:115], v[80:83]\n"
      "v_exp_f32 v184, v184\n"
      "v_mfma_f32_16x16x32_bf16 v[84:87], v[192:195], a[84:87], v[84:87]\n"
      "ds_read_b128 a[52:55], v231 offset:53504\n"
      "v_add_u32 v236, 0x1000, v236\n"
      "v_mfma_f32_16x16x32_bf16 v[84:87], v[196:199], a[116:119], v[84:87]\n"
      "v_exp_f32 v185, v185\n"
      "v_mfma_f32_16x16x32_bf16 v[88:91], v[192:195], a[88:91], v[88:91]\n"
      "v_cvt_pk_bf16_f32 v204, v184, v185\n"
      "v_mfma_f32_16x16x32_bf16 v[88:91], v[196:199], a[120:123], v[88:91]\n"
      "v_exp_f32 v186, v186\n"
      "v_mfma_f32_16x16x32_bf16 v[92:95], v[192:195], a[92:95], v[92:95]\n"
      "ds_read_b128 a[48:51], v230 offset:53248\n"
      "v_mfma_f32_16x16x32_bf16 v[92:95], v[196:199], a[124:127], v[92:95]\n"
      "v_exp_f32 v187, v187\n"
      "v_smfmac_f32_16x16x64_bf16 a[220:223], a[208:211], v[192:199], v247\n"
      "s_waitcnt lgkmcnt(1)\n"
      "v_cvt_pk_bf16_f32 v205, v186, v187\n"
      "v_mfma_f32_16x16x32_bf16 v[176:179], a[0:3], a[128:131], v[208:211]\n"
      "v_exp_f32 v188, v188\n"
      "v_mfma_f32_16x16x32_bf16 v[176:179], a[4:7], a[132:135], v[176:179]\n"
      "v_mfma_f32_16x16x32_bf16 v[176:179], a[8:11], a[136:139], v[176:179]\n"
      "v_exp_f32 v189, v189\n"
      "v_mfma_f32_16x16x32_bf16 v[176:179], a[12:15], a[140:143], v[176:179]\n"
      "v_cvt_pk_bf16_f32 v206, v188, v189\n"
      "v_mfma_f32_16x16x32_bf16 v[180:183], a[28:31], a[140:143], v[208:211]\n"
      "v_exp_f32 v190, v190\n"
      "v_mfma_f32_16x16x32_bf16 v[180:183], a[24:27], a[136:139], v[180:183]\n"
      "v_mfma_f32_16x16x32_bf16 v[180:183], a[20:23], a[132:135], v[180:183]\n"
      "v_exp_f32 v191, v191\n"
      "v_mfma_f32_16x16x32_bf16 v[180:183], a[16:19], a[128:131], v[180:183]\n"
      "s_waitcnt lgkmcnt(0)\n"
      "v_cvt_pk_bf16_f32 v207, v190, v191\n"
      "v_mfma_f32_16x16x32_bf16 v[184:187], a[32:35], a[128:131], v[208:211]\n"
      "s_memrealtime s[76:77]\n"
      "v_exp_f32 v160, v160\n"
      "v_mfma_f32_16x16x32_bf16 v[184:187], a[36:39], a[132:135], v[184:187]\n"
      "v_add_f32 v240, v240, v160\n"
      "v_mfma_f32_16x16x32_bf16 v[184:187], a[40:43], a[136:139], v[184:187]\n"
      "v_exp_f32 v161, v161\n"
      "v_mfma_f32_16x16x32_bf16 v[184:187], a[44:47], a[140:143], v[184:187]\n"
      "v_exp_f32 v162, v162\n"
      "v_mfma_f32_16x16x32_bf16 v[188:191], a[60:63], a[140:143], v[208:211]\n"
      "s_add_u32 m0, s23, 81920\n"
      "v_add_f32 v240, v240, v161\n"
      "v_mfma_f32_16x16x32_bf16 v[188:191], a[56:59], a[136:139], v[188:191]\n"
      "global_load_lds_dwordx4 v237, s[28:29] offset:0 nt\n"
      "v_mfma_f32_16x16x32_bf16 v[188:191], a[52:55], a[132:135], v[188:191]\n"
      "v_exp_f32 v163, v163\n"
      "v_mfma_f32_16x16x32_bf16 v[188:191], a[48:51], a[128:131], v[188:191]\n"
      "v_cvt_pk_bf16_f32 v192, v160, v161\n"
      "v_add_f32 v240, v240, v162\n"
      "v_mfma_f32_16x16x32_bf16 v[96:99], v[200:203], a[64:67], v[96:99]\n"
      "s_waitcnt lgkmcnt(0)\n"
      "v_exp_f32 v164, v164\n"
      "v_mfma_f32_16x16x32_bf16 v[96:99], v[204:207], a[96:99], v[96:99]\n"
      "s_sub_u32 s79, s76, s78\n"
      "v_add_f32 v240, v240, v164\n"
      "v_add_f32 v240, v240, v163\n"
      "v_mfma_f32_16x16x32_bf16 v[100:103], v[200:203], a[68:71], v[100:103]\n"
      "s_cbranch_scc1 SPINp2_%=\n"
      "BACKp2_%=:\n"
      "v_exp_f32 v165, v165\n"
      "v_mfma_f32_16x16x32_bf16 v[100:103], v[204:207], a[100:103], v[100:103]\n"
      "s_sub_u32 s76, s76, 4\n"
      "v_add_f32 v240, v240, v165\n"
      "v_cvt_pk_bf16_f32 v194, v164, v165\n"
      "v_mfma_f32_16x16x32_bf16 v[104:107], v[200:203], a[72:75], v[104:107]\n"
      "s_max_u32 s78, s78, s76\n"
      "v_exp_f32 v166, v166\n"
      "v_mfma_f32_16x16x32_bf16 v[104:107], v[204:207], a[104:107], v[104:107]\n"
      "s_sub_u32 s81, s81, 3\n"
      "ds_read_b64_tr_b16 a[64:65], v232 offset:49152\n"
      "v_add_f32 v240, v240, v166\n"
      "v_mfma_f32_16x16x32_bf16 v[108:111], v[200:203], a[76:79], v[108:111]\n"
      "s_max_u32 s81, s81, 140\n"
      "v_exp_f32 v167, v167\n"
      "v_mfma_f32_16x16x32_bf16 v[108:111], v[204:207], a[108:111], v[108:111]\n"
      "s_sub_u32 s79, s76, s83\n"
      "v_add_f32 v240, v240, v167\n"
      "v_cvt_pk_bf16_f32 v195, v166, v167\n"
      "v_mfma_f32_16x16x32_bf16 v[112:115], v[200:203], a[80:83], v[112:115]\n"
      "s_cmp_gt_u32 s79, 340\n"
      "ds_read_b64_tr_b16 a[66:67], v232 offset:53248\n"
      "ds_read_b64_tr_b16 a[68:69], v233 offset:49152\n"
      "v_mfma_f32_16x16x32_bf16 v[112:115], v[204:207], a[112:115], v[112:115]\n"
      "s_cselect_b32 s81, 162, s81\n"
      "v_exp_f32 v176, v176\n"
      "v_mfma_f32_16x16x32_bf16 v[116:119], v[200:203], a[84:87], v[116:119]\n"
      "s_mov_b32 s83, s82\n"
      "ds_read_b64_tr_b16 a[70:71], v233 offset:53248\n"
      "ds_read_b64_tr_b16 a[72:73], v232 offset:49408\n"
      "v_mfma_f32_16x16x32_bf16 v[116:119], v[204:207], a[116:119], v[116:119]\n"
      "s_mov_b32 s82, s76\n"
      "v_exp_f32 v177, v177\n"
      "v_mfma_f32_16x16x32_bf16 v[120:123], v[200:203], a[88:91], v[120:123]\n"
      "s_add_u32 s78, s78, s81\n"
      "ds_read_b64_tr_b16 a[74:75], v232 offset:53504\n"
      "v_cvt_pk_bf16_f32 v193, v162, v163\n"
      "v_mfma_f32_16x16x32_bf16 v[120:123], v[204:207], a[120:123], v[120:123]\n"
      "v_exp_f32 v178, v178\n"
      "v_mfma_f32_16x16x32_bf16 v[124:127], v[200:203], a[92:95], v[124:127]\n"
      "ds_read_b64_tr_b16 a[76:77], v233 offset:49408\n"
      "v_mfma_f32_16x16x32_bf16 v[124:127], v[204:207], a[124:127], v[124:127]\n"
      "v_exp_f32 v179, v179\n"
      "v_smfmac_f32_16x16x64_bf16 a[220:223], a[212:215], v[200:207], v247\n"
      "v_cvt_pk_bf16_f32 v200, v176, v177\n"
      "v_mfma_f32_16x16x32_bf16 v[160:163], a[0:3], a[144:147], v[212:215]\n"
      "v_exp_f32 v180, v180\n"
      "v_mfma_f32_16x16x32_bf16 v[160:163], a[4:7], a[148:151], v[160:163]\n"
      "ds_read_b64_tr_b16 a[78:79], v233 offset:53504\n"
      "v_cvt_pk_bf16_f32 v201, v178, v179\n"
      "v_mfma_f32_16x16x32_bf16 v[160:163], a[8:11], a[152:155], v[160:163]\n"
      "v_exp_f32 v181, v181\n"
      "v_mfma_f32_16x16x32_bf16 v[160:163], a[12:15], a[156:159], v[160:163]\n"
      "v_exp_f32 v182, v182\n"
      "v_mfma_f32_16x16x32_bf16 v[164:167], a[28:31], a[156:159], v[212:215]\n"
      "s_add_u32 m0, s23, 84992\n"
      "v_cvt_pk_bf16_f32 v202, v180, v181\n"
      "v_mfma_f32_16x16x32_bf16 v[164:167], a[24:27], a[152:155], v[164:167]\n"
      "global_load_lds_dwordx4 v237, s[28:29] offset:1024 nt\n"
      "v_mfma_f32_16x16x32_bf16 v[164:167], a[20:23], a[148:151], v[164:167]\n"
      "v_exp_f32 v183, v183\n"
      "v_mfma_f32_16x16x32_bf16 v[164:167], a[16:19], a[144:147], v[164:167]\n"
      "ds_read_b64_tr_b16 a[80:81], v232 offset:49664\n"
      "v_cvt_pk_bf16_f32 v203, v182, v183\n"
      "v_mfma_f32_16x16x32_bf16 v[168:171], a[32:35], a[144:147], v[212:215]\n"
      "v_exp_f32 v184, v184\n"
      "v_mfma_f32_16x16x32_bf16 v[168:171], a[36:39], a[148:151], v[168:171]\n"
      "ds_read_b64_tr_b16 a[82:83], v232 offset:53760\n"
      "ds_read_b64_tr_b16 a[84:85], v233 offset:49664\n"
      "v_mfma_f32_16x16x32_bf16 v[168:171], a[40:43], a[152:155], v[168:171]\n"
      "v_exp_f32 v185, v185\n"
      "v_mfma_f32_16x16x32_bf16 v[168:171], a[44:47], a[156:159], v[168:171]\n"
      "ds_read_b64_tr_b16 a[86:87], v233 offset:53760\n"
      "v_cvt_pk_bf16_f32 v204, v184, v185\n"
      "v_mfma_f32_16x16x32_bf16 v[172:175], a[60:63], a[156:159], v[212:215]\n"
      "v_exp_f32 v186, v186\n"
      "v_mfma_f32_16x16x32_bf16 v[172:175], a[56:59], a[152:155], v[172:175]\n"
      "ds_read_b64_tr_b16 a[88:89], v232 offset:49920\n"
      "ds_read_b64_tr_b16 a[90:91], v232 offset:54016\n"
      "v_mfma_f32_16x16x32_bf16 v[172:175], a[52:55], a[148:151], v[172:175]\n"
      "v_exp_f32 v187, v187\n"
      "v_mfma_f32_16x16x32_bf16 v[172:175], a[48:51], a[144:147], v[172:175]\n"
      "ds_read_b64_tr_b16 a[92:93], v233 offset:49920\n"
      "v_cvt_pk_bf16_f32 v205, v186, v187\n"
      "v_mfma_f32_16x16x32_bf16 v[128:131], v[192:195], a[96:99], v[128:131]\n"
      "v_exp_f32 v188, v188\n"
      "v_mfma_f32_16x16x32_bf16 v[132:135], v[192:195], a[100:103], v[132:135]\n"
      "ds_read_b64_tr_b16 a[96:97], v234 offset:49152\n"
      "ds_read_b64_tr_b16 a[98:99], v234 offset:53248\n"
      "v_mfma_f32_16x16x32_bf16 v[136:139], v[192:195], a[104:107], v[136:139]\n"
      "v_exp_f32 v189, v189\n"
      "v_mfma_f32_16x16x32_bf16 v[140:143], v[192:195], a[108:111], v[140:143]\n"
      "ds_read_b64_tr_b16 a[100:101], v235 offset:49152\n"
      "v_cvt_pk_bf16_f32 v206, v188, v189\n"
      "v_mfma_f32_16x16x32_bf16 v[144:147], v[192:195], a[112:115], v[144:147]\n"
      "v_exp_f32 v190, v190\n"
      "v_mfma_f32_16x16x32_bf16 v[148:151], v[192:195], a[116:119], v[148:151]\n"
      "ds_read_b64_tr_b16 a[102:103], v235 offset:53248\n"
      "ds_read_b64_tr_b16 a[94:95], v233 offset:54016\n"
      "v_mfma_f32_16x16x32_bf16 v[152:155], v[192:195], a[120:123], v[152:155]\n"
      "v_exp_f32 v191, v191\n"
      "v_mfma_f32_16x16x32_bf16 v[156:159], v[192:195], a[124:127], v[156:159]\n"
      "s_add_u32 m0, s23, 88064\n"
      "v_cvt_pk_bf16_f32 v207, v190, v191\n"
      "v_mfma_f32_16x16x32_bf16 v[176:179], a[0:3], a[160:163], v[216:219]\n"
      "global_load_lds_dwordx4 v237, s[28:29] offset:2048 nt\n"
      "v_mfma_f32_16x16x32_bf16 v[176:179], a[4:7], a[164:167], v[176:179]\n"
      "v_exp_f32 v160, v160\n"
      "v_mfma_f32_16x16x32_bf16 v[176:179], a[8:11], a[168:171], v[176:179]\n"
      "ds_read_b64_tr_b16 a[104:105], v234 offset:49408\n"
      "ds_read_b64_tr_b16 a[106:107], v234 offset:53504\n"
      "v_mfma_f32_16x16x32_bf16 v[176:179], a[12:15], a[172:175], v[176:179]\n"
      "v_exp_f32 v161, v161\n"
      "v_mfma_f32_16x16x32_bf16 v[180:183], a[28:31], a[172:175], v[216:219]\n"
      "ds_read_b64_tr_b16 a[108:109], v235 offset:49408\n"
      "ds_read_b64_tr_b16 a[110:111], v235 offset:53504\n"
      "v_mfma_f32_16x16x32_bf16 v[180:183], a[24:27], a[168:171], v[180:183]\n"
      "v_exp_f32 v162, v162\n"
      "v_mfma_f32_16x16x32_bf16 v[180:183], a[20:23], a[164:167], v[180:183]\n"
      "ds_read_b64_tr_b16 a[112:113], v234 offset:49664\n"
      "v_cvt_pk_bf16_f32 v192, v160, v161\n"
      "v_mfma_f32_16x16x32_bf16 v[180:183], a[16:19], a[160:163], v[180:183]\n"
      "v_exp_f32 v163, v163\n"
      "v_mfma_f32_16x16x32_bf16 v[184:187], a[32:35], a[160:163], v[216:219]\n"
      "ds_read_b64_tr_b16 a[114:115], v234 offset:53760\n"
      "v_cvt_pk_bf16_f32 v193, v162, v163\n"
      "v_mfma_f32_16x16x32_bf16 v[184:187], a[36:39], a[164:167], v[184:187]\n"
      "v_exp_f32 v164, v164\n"
      "v_mfma_f32_16x16x32_bf16 v[184:187], a[40:43], a[168:171], v[184:187]\n"
      "ds_read_b64_tr_b16 a[116:117], v235 offset:49664\n"
      "v_mfma_f32_16x16x32_bf16 v[184:187], a[44:47], a[172:175], v[184:187]\n"
      "v_exp_f32 v165, v165\n"
      "v_mfma_f32_16x16x32_bf16 v[188:191], a[60:63], a[172:175], v[216:219]\n"
      "ds_read_b64_tr_b16 a[118:119], v235 offset:53760\n"
      "v_cvt_pk_bf16_f32 v194, v164, v165\n"
      "v_mfma_f32_16x16x32_bf16 v[188:191], a[56:59], a[168:171], v[188:191]\n"
      "v_exp_f32 v166, v166\n"
      "v_mfma_f32_16x16x32_bf16 v[188:191], a[52:55], a[164:167], v[188:191]\n"
      "ds_read_b64_tr_b16 a[120:121], v234 offset:49920\n"
      "ds_read_b64_tr_b16 a[122:123], v234 offset:54016\n"
      "v_mfma_f32_16x16x32_bf16 v[188:191], a[48:51], a[160:163], v[188:191]\n"
      "s_waitcnt lgkmcnt(4)\n"
      "v_exp_f32 v167, v167\n"
      "v_mfma_f32_16x16x32_bf16 v[0:3], v[200:203], a[64:67], v[0:3]\n"
      "v_exp_f32 v168, v168\n"
      "v_mfma_f32_16x16x32_bf16 v[0:3], v[204:207], a[96:99], v[0:3]\n"
      "s_add_u32 m0, s23, 91136\n"
      "ds_read_b64_tr_b16 a[124:125], v235 offset:49920\n"
      "v_mfma_f32_16x16x32_bf16 v[4:7], v[200:203], a[68:71], v[4:7]\n"
      "global_load_lds_dwordx4 v237, s[28:29] offset:3072 nt\n"
      "v_mfma_f32_16x16x32_bf16 v[4:7], v[204:207], a[100:103], v[4:7]\n"
      "v_exp_f32 v169, v169\n"
      "v_mfma_f32_16x16x32_bf16 v[8:11], v[200:203], a[72:75], v[8:11]\n"
      "ds_read_b64_tr_b16 a[126:127], v235 offset:54016\n"
      "v_cvt_pk_bf16_f32 v195, v166, v167\n"
      "v_mfma_f32_16x16x32_bf16 v[8:11], v[204:207], a[104:107], v[8:11]\n"
      "v_exp_f32 v170, v170\n"
      "v_mfma_f32_16x16x32_bf16 v[12:15], v[200:203], a[76:79], v[12:15]\n"
      "v_cvt_pk_bf16_f32 v196, v168, v169\n"
      "v_add_u32 v237, 0x1000, v237\n"
      "v_mfma_f32_16x16x32_bf16 v[12:15], v[204:207], a[108:111], v[12:15]\n"
      "v_exp_f32 v171, v171\n"
      "v_mfma_f32_16x16x32_bf16 v[16:19], v[200:203], a[80:83], v[16:19]\n"
      "v_cvt_pk_bf16_f32 v197, v170, v171\n"
      "v_mfma_f32_16x16x32_bf16 v[16:19], v[204:207], a[112:115], v[16:19]\n"
      "v_exp_f32 v172, v172\n"
      "v_mfma_f32_16x16x32_bf16 v[20:23], v[200:203], a[84:87], v[20:23]\n"
      "s_waitcnt lgkmcnt(0)\n"
      "v_mfma_f32_16x16x32_bf16 v[20:23], v[204:207], a[116:119], v[20:23]\n"
      "v_exp_f32 v173, v173\n"
      "v_mfma_f32_16x16x32_bf16 v[24:27], v[200:203], a[88:91], v[24:27]\n"
      "v_cvt_pk_bf16_f32 v198, v172, v173\n"
      "v_mfma_f32_16x16x32_bf16 v[24:27], v[204:207], a[120:123], v[24:27]\n"
      "v_exp_f32 v174, v174\n"
      "v_mfma_f32_16x16x32_bf16 v[28:31], v[200:203], a[92:95], v[28:31]\n"
      "v_mfma_f32_16x16x32_bf16 v[28:31], v[204:207], a[124:127], v[28:31]\n"
      "v_exp_f32 v175, v175\n"
      "v_smfmac_f32_16x16x64_bf16 a[216:219], a[208:211], v[200:207], v247\n"
      "s_waitcnt vmcnt(9) lgkmcnt(0)\n"
      "v_cvt_pk_bf16_f32 v199, v174, v175\n"
      "v_mfma_f32_16x16x32_bf16 v[160:163], a[0:3], a[176:179], v[220:223]\n"
      "s_barrier\n"
      "v_mfma_f32_16x16x32_bf16 v[160:163], a[4:7], a[180:183], v[160:163]\n"
      "v_exp_f32 v176, v176\n"
      "v_mfma_f32_16x16x32_bf16 v[160:163], a[8:11], a[184:187], v[160:163]\n"
      "ds_read_b128 a[0:3], v228 offset:0\n"
      "v_mfma_f32_16x16x32_bf16 v[160:163], a[12:15], a[188:191], v[160:163]\n"
      "v_exp_f32 v177, v177\n"
      "v_mfma_f32_16x16x32_bf16 v[164:167], a[28:31], a[188:191], v[220:223]\n"
      "v_exp_f32 v178, v178\n"
      "v_mfma_f32_16x16x32_bf16 v[164:167], a[24:27], a[184:187], v[164:167]\n"
      "s_add_u32 m0, s23, 32768\n"
      "v_cvt_pk_bf16_f32 v200, v176, v177\n"
      "v_mfma_f32_16x16x32_bf16 v[164:167], a[20:23], a[180:183], v[164:167]\n"
      "global_load_lds_dwordx4 v236, s[24:25] offset:0 nt\n"
      "v_mfma_f32_16x16x32_bf16 v[164:167], a[16:19], a[176:179], v[164:167]\n"
      "v_exp_f32 v179, v179\n"
      "v_mfma_f32_16x16x32_bf16 v[168:171], a[32:35], a[176:179], v[220:223]\n"
      "ds_read_b128 a[4:7], v229 offset:256\n"
      "v_cvt_pk_bf16_f32 v201, v178, v179\n"
      "v_mfma_f32_16x16x32_bf16 v[168:171], a[36:39], a[180:183], v[168:171]\n"
      "v_exp_f32 v180, v180\n"
      "v_mfma_f32_16x16x32_bf16 v[168:171], a[40:43], a[184:187], v[168:171]\n"
      "ds_read_b128 a[8:11], v228 offset:512\n"
      "v_mfma_f32_16x16x32_bf16 v[168:171], a[44:47], a[188:191], v[168:171]\n"
      "v_exp_f32 v181, v181\n"
      "v_mfma_f32_16x16x32_bf16 v[172:175], a[60:63], a[188:191], v[220:223]\n"
      "v_cvt_pk_bf16_f32 v202, v180, v181\n"
      "v_mfma_f32_16x16x32_bf16 v[172:175], a[56:59], a[184:187], v[172:175]\n"
      "v_exp_f32 v182, v182\n"
      "v_mfma_f32_16x16x32_bf16 v[172:175], a[52:55], a[180:183], v[172:175]\n"
      "ds_read_b128 a[12:15], v229 offset:768\n"
      "v_mfma_f32_16x16x32_bf16 v[172:175], a[48:51], a[176:179], v[172:175]\n"
      "v_exp_f32 v183, v183\n"
      "v_mfma_f32_16x16x32_bf16 v[32:35], v[192:195], a[64:67], v[32:35]\n"
      "v_exp_f32 v184, v184\n"
      "v_mfma_f32_16x16x32_bf16 v[32:35], v[196:199], a[96:99], v[32:35]\n"
      "s_add_u32 m0, s23, 35840\n"
      "v_cvt_pk_bf16_f32 v203, v182, v183\n"
      "v_mfma_f32_16x16x32_bf16 v[36:39], v[192:195], a[68:71], v[36:39]\n"
      "global_load_lds_dwordx4 v236, s[24:25] offset:1024 nt\n"
      "v_mfma_f32_16x16x32_bf16 v[36:39], v[196:199], a[100:103], v[36:39]\n"
      "v_exp_f32 v185, v185\n"
      "v_mfma_f32_16x16x32_bf16 v[40:43], v[192:195], a[72:75], v[40:43]\n"
      "ds_read_b128 a[28:31], v229 offset:4864\n"
      "v_cvt_pk_bf16_f32 v204, v184, v185\n"
      "v_mfma_f32_16x16x32_bf16 v[40:43], v[196:199], a[104:107], v[40:43]\n"
      "v_exp_f32 v186, v186\n"
      "v_mfma_f32_16x16x32_bf16 v[44:47], v[192:195], a[76:79], v[44:47]\n"
      "ds_read_b128 a[24:27], v228 offset:4608\n"
      "v_mfma_f32_16x16x32_bf16 v[44:47], v[196:199], a[108:111], v[44:47]\n"
      "v_exp_f32 v187, v187\n"
      "v_mfma_f32_16x16x32_bf16 v[48:51], v[192:195], a[80:83], v[48:51]\n"
      "v_cvt_pk_bf16_f32 v205, v186, v187\n"
      "v_mfma_f32_16x16x32_bf16 v[48:51], v[196:199], a[112:115], v[48:51]\n"
      "v_exp_f32 v188, v188\n"
      "v_mfma_f32_16x16x32_bf16 v[52:55], v[192:195], a[84:87], v[52:55]\n"
      "ds_read_b128 a[20:23], v229 offset:4352\n"
      "v_mfma_f32_16x16x32_bf16 v[52:55], v[196:199], a[116:119], v[52:55]\n"
      "v_exp_f32 v189, v189\n"
      "v_mfma_f32_16x16x32_bf16 v[56:59], v[192:195], a[88:91], v[56:59]\n"
      "v_exp_f32 v190, v190\n"
      "v_mfma_f32_16x16x32_bf16 v[56:59], v[196:199], a[120:123], v[56:59]\n"
      "s_add_u32 m0, s23, 38912\n"
      "v_cvt_pk_bf16_f32 v206, v188, v189\n"
      "v_mfma_f32_16x16x32_bf16 v[60:63], v[192:195], a[92:95], v[60:63]\n"
      "global_load_lds_dwordx4 v236, s[24:25] offset:2048 nt\n"
      "v_mfma_f32_16x16x32_bf16 v[60:63], v[196:199], a[124:127], v[60:63]\n"
      "v_exp_f32 v191, v191\n"
      "v_smfmac_f32_16x16x64_bf16 a[216:219], a[212:215], v[192:199], v247\n"
      "v_cvt_pk_bf16_f32 v207, v190, v191\n"
      "v_mfma_f32_16x16x32_bf16 v[176:179], a[32:35], a[192:195], v[224:227]\n"
      "v_exp_f32 v160, v160\n"
      "v_mfma_f32_16x16x32_bf16 v[176:179], a[36:39], a[196:199], v[176:179]\n"
      "ds_read_b128 a[16:19], v228 offset:4096\n"
      "ds_read_b128 a[32:35], v230 offset:0\n"
      "v_mfma_f32_16x16x32_bf16 v[176:179], a[40:43], a[200:203], v[176:179]\n"
      "v_exp_f32 v161, v161\n"
      "v_mfma_f32_16x16x32_bf16 v[176:179], a[44:47], a[204:207], v[176:179]\n"
      "ds_read_b128 a[36:39], v231 offset:256\n"
      "v_cvt_pk_bf16_f32 v192, v160, v161\n"
      "v_mfma_f32_16x16x32_bf16 v[180:183], a[60:63], a[204:207], v[224:227]\n"
      "v_exp_f32 v162, v162\n"
      "v_mfma_f32_16x16x32_bf16 v[180:183], a[56:59], a[200:203], v[180:183]\n"
      "v_mfma_f32_16x16x32_bf16 v[180:183], a[52:55], a[196:199], v[180:183]\n"
      "v_exp_f32 v163, v163\n"
      "v_mfma_f32_16x16x32_bf16 v[180:183], a[48:51], a[192:195], v[180:183]\n"
      "ds_read_b128 a[40:43], v230 offset:512\n"
      "v_cvt_pk_bf16_f32 v193, v162, v163\n"
      "v_mfma_f32_16x16x32_bf16 v[64:67], v[200:203], a[64:67], v[64:67]\n"
      "v_mfma_f32_16x16x32_bf16 v[64:67], v[204:207], a[96:99], v[64:67]\n"
      "v_exp_f32 v164, v164\n"
      "v_mfma_f32_16x16x32_bf16 v[68:71], v[200:203], a[68:71], v[68:71]\n"
      "v_exp_f32 v165, v165\n"
      "v_mfma_f32_16x16x32_bf16 v[68:71], v[204:207], a[100:103], v[68:71]\n"
      "s_add_u32 m0, s23, 41984\n"
      "v_cvt_pk_bf16_f32 v194, v164, v165\n"
      "v_mfma_f32_16x16x32_bf16 v[72:75], v[200:203], a[72:75], v[72:75]\n"
      "global_load_lds_dwordx4 v236, s[24:25] offset:3072 nt\n"
      "v_mfma_f32_16x16x32_bf16 v[72:75], v[204:207], a[104:107], v[72:75]\n"
      "v_exp_f32 v166, v166\n"
      "v_mfma_f32_16x16x32_bf16 v[76:79], v[200:203], a[76:79], v[76:79]\n"
      "ds_read_b128 a[44:47], v231 offset:768\n"
      "ds_read_b128 a[60:63], v231 offset:4864\n"
      "v_mfma_f32_16x16x32_bf16 v[76:79], v[204:207], a[108:111], v[76:79]\n"
      "v_exp_f32 v167, v167\n"
      "v_mfma_f32_16x16x32_bf16 v[80:83], v[200:203], a[80:83], v[80:83]\n"
      "ds_read_b128 a[56:59], v230 offset:4608\n"
      "v_cvt_pk_bf16_f32 v195, v166, v167\n"
      "v_mfma_f32_16x16x32_bf16 v[80:83], v[204:207], a[112:115], v[80:83]\n"
      "v_exp_f32 v168, v168\n"
      "v_mfma_f32_16x16x32_bf16 v[84:87], v[200:203], a[84:87], v[84:87]\n"
      "ds_read_b128 a[52:55], v231 offset:4352\n"
      "v_add_u32 v236, 0x1000, v236\n"
      "v_mfma_f32_16x16x32_bf16 v[84:87], v[204:207], a[116:119], v[84:87]\n"
      "v_exp_f32 v169, v169\n"
      "v_mfma_f32_16x16x32_bf16 v[88:91], v[200:203], a[88:91], v[88:91]\n"
      "v_cvt_pk_bf16_f32 v196, v168, v169\n"
      "v_mfma_f32_16x16x32_bf16 v[88:91], v[204:207], a[120:123], v[88:91]\n"
      "v_exp_f32 v170, v170\n"
      "v_mfma_f32_16x16x32_bf16 v[92:95], v[200:203], a[92:95], v[92:95]\n"
      "ds_read_b128 a[48:51], v230 offset:4096\n"
      "v_mfma_f32_16x16x32_bf16 v[92:95], v[204:207], a[124:127], v[92:95]\n"
      "v_exp_f32 v171, v171\n"
      "v_smfmac_f32_16x16x64_bf16 a[220:223], a[208:211], v[200:207], v247\n"
      "s_waitcnt lgkmcnt(1)\n"
      "v_cvt_pk_bf16_f32 v197, v170, v171\n"
      "v_mfma_f32_16x16x32_bf16 v[160:163], a[0:3], a[128:131], v[208:211]\n"
      "v_exp_f32 v172, v172\n"
      "v_mfma_f32_16x16x32_bf16 v[160:163], a[4:7], a[132:135], v[160:163]\n"
      "v_mfma_f32_16x16x32_bf16 v[160:163], a[8:11], a[136:139], v[160:163]\n"
      "v_exp_f32 v173, v173\n"
      "v_mfma_f32_16x16x32_bf16 v[160:163], a[12:15], a[140:143], v[160:163]\n"
      "v_cvt_pk_bf16_f32 v198, v172, v173\n"
      "v_mfma_f32_16x16x32_bf16 v[164:167], a[28:31], a[140:143], v[208:211]\n"
      "v_exp_f32 v174, v174\n"
      "v_mfma_f32_16x16x32_bf16 v[164:167], a[24:27], a[136:139], v[164:167]\n"
      "v_mfma_f32_16x16x32_bf16 v[164:167], a[20:23], a[132:135], v[164:167]\n"
      "v_exp_f32 v175, v175\n"
      "v_mfma_f32_16x16x32_bf16 v[164:167], a[16:19], a[128:131], v[164:167]\n"
      "s_waitcnt lgkmcnt(0)\n"
      "v_cvt_pk_bf16_f32 v199, v174, v175\n"
      "v_mfma_f32_16x16x32_bf16 v[168:171], a[32:35], a[128:131], v[208:211]\n"
      "s_memrealtime s[76:77]\n"
      "v_exp_f32 v176, v176\n"
      "v_mfma_f32_16x16x32_bf16 v[168:171], a[36:39], a[132:135], v[168:171]\n"
      "v_add_f32 v240, v240, v176\n"
      "v_mfma_f32_16x16x32_bf16 v[168:171], a[40:43], a[136:139], v[168:171]\n"
      "v_exp_f32 v177, v177\n"
      "v_mfma_f32_16x16x32_bf16 v[168:171], a[44:47], a[140:143], v[168:171]\n"
      "v_exp_f32 v178, v178\n"
      "v_mfma_f32_16x16x32_bf16 v[172:175], a[60:63], a[140:143], v[208:211]\n"
      "s_add_u32 m0, s23, 98304\n"
      "v_add_f32 v240, v240, v177\n"
      "v_mfma_f32_16x16x32_bf16 v[172:175], a[56:59], a[136:139], v[172:175]\n"
      "global_load_lds_dwordx4 v237, s[28:29] offset:0 nt\n"
      "v_mfma_f32_16x16x32_bf16 v[172:175], a[52:55], a[132:135], v[172:175]\n"
      "v_exp_f32 v179, v179\n"
      "v_mfma_f32_16x16x32_bf16 v[172:175], a[48:51], a[128:131], v[172:175]\n"
      "v_cvt_pk_bf16_f32 v200, v176, v177\n"
      "v_add_f32 v240, v240, v178\n"
      "v_mfma_f32_16x16x32_bf16 v[96:99], v[192:195], a[64:67], v[96:99]\n"
      "s_waitcnt lgkmcnt(0)\n"
      "v_exp_f32 v180, v180\n"
      "v_mfma_f32_16x16x32_bf16 v[96:99], v[196:199], a[96:99], v[96:99]\n"
      "s_sub_u32 s79, s76, s78\n"
      "v_add_f32 v240, v240, v180\n"
      "v_add_f32 v240, v240, v179\n"
      "v_mfma_f32_16x16x32_bf16 v[100:103], v[192:195], a[68:71], v[100:103]\n"
      "s_cbranch_scc1 SPINp3_%=\n"
      "BACKp3_%=:\n"
      "v_exp_f32 v181, v181\n"
      "v_mfma_f32_16x16x32_bf16 v[100:103], v[196:199], a[100:103], v[100:103]\n"
      "s_sub_u32 s76, s76, 4\n"
      "v_add_f32 v240, v240, v181\n"
      "v_cvt_pk_bf16_f32 v202, v180, v181\n"
      "v_mfma_f32_16x16x32_bf16 v[104:107], v[192:195], a[72:75], v[104:107]\n"
      "s_max_u32 s78, s78, s76\n"
      "v_exp_f32 v182, v182\n"
      "v_mfma_f32_16x16x32_bf16 v[104:107], v[196:199], a[104:107], v[104:107]\n"
      "s_sub_u32 s81, s81, 3\n"
      "ds_read_b64_tr_b16 a[64:65], v232 offset:0\n"
      "v_add_f32 v240, v240, v182\n"
      "v_mfma_f32_16x16x32_bf16 v[108:111], v[192:195], a[76:79], v[108:111]\n"
      "s_max_u32 s81, s81, 140\n"
      "v_exp_f32 v183, v183\n"
      "v_mfma_f32_16x16x32_bf16 v[108:111], v[196:199], a[108:111], v[108:111]\n"
      "s_sub_u32 s79, s76, s83\n"
      "v_add_f32 v240, v240, v183\n"
      "v_cvt_pk_bf16_f32 v203, v182, v183\n"
      "v_mfma_f32_16x16x32_bf16 v[112:115], v[192:195], a[80:83], v[112:115]\n"
      "s_cmp_gt_u32 s79, 340\n"
      "ds_read_b64_tr_b16 a[66:67], v232 offset:4096\n"
      "ds_read_b64_tr_b16 a[68:69], v233 offset:0\n"
      "v_mfma_f32_16x16x32_bf16 v[112:115], v[196:199], a[112:115], v[112:115]\n"
      "s_cselect_b32 s81, 162, s81\n"
      "v_exp_f32 v160, v160\n"
      "v_mfma_f32_16x16x32_bf16 v[116:119], v[192:195], a[84:87], v[116:119]\n"
      "s_mov_b32 s83, s82\n"
      "ds_read_b64_tr_b16 a[70:71], v233 offset:4096\n"
      "ds_read_b64_tr_b16 a[72:73], v232 offset:256\n"
      "v_mfma_f32_16x16x32_bf16 v[116:119], v[196:199], a[116:119], v[116:119]\n"
      "s_mov_b32 s82, s76\n"
      "v_exp_f32 v161, v161\n"
      "v_mfma_f32_16x16x32_bf16 v[120:123], v[192:195], a[88:91], v[120:123]\n"
      "s_add_u32 s78, s78, s81\n"
      "ds_read_b64_tr_b16 a[74:75], v232 offset:4352\n"
      "v_cvt_pk_bf16_f32 v201, v178, v179\n"
      "v_mfma_f32_16x16x32_bf16 v[120:123], v[196:199], a[120:123], v[120:123]\n"
      "v_exp_f32 v162, v162\n"
      "v_mfma_f32_16x16x32_bf16 v[124:127], v[192:195], a[92:95], v[124:127]\n"
      "ds_read_b64_tr_b16 a[76:77], v233 offset:256\n"
      "v_mfma_f32_16x16x32_bf16 v[124:127], v[196:199], a[124:127], v[124:127]\n"
      "v_exp_f32 v163, v163\n"
      "v_smfmac_f32_16x16x64_bf16 a[220:223], a[212:215], v[192:199], v247\n"
      "v_cvt_pk_bf16_f32 v192, v160, v161\n"
      "v_mfma_f32_16x16x32_bf16 v[176:179], a[0:3], a[144:147], v[212:215]\n"
      "v_exp_f32 v164, v164\n"
      "v_mfma_f32_16x16x32_bf16 v[176:179], a[4:7], a[148:151], v[176:179]\n"
      "ds_read_b64_tr_b16 a[78:79], v233 offset:4352\n"
      "v_cvt_pk_bf16_f32 v193, v162, v163\n"
      "v_mfma_f32_16x16x32_bf16 v[176:179], a[8:11], a[152:155], v[176:179]\n"
      "v_exp_f32 v165, v165\n"
      "v_mfma_f32_16x16x32_bf16 v[176:179], a[12:15], a[156:159], v[176:179]\n"
      "v_exp_f32 v166, v166\n"
      "v_mfma_f32_16x16x32_bf16 v[180:183], a[28:31], a[156:159], v[212:215]\n"
      "s_add_u32 m0, s23, 101376\n"
      "v_cvt_pk_bf16_f32 v194, v164, v165\n"
      "v_mfma_f32_16x16x32_bf16 v[180:183], a[24:27], a[152:155], v[180:183]\n"
      "global_load_lds_dwordx4 v237, s[28:29] offset:1024 nt\n"
      "v_mfma_f32_16x16x32_bf16 v[180:183], a[20:23], a[148:151], v[180:183]\n"
      "v_exp_f32 v167, v167\n"
      "v_mfma_f32_16x16x32_bf16 v[180:183], a[16:19], a[144:147], v[180:183]\n"
      "ds_read_b64_tr_b16 a[80:81], v232 offset:512\n"
      "v_cvt_pk_bf16_f32 v195, v166, v167\n"
      "v_mfma_f32_16x16x32_bf16 v[184:187], a[32:35], a[144:147], v[212:215]\n"
      "v_exp_f32 v168, v168\n"
      "v_mfma_f32_16x16x32_bf16 v[184:187], a[36:39], a[148:151], v[184:187]\n"
      "ds_read_b64_tr_b16 a[82:83], v232 offset:4608\n"
      "ds_read_b64_tr_b16 a[84:85], v233 offset:512\n"
      "v_mfma_f32_16x16x32_bf16 v[184:187], a[40:43], a[152:155], v[184:187]\n"
      "v_exp_f32 v169, v169\n"
      "v_mfma_f32_16x16x32_bf16 v[184:187], a[44:47], a[156:159], v[184:187]\n"
      "ds_read_b64_tr_b16 a[86:87], v233 offset:4608\n"
      "v_cvt_pk_bf16_f32 v196, v168, v169\n"
      "v_mfma_f32_16x16x32_bf16 v[188:191], a[60:63], a[156:159], v[212:215]\n"
      "v_exp_f32 v170, v170\n"
      "v_mfma_f32_16x16x32_bf16 v[188:191], a[56:59], a[152:155], v[188:191]\n"
      "ds_read_b64_tr_b16 a[88:89], v232 offset:768\n"
      "ds_read_b64_tr_b16 a[90:91], v232 offset:4864\n"
      "v_mfma_f32_16x16x32_bf16 v[188:191], a[52:55], a[148:151], v[188:191]\n"
      "v_exp_f32 v171, v171\n"
      "v_mfma_f32_16x16x32_bf16 v[188:191], a[48:51], a[144:147], v[188:191]\n"
      "ds_read_b64_tr_b16 a[92:93], v233 offset:768\n"
      "v_cvt_pk_bf16_f32 v197, v170, v171\n"
      "v_mfma_f32_16x16x32_bf16 v[128:131], v[200:203], a[96:99], v[128:131]\n"
      "v_exp_f32 v172, v172\n"
      "v_mfma_f32_16x16x32_bf16 v[132:135], v[200:203], a[100:103], v[132:135]\n"
      "ds_read_b64_tr_b16 a[96:97], v234 offset:0\n"
      "ds_read_b64_tr_b16 a[98:99], v234 offset:4096\n"
      "v_mfma_f32_16x16x32_bf16 v[136:139], v[200:203], a[104:107], v[136:139]\n"
      "v_exp_f32 v173, v173\n"
      "v_mfma_f32_16x16x32_bf16 v[140:143], v[200:203], a[108:111], v[140:143]\n"
      "ds_read_b64_tr_b16 a[100:101], v235 offset:0\n"
      "v_cvt_pk_bf16_f32 v198, v172, v173\n"
      "v_mfma_f32_16x16x32_bf16 v[144:147], v[200:203], a[112:115], v[144:147]\n"
      "v_exp_f32 v174, v174\n"
      "v_mfma_f32_16x16x32_bf16 v[148:151], v[200:203], a[116:119], v[148:151]\n"
      "ds_read_b64_tr_b16 a[102:103], v235 offset:4096\n"
      "ds_read_b64_tr_b16 a[94:95], v233 offset:4864\n"
      "v_mfma_f32_16x16x32_bf16 v[152:155], v[200:203], a[120:123], v[152:155]\n"
      "v_exp_f32 v175, v175\n"
      "v_mfma_f32_16x16x32_bf16 v[156:159], v[200:203], a[124:127], v[156:159]\n"
      "s_add_u32 m0, s23, 104448\n"
      "v_cvt_pk_bf16_f32 v199, v174, v175\n"
      "v_mfma_f32_16x16x32_bf16 v[160:163], a[0:3], a[160:163], v[216:219]\n"
      "global_load_lds_dwordx4 v237, s[28:29] offset:2048 nt\n"
      "v_mfma_f32_16x16x32_bf16 v[160:163], a[4:7], a[164:167], v[160:163]\n"
      "v_exp_f32 v176, v176\n"
      "v_mfma_f32_16x16x32_bf16 v[160:163], a[8:11], a[168:171], v[160:163]\n"
      "ds_read_b64_tr_b16 a[104:105], v234 offset:256\n"
      "ds_read_b64_tr_b16 a[106:107], v234 offset:4352\n"
      "v_mfma_f32_16x16x32_bf16 v[160:163], a[12:15], a[172:175], v[160:163]\n"
      "v_exp_f32 v177, v177\n"
      "v_mfma_f32_16x16x32_bf16 v[164:167], a[28:31], a[172:175], v[216:219]\n"
      "ds_read_b64_tr_b16 a[108:109], v235 offset:256\n"
      "ds_read_b64_tr_b16 a[110:111], v235 offset:4352\n"
      "v_mfma_f32_16x16x32_bf16 v[164:167], a[24:27], a[168:171], v[164:167]\n"
      "v_exp_f32 v178, v178\n"
      "v_mfma_f32_16x16x32_bf16 v[164:167], a[20:23], a[164:167], v[164:167]\n"
      "ds_read_b64_tr_b16 a[112:113], v234 offset:512\n"
      "v_cvt_pk_bf16_f32 v200, v176, v177\n"
      "v_mfma_f32_16x16x32_bf16 v[164:167], a[16:19], a[160:163], v[164:167]\n"
      "v_exp_f32 v179, v179\n"
      "v_mfma_f32_16x16x32_bf16 v[168:171], a[32:35], a[160:163], v[216:219]\n"
      "ds_read_b64_tr_b16 a[114:115], v234 offset:4608\n"
      "v_cvt_pk_bf16_f32 v201, v178, v179\n"
      "v_mfma_f32_16x16x32_bf16 v[168:171], a[36:39], a[164:167], v[168:171]\n"
      "v_exp_f32 v180, v180\n"
      "v_mfma_f32_16x16x32_bf16 v[168:171], a[40:43], a[168:171], v[168:171]\n"
      "ds_read_b64_tr_b16 a[116:117], v235 offset:512\n"
      "v_mfma_f32_16x16x32_bf16 v[168:171], a[44:47], a[172:175], v[168:171]\n"
      "v_exp_f32 v181, v181\n"
      "v_mfma_f32_16x16x32_bf16 v[172:175], a[60:63], a[172:175], v[216:219]\n"
      "ds_read_b64_tr_b16 a[118:119], v235 offset:4608\n"
      "v_cvt_pk_bf16_f32 v202, v180, v181\n"
      "v_mfma_f32_16x16x32_bf16 v[172:175], a[56:59], a[168:171], v[172:175]\n"
      "v_exp_f32 v182, v182\n"
      "v_mfma_f32_16x16x32_bf16 v[172:175], a[52:55], a[164:167], v[172:175]\n"
      "ds_read_b64_tr_b16 a[120:121], v234 offset:768\n"
      "ds_read_b64_tr_b16 a[122:123], v234 offset:4864\n"
      "v_mfma_f32_16x16x32_bf16 v[172:175], a[48:51], a[160:163], v[172:175]\n"
      "s_waitcnt lgkmcnt(4)\n"
      "v_exp_f32 v183, v183\n"
      "v_mfma_f32_16x16x32_bf16 v[0:3], v[192:195], a[64:67], v[0:3]\n"
      "v_exp_f32 v184, v184\n"
      "v_mfma_f32_16x16x32_bf16 v[0:3], v[196:199], a[96:99], v[0:3]\n"
      "s_add_u32 m0, s23, 107520\n"
      "ds_read_b64_tr_b16 a[124:125], v235 offset:768\n"
      "v_mfma_f32_16x16x32_bf16 v[4:7], v[192:195], a[68:71], v[4:7]\n"
      "global_load_lds_dwordx4 v237, s[28:29] offset:3072 nt\n"
      "v_mfma_f32_16x16x32_bf16 v[4:7], v[196:199], a[100:103], v[4:7]\n"
      "v_exp_f32 v185, v185\n"
      "v_mfma_f32_16x16x32_bf16 v[8:11], v[192:195], a[72:75], v[8:11]\n"
      "ds_read_b64_tr_b16 a[126:127], v235 offset:4864\n"
      "v_cvt_pk_bf16_f32 v203, v182, v183\n"
      "v_mfma_f32_16x16x32_bf16 v[8:11], v[196:199], a[104:107], v[8:11]\n"
      "v_exp_f32 v186, v186\n"
      "v_mfma_f32_16x16x32_bf16 v[12:15], v[192:195], a[76:79], v[12:15]\n"
      "v_cvt_pk_bf16_f32 v204, v184, v185\n"
      "v_add_u32 v237, 0x1000, v237\n"
      "v_mfma_f32_16x16x32_bf16 v[12:15], v[196:199], a[108:111], v[12:15]\n"
      "v_exp_f32 v187, v187\n"
      "v_mfma_f32_16x16x32_bf16 v[16:19], v[192:195], a[80:83], v[16:19]\n"
      "v_cvt_pk_bf16_f32 v205, v186, v187\n"
      "v_mfma_f32_16x16x32_bf16 v[16:19], v[196:199], a[112:115], v[16:19]\n"
      "v_exp_f32 v188, v188\n"
      "v_mfma_f32_16x16x32_bf16 v[20:23], v[192:195], a[84:87], v[20:23]\n"
      "s_waitcnt lgkmcnt(0)\n"
      "v_mfma_f32_16x16x32_bf16 v[20:23], v[196:199], a[116:119], v[20:23]\n"
      "v_exp_f32 v189, v189\n"
      "v_mfma_f32_16x16x32_bf16 v[24:27], v[192:195], a[88:91], v[24:27]\n"
      "v_cvt_pk_bf16_f32 v206, v188, v189\n"
      "v_mfma_f32_16x16x32_bf16 v[24:27], v[196:199], a[120:123], v[24:27]\n"
      "v_exp_f32 v190, v190\n"
      "v_mfma_f32_16x16x32_bf16 v[28:31], v[192:195], a[92:95], v[28:31]\n"
      "v_mfma_f32_16x16x32_bf16 v[28:31], v[196:199], a[124:127], v[28:31]\n"
      "v_exp_f32 v191, v191\n"
      "v_smfmac_f32_16x16x64_bf16 a[216:219], a[208:211], v[192:199], v247\n"
      "s_waitcnt vmcnt(8) lgkmcnt(0)\n"
      "v_cvt_pk_bf16_f32 v207, v190, v191\n"
      "v_mfma_f32_16x16x32_bf16 v[176:179], a[0:3], a[176:179], v[220:223]\n"
      "s_barrier\n"
      "v_mfma_f32_16x16x32_bf16 v[176:179], a[4:7], a[180:183], v[176:179]\n"
      "v_exp_f32 v160, v160\n"
      "v_mfma_f32_16x16x32_bf16 v[176:179], a[8:11], a[184:187], v[176:179]\n"
      "ds_read_b128 a[0:3], v228 offset:16384\n"
      "v_mfma_f32_16x16x32_bf16 v[176:179], a[12:15], a[188:191], v[176:179]\n"
      "v_exp_f32 v161, v161\n"
      "v_mfma_f32_16x16x32_bf16 v[180:183], a[28:31], a[188:191], v[220:223]\n"
      "v_exp_f32 v162, v162\n"
      "v_mfma_f32_16x16x32_bf16 v[180:183], a[24:27], a[184:187], v[180:183]\n"
      "s_add_u32 m0, s23, 49152\n"
      "v_cvt_pk_bf16_f32 v192, v160, v161\n"
      "v_mfma_f32_16x16x32_bf16 v[180:183], a[20:23], a[180:183], v[180:183]\n"
      "global_load_lds_dwordx4 v236, s[24:25] offset:0 nt\n"
      "v_mfma_f32_16x16x32_bf16 v[180:183], a[16:19], a[176:179], v[180:183]\n"
      "v_exp_f32 v163, v163\n"
      "v_mfma_f32_16x16x32_bf16 v[184:187], a[32:35], a[176:179], v[220:223]\n"
      "ds_read_b128 a[4:7], v229 offset:16640\n"
      "v_cvt_pk_bf16_f32 v193, v162, v163\n"
      "v_mfma_f32_16x16x32_bf16 v[184:187], a[36:39], a[180:183], v[184:187]\n"
      "v_exp_f32 v164, v164\n"
      "v_mfma_f32_16x16x32_bf16 v[184:187], a[40:43], a[184:187], v[184:187]\n"
      "ds_read_b128 a[8:11], v228 offset:16896\n"
      "v_mfma_f32_16x16x32_bf16 v[184:187], a[44:47], a[188:191], v[184:187]\n"
      "v_exp_f32 v165, v165\n"
      "v_mfma_f32_16x16x32_bf16 v[188:191], a[60:63], a[188:191], v[220:223]\n"
      "v_cvt_pk_bf16_f32 v194, v164, v165\n"
      "v_mfma_f32_16x16x32_bf16 v[188:191], a[56:59], a[184:187], v[188:191]\n"
      "v_exp_f32 v166, v166\n"
      "v_mfma_f32_16x16x32_bf16 v[188:191], a[52:55], a[180:183], v[188:191]\n"
      "ds_read_b128 a[12:15], v229 offset:17152\n"
      "v_mfma_f32_16x16x32_bf16 v[188:191], a[48:51], a[176:179], v[188:191]\n"
      "v_exp_f32 v167, v167\n"
      "v_mfma_f32_16x16x32_bf16 v[32:35], v[200:203], a[64:67], v[32:35]\n"
      "v_exp_f32 v168, v168\n"
      "v_mfma_f32_16x16x32_bf16 v[32:35], v[204:207], a[96:99], v[32:35]\n"
      "s_add_u32 m0, s23, 52224\n"
      "v_cvt_pk_bf16_f32 v195, v166, v167\n"
      "v_mfma_f32_16x16x32_bf16 v[36:39], v[200:203], a[68:71], v[36:39]\n"
      "global_load_lds_dwordx4 v236, s[24:25] offset:1024 nt\n"
      "v_mfma_f32_16x16x32_bf16 v[36:39], v[204:207], a[100:103], v[36:39]\n"
      "v_exp_f32 v169, v169\n"
      "v_mfma_f32_16x16x32_bf16 v[40:43], v[200:203], a[72:75], v[40:43]\n"
      "ds_read_b128 a[28:31], v229 offset:21248\n"
      "v_cvt_pk_bf16_f32 v196, v168, v169\n"
      "v_mfma_f32_16x16x32_bf16 v[40:43], v[204:207], a[104:107], v[40:43]\n"
      "v_exp_f32 v170, v170\n"
      "v_mfma_f32_16x16x32_bf16 v[44:47], v[200:203], a[76:79], v[44:47]\n"
      "ds_read_b128 a[24:27], v228 offset:20992\n"
      "v_mfma_f32_16x16x32_bf16 v[44:47], v[204:207], a[108:111], v[44:47]\n"
      "v_exp_f32 v171, v171\n"
      "v_mfma_f32_16x16x32_bf16 v[48:51], v[200:203], a[80:83], v[48:51]\n"
      "v_cvt_pk_bf16_f32 v197, v170, v171\n"
      "v_mfma_f32_16x16x32_bf16 v[48:51], v[204:207], a[112:115], v[48:51]\n"
      "v_exp_f32 v172, v172\n"
      "v_mfma_f32_16x16x32_bf16 v[52:55], v[200:203], a[84:87], v[52:55]\n"
      "ds_read_b128 a[20:23], v229 offset:20736\n"
      "v_mfma_f32_16x16x32_bf16 v[52:55], v[204:207], a[116:119], v[52:55]\n"
      "v_exp_f32 v173, v173\n"
      "v_mfma_f32_16x16x32_bf16 v[56:59], v[200:203], a[88:91], v[56:59]\n"
      "v_exp_f32 v174, v174\n"
      "v_mfma_f32_16x16x32_bf16 v[56:59], v[204:207], a[120:123], v[56:59]\n"
      "s_add_u32 m0, s23, 55296\n"
      "v_cvt_pk_bf16_f32 v198, v172, v173\n"
      "v_mfma_f32_16x16x32_bf16 v[60:63], v[200:203], a[92:95], v[60:63]\n"
      "global_load_lds_dwordx4 v236, s[24:25] offset:2048 nt\n"
      "v_mfma_f32_16x16x32_bf16 v[60:63], v[204:207], a[124:127], v[60:63]\n"
      "v_exp_f32 v175, v175\n"
      "v_smfmac_f32_16x16x64_bf16 a[216:219], a[212:215], v[200:207], v247\n"
      "v_cvt_pk_bf16_f32 v199, v174, v175\n"
      "v_mfma_f32_16x16x32_bf16 v[160:163], a[32:35], a[192:195], v[224:227]\n"
      "v_exp_f32 v176, v176\n"
      "v_mfma_f32_16x16x32_bf16 v[160:163], a[36:39], a[196:199], v[160:163]\n"
      "ds_read_b128 a[16:19], v228 offset:20480\n"
      "ds_read_b128 a[32:35], v230 offset:16384\n"
      "v_mfma_f32_16x16x32_bf16 v[160:163], a[40:43], a[200:203], v[160:163]\n"
      "v_exp_f32 v177, v177\n"
      "v_mfma_f32_16x16x32_bf16 v[160:163], a[44:47], a[204:207], v[160:163]\n"
      "ds_read_b128 a[36:39], v231 offset:16640\n"
      "v_cvt_pk_bf16_f32 v200, v176, v177\n"
      "v_mfma_f32_16x16x32_bf16 v[164:167], a[60:63], a[204:207], v[224:227]\n"
      "v_exp_f32 v178, v178\n"
      "v_mfma_f32_16x16x32_bf16 v[164:167], a[56:59], a[200:203], v[164:167]\n"
      "v_mfma_f32_16x16x32_bf16 v[164:167], a[52:55], a[196:199], v[164:167]\n"
      "v_exp_f32 v179, v179\n"
      "v_mfma_f32_16x16x32_bf16 v[164:167], a[48:51], a[192:195], v[164:167]\n"
      "ds_read_b128 a[40:43], v230 offset:16896\n"
      "v_cvt_pk_bf16_f32 v201, v178, v179\n"
      "v_mfma_f32_16x16x32_bf16 v[64:67], v[192:195], a[64:67], v[64:67]\n"
      "v_mfma_f32_16x16x32_bf16 v[64:67], v[196:199], a[96:99], v[64:67]\n"
      "v_exp_f32 v180, v180\n"
      "v_mfma_f32_16x16x32_bf16 v[68:71], v[192:195], a[68:71], v[68:71]\n"
      "v_exp_f32 v181, v181\n"
      "v_mfma_f32_16x16x32_bf16 v[68:71], v[196:199], a[100:103], v[68:71]\n"
      "s_add_u32 m0, s23, 58368\n"
      "v_cvt_pk_bf16_f32 v202, v180, v181\n"
      "v_mfma_f32_16x16x32_bf16 v[72:75], v[192:195], a[72:75], v[72:75]\n"
      "global_load_lds_dwordx4 v236, s[24:25] offset:3072 nt\n"
      "v_mfma_f32_16x16x32_bf16 v[72:75], v[196:199], a[104:107], v[72:75]\n"
      "v_exp_f32 v182, v182\n"
      "v_mfma_f32_16x16x32_bf16 v[76:79], v[192:195], a[76:79], v[76:79]\n"
      "ds_read_b128 a[44:47], v231 offset:17152\n"
      "ds_read_b128 a[60:63], v231 offset:21248\n"
      "v_mfma_f32_16x16x32_bf16 v[76:79], v[196:199], a[108:111], v[76:79]\n"
      "v_exp_f32 v183, v183\n"
      "v_mfma_f32_16x16x32_bf16 v[80:83], v[192:195], a[80:83], v[80:83]\n"
      "ds_read_b128 a[56:59], v230 offset:20992\n"
      "v_cvt_pk_bf16_f32 v203, v182, v183\n"
      "v_mfma_f32_16x16x32_bf16 v[80:83], v[196:199], a[112:115], v[80:83]\n"
      "v_exp_f32 v184, v184\n"
      "v_mfma_f32_16x16x32_bf16 v[84:87], v[192:195], a[84:87], v[84:87]\n"
      "ds_read_b128 a[52:55], v231 offset:20736\n"
      "v_add_u32 v236, s56, v236\n"
      "v_mfma_f32_16x16x32_bf16 v[84:87], v[196:199], a[116:119], v[84:87]\n"
      "v_exp_f32 v185, v185\n"
      "v_mfma_f32_16x16x32_bf16 v[88:91], v[192:195], a[88:91], v[88:91]\n"
      "v_cvt_pk_bf16_f32 v204, v184, v185\n"
      "v_mfma_f32_16x16x32_bf16 v[88:91], v[196:199], a[120:123], v[88:91]\n"
      "v_exp_f32 v186, v186\n"
      "v_mfma_f32_16x16x32_bf16 v[92:95], v[192:195], a[92:95], v[92:95]\n"
      "s_add_u32 s58, s58, 1\n"
      "ds_read_b128 a[48:51], v230 offset:20480\n"
      "v_mfma_f32_16x16x32_bf16 v[92:95], v[196:199], a[124:127], v[92:95]\n"
      "s_cmp_lt_u32 s58, 7\n"
      "v_exp_f32 v187, v187\n"
      "v_smfmac_f32_16x16x64_bf16 a[220:223], a[208:211], v[192:199], v247\n"
      "s_waitcnt lgkmcnt(1)\n"
      "v_cvt_pk_bf16_f32 v205, v186, v187\n"
      "v_mfma_f32_16x16x32_bf16 v[176:179], a[0:3], a[128:131], v[208:211]\n"
      "v_exp_f32 v188, v188\n"
      "v_mfma_f32_16x16x32_bf16 v[176:179], a[4:7], a[132:135], v[176:179]\n"
      "s_cbranch_scc1 LOOP_%=\n"
      "v_mfma_f32_16x16x32_bf16 v[176:179], a[8:11], a[136:139], v[176:179]\n"
      "s_mul_i32 s74, s59, 18\n"
      "ds_read_b64_tr_b16 a[224:225], v245 offset:0\n"
      "v_exp_f32 v189, v189\n"
      "v_mfma_f32_16x16x32_bf16 v[176:179], a[12:15], a[140:143], v[176:179]\n"
      "s_lshl_b32 s73, s22, 2\n"
      "ds_read_b64_tr_b16 a[226:227], v245 offset:4096\n"
      "v_cvt_pk_bf16_f32 v206, v188, v189\n"
      "v_mfma_f32_16x16x32_bf16 v[180:183], a[28:31], a[140:143], v[208:211]\n"
      "s_add_u32 s73, s73, s74\n"
      "ds_read_b64_tr_b16 a[228:229], v246 offset:0\n"
      "v_exp_f32 v190, v190\n"
      "v_mfma_f32_16x16x32_bf16 v[180:183], a[24:27], a[136:139], v[180:183]\n"
      "s_lshl_b32 s73, s73, 5\n"
      "ds_read_b64_tr_b16 a[230:231], v246 offset:4096\n"
      "ds_read_b64_tr_b16 a[232:233], v245 offset:8192\n"
      "v_mfma_f32_16x16x32_bf16 v[180:183], a[20:23], a[132:135], v[180:183]\n"
      "s_add_u32 s73, s73, s60\n"
      "ds_read_b64_tr_b16 a[234:235], v245 offset:12288\n"
      "v_exp_f32 v191, v191\n"
      "v_mfma_f32_16x16x32_bf16 v[180:183], a[16:19], a[128:131], v[180:183]\n"
      "s_lshl_b32 s84, s73, 12\n"
      "ds_read_b64_tr_b16 a[236:237], v246 offset:8192\n"
      "v_cvt_pk_bf16_f32 v207, v190, v191\n"
      "v_mfma_f32_16x16x32_bf16 v[184:187], a[32:35], a[128:131], v[208:211]\n"
      "s_add_u32 s75, s94, 4\n"
      "v_exp_f32 v160, v160\n"
      "v_mfma_f32_16x16x32_bf16 v[184:187], a[36:39], a[132:135], v[184:187]\n"
      "s_and_b32 s75, s75, 3\n"
      "ds_read_b64_tr_b16 a[238:239], v246 offset:12288\n"
      "v_add_f32 v240, v240, v160\n"
      "v_mfma_f32_16x16x32_bf16 v[184:187], a[40:43], a[136:139], v[184:187]\n"
      "s_lshl_b32 s75, s75, 17\n"
      "v_exp_f32 v161, v161\n"
      "v_mfma_f32_16x16x32_bf16 v[184:187], a[44:47], a[140:143], v[184:187]\n"
      "s_add_u32 s87, s84, s75\n"
      "v_exp_f32 v162, v162\n"
      "v_mfma_f32_16x16x32_bf16 v[188:191], a[60:63], a[140:143], v[208:211]\n"
      "s_add_u32 m0, s23, 114688\n"
      "ds_read_b64_tr_b16 a[240:241], v245 offset:16384\n"
      "v_add_f32 v240, v240, v161\n"
      "v_mfma_f32_16x16x32_bf16 v[188:191], a[56:59], a[136:139], v[188:191]\n"
      "s_add_u32 s75, s94, 3\n"
      "global_load_lds_dwordx4 v237, s[28:29] offset:0 nt\n"
      "v_mfma_f32_16x16x32_bf16 v[188:191], a[52:55], a[132:135], v[188:191]\n"
      "s_waitcnt lgkmcnt(2)\n"
      "v_exp_f32 v163, v163\n"
      "v_mfma_f32_16x16x32_bf16 v[188:191], a[48:51], a[128:131], v[188:191]\n"
      "s_add_u32 s93, s93, 114688\n"
      "v_cvt_pk_bf16_f32 v192, v160, v161\n"
      "v_add_f32 v240, v240, v162\n"
      "v_mfma_f32_16x16x32_bf16 v[96:99], v[200:203], a[64:67], v[96:99]\n"
      "s_and_b32 s93, s93, 0xfffff\n"
      "v_exp_f32 v164, v164\n"
      "v_mfma_f32_16x16x32_bf16 v[96:99], v[204:207], a[96:99], v[96:99]\n"
      "s_and_b32 s75, s75, 3\n"
      "v_add_f32 v240, v240, v164\n"
      "v_add_f32 v240, v240, v163\n"
      "v_mfma_f32_16x16x32_bf16 v[100:103], v[200:203], a[68:71], v[100:103]\n"
      "s_lshl_b32 s75, s75, 17\n"
      "v_exp_f32 v165, v165\n"
      "v_mfma_f32_16x16x32_bf16 v[100:103], v[204:207], a[100:103], v[100:103]\n"
      "s_add_u32 s86, s84, s75\n"
      "v_add_f32 v240, v240, v165\n"
      "v_cvt_pk_bf16_f32 v194, v164, v165\n"
      "v_mfma_f32_16x16x32_bf16 v[104:107], v[200:203], a[72:75], v[104:107]\n"
      "s_add_u32 s75, s94, 2\n"
      "v_exp_f32 v166, v166\n"
      "v_mfma_f32_16x16x32_bf16 v[104:107], v[204:207], a[104:107], v[104:107]\n"
      "s_add_u32 m0, s23, 0\n"
      "ds_read_b64_tr_b16 a[64:65], v232 offset:16384\n"
      "v_add_f32 v240, v240, v166\n"
      "v_mfma_f32_16x16x32_bf16 v[108:111], v[200:203], a[76:79], v[108:111]\n"
      "s_and_b32 s75, s75, 3\n"
      "buffer_load_dwordx4 v250, s[48:51], s93 offen lds\n"
      "v_exp_f32 v167, v167\n"
      "v_mfma_f32_16x16x32_bf16 v[108:111], v[204:207], a[108:111], v[108:111]\n"
      "s_add_u32 s93, s93, 65536\n"
      "v_add_f32 v240, v240, v167\n"
      "v_cvt_pk_bf16_f32 v195, v166, v167\n"
      "v_mfma_f32_16x16x32_bf16 v[112:115], v[200:203], a[80:83], v[112:115]\n"
      "s_lshl_b32 s75, s75, 17\n"
      "ds_read_b64_tr_b16 a[66:67], v232 offset:20480\n"
      "ds_read_b64_tr_b16 a[68:69], v233 offset:16384\n"
      "v_mfma_f32_16x16x32_bf16 v[112:115], v[204:207], a[112:115], v[112:115]\n"
      "s_add_u32 s85, s84, s75\n"
      "v_exp_f32 v176, v176\n"
      "v_mfma_f32_16x16x32_bf16 v[116:119], v[200:203], a[84:87], v[116:119]\n"
      "s_add_u32 s75, s94, 1\n"
      "ds_read_b64_tr_b16 a[70:71], v233 offset:20480\n"
      "ds_read_b64_tr_b16 a[72:73], v232 offset:16640\n"
      "v_mfma_f32_16x16x32_bf16 v[116:119], v[204:207], a[116:119], v[116:119]\n"
      "s_and_b32 s75, s75, 3\n"
      "v_exp_f32 v177, v177\n"
      "v_mfma_f32_16x16x32_bf16 v[120:123], v[200:203], a[88:91], v[120:123]\n"
      "s_lshl_b32 s75, s75, 17\n"
      "ds_read_b64_tr_b16 a[74:75], v232 offset:20736\n"
      "v_cvt_pk_bf16_f32 v193, v162, v163\n"
      "v_mfma_f32_16x16x32_bf16 v[120:123], v[204:207], a[120:123], v[120:123]\n"
      "s_add_u32 m0, s23, 4096\n"
      "v_exp_f32 v178, v178\n"
      "v_mfma_f32_16x16x32_bf16 v[124:127], v[200:203], a[92:95], v[124:127]\n"
      "s_add_u32 s84, s84, s75\n"
      "buffer_load_dwordx4 v250, s[48:51], s93 offen lds\n"
      "ds_read_b64_tr_b16 a[76:77], v233 offset:16640\n"
      "v_mfma_f32_16x16x32_bf16 v[124:127], v[204:207], a[124:127], v[124:127]\n"
      "s_sub_u32 s93, s93, 61440\n"
      "v_exp_f32 v179, v179\n"
      "v_smfmac_f32_16x16x64_bf16 a[220:223], a[212:215], v[200:207], v247\n"
      "s_lshr_b32 s73, s22, 1\n"
      "v_cvt_pk_bf16_f32 v200, v176, v177\n"
      "v_mfma_f32_16x16x32_bf16 v[160:163], a[0:3], a[144:147], v[212:215]\n"
      "s_add_u32 s73, s73, s74\n"
      "v_exp_f32 v180, v180\n"
      "v_mfma_f32_16x16x32_bf16 v[160:163], a[4:7], a[148:151], v[160:163]\n"
      "s_add_u32 s73, s73, 16\n"
      "ds_read_b64_tr_b16 a[78:79], v233 offset:20736\n"
      "v_cvt_pk_bf16_f32 v201, v178, v179\n"
      "v_mfma_f32_16x16x32_bf16 v[160:163], a[8:11], a[152:155], v[160:163]\n"
      "s_lshl_b32 s73, s73, 5\n"
      "v_exp_f32 v181, v181\n"
      "v_mfma_f32_16x16x32_bf16 v[160:163], a[12:15], a[156:159], v[160:163]\n"
      "s_add_u32 s73, s73, s60\n"
      "v_exp_f32 v182, v182\n"
      "v_mfma_f32_16x16x32_bf16 v[164:167], a[28:31], a[156:159], v[212:215]\n"
      "s_add_u32 m0, s23, 117760\n"
      "ds_read_b64_tr_b16 a[242:243], v245 offset:20480\n"
      "v_cvt_pk_bf16_f32 v202, v180, v181\n"
      "v_mfma_f32_16x16x32_bf16 v[164:167], a[24:27], a[152:155], v[164:167]\n"
      "s_lshl_b32 s88, s73, 12\n"
      "global_load_lds_dwordx4 v237, s[28:29] offset:1024 nt\n"
      "v_mfma_f32_16x16x32_bf16 v[164:167], a[20:23], a[148:151], v[164:167]\n"
      "s_and_b32 s73, s60, 1\n"
      "v_exp_f32 v183, v183\n"
      "v_mfma_f32_16x16x32_bf16 v[164:167], a[16:19], a[144:147], v[164:167]\n"
      "s_add_u32 s68, s74, s73\n"
      "ds_read_b64_tr_b16 a[80:81], v232 offset:16896\n"
      "v_cvt_pk_bf16_f32 v203, v182, v183\n"
      "v_mfma_f32_16x16x32_bf16 v[168:171], a[32:35], a[144:147], v[212:215]\n"
      "s_add_u32 s68, s68, 16\n"
      "v_exp_f32 v184, v184\n"
      "v_mfma_f32_16x16x32_bf16 v[168:171], a[36:39], a[148:151], v[168:171]\n"
      "s_lshl_b32 s73, s73, 3\n"
      "ds_read_b64_tr_b16 a[82:83], v232 offset:20992\n"
      "ds_read_b64_tr_b16 a[84:85], v233 offset:16896\n"
      "v_mfma_f32_16x16x32_bf16 v[168:171], a[40:43], a[152:155], v[168:171]\n"
      "s_add_u32 s74, s74, s73\n"
      "v_exp_f32 v185, v185\n"
      "v_mfma_f32_16x16x32_bf16 v[168:171], a[44:47], a[156:159], v[168:171]\n"
      "s_and_b32 s73, s60, 4\n"
      "ds_read_b64_tr_b16 a[86:87], v233 offset:20992\n"
      "v_cvt_pk_bf16_f32 v204, v184, v185\n"
      "v_mfma_f32_16x16x32_bf16 v[172:175], a[60:63], a[156:159], v[212:215]\n"
      "s_add_u32 s74, s74, s73\n"
      "v_exp_f32 v186, v186\n"
      "v_mfma_f32_16x16x32_bf16 v[172:175], a[56:59], a[152:155], v[172:175]\n"
      "s_lshl_b32 s73, s73, 1\n"
      "ds_read_b64_tr_b16 a[88:89], v232 offset:17152\n"
      "ds_read_b64_tr_b16 a[90:91], v232 offset:21248\n"
      "v_mfma_f32_16x16x32_bf16 v[172:175], a[52:55], a[148:151], v[172:175]\n"
      "s_sub_u32 s95, 4, s73\n"
      "v_exp_f32 v187, v187\n"
      "v_mfma_f32_16x16x32_bf16 v[172:175], a[48:51], a[144:147], v[172:175]\n"
      "s_lshl_b32 s73, s59, 5\n"
      "ds_read_b64_tr_b16 a[92:93], v233 offset:17152\n"
      "v_cvt_pk_bf16_f32 v205, v186, v187\n"
      "v_mfma_f32_16x16x32_bf16 v[128:131], v[192:195], a[96:99], v[128:131]\n"
      "s_and_b32 s75, s60, 7\n"
      "v_exp_f32 v188, v188\n"
      "v_mfma_f32_16x16x32_bf16 v[132:135], v[192:195], a[100:103], v[132:135]\n"
      "s_add_u32 s73, s73, s75\n"
      "ds_read_b64_tr_b16 a[96:97], v234 offset:16384\n"
      "ds_read_b64_tr_b16 a[98:99], v234 offset:20480\n"
      "v_mfma_f32_16x16x32_bf16 v[136:139], v[192:195], a[104:107], v[136:139]\n"
      "s_lshl_b32 s73, s73, 4\n"
      "v_exp_f32 v189, v189\n"
      "v_mfma_f32_16x16x32_bf16 v[140:143], v[192:195], a[108:111], v[140:143]\n"
      "s_add_u32 s73, s73, s22\n"
      "ds_read_b64_tr_b16 a[100:101], v235 offset:16384\n"
      "v_cvt_pk_bf16_f32 v206, v188, v189\n"
      "v_mfma_f32_16x16x32_bf16 v[144:147], v[192:195], a[112:115], v[144:147]\n"
      "s_lshl_b32 s75, s94, 7\n"
      "v_exp_f32 v190, v190\n"
      "v_mfma_f32_16x16x32_bf16 v[148:151], v[192:195], a[116:119], v[148:151]\n"
      "s_add_u32 s47, s73, s75\n"
      "ds_read_b64_tr_b16 a[102:103], v235 offset:20480\n"
      "ds_read_b64_tr_b16 a[94:95], v233 offset:21248\n"
      "v_mfma_f32_16x16x32_bf16 v[152:155], v[192:195], a[120:123], v[152:155]\n"
      "s_mul_i32 s47, s47, 3072\n"
      "v_exp_f32 v191, v191\n"
      "v_mfma_f32_16x16x32_bf16 v[156:159], v[192:195], a[124:127], v[156:159]\n"
      "s_add_u32 m0, s23, 120832\n"
      "ds_read_b64_tr_b16 a[244:245], v246 offset:16384\n"
      "v_cvt_pk_bf16_f32 v207, v190, v191\n"
      "v_mfma_f32_16x16x32_bf16 v[176:179], a[0:3], a[160:163], v[216:219]\n"
      "s_add_u32 s47, s47, 18874368\n"
      "global_load_lds_dwordx4 v237, s[28:29] offset:2048 nt\n"
      "v_mfma_f32_16x16x32_bf16 v[176:179], a[4:7], a[164:167], v[176:179]\n"
      "s_lshl_b32 s75, s94, 2\n"
      "v_exp_f32 v160, v160\n"
      "v_mfma_f32_16x16x32_bf16 v[176:179], a[8:11], a[168:171], v[176:179]\n"
      "s_add_u32 s73, s73, s75\n"
      "ds_read_b64_tr_b16 a[104:105], v234 offset:16640\n"
      "ds_read_b64_tr_b16 a[106:107], v234 offset:20736\n"
      "v_mfma_f32_16x16x32_bf16 v[176:179], a[12:15], a[172:175], v[176:179]\n"
      "s_add_u32 s75, s94, 1\n"
      "v_exp_f32 v161, v161\n"
      "v_mfma_f32_16x16x32_bf16 v[180:183], a[28:31], a[172:175], v[216:219]\n"
      "s_and_b32 s75, s75, 3\n"
      "ds_read_b64_tr_b16 a[108:109], v235 offset:16640\n"
      "ds_read_b64_tr_b16 a[110:111], v235 offset:20736\n"
      "v_mfma_f32_16x16x32_bf16 v[180:183], a[24:27], a[168:171], v[180:183]\n"
      "s_add_u32 s62, s74, s75\n"
      "v_exp_f32 v162, v162\n"
      "v_mfma_f32_16x16x32_bf16 v[180:183], a[20:23], a[164:167], v[180:183]\n"
      "s_add_u32 s63, s62, s95\n"
      "ds_read_b64_tr_b16 a[112:113], v234 offset:16896\n"
      "v_cvt_pk_bf16_f32 v192, v160, v161\n"
      "v_mfma_f32_16x16x32_bf16 v[180:183], a[16:19], a[160:163], v[180:183]\n"
      "s_lshl_b32 s75, s75, 7\n"
      "v_exp_f32 v163, v163\n"
      "v_mfma_f32_16x16x32_bf16 v[184:187], a[32:35], a[160:163], v[216:219]\n"
      "s_add_u32 s75, s75, s73\n"
      "ds_read_b64_tr_b16 a[114:115], v234 offset:20992\n"
      "v_cvt_pk_bf16_f32 v193, v162, v163\n"
      "v_mfma_f32_16x16x32_bf16 v[184:187], a[36:39], a[164:167], v[184:187]\n"
      "s_add_u32 m0, s23, 8192\n"
      "v_exp_f32 v164, v164\n"
      "v_mfma_f32_16x16x32_bf16 v[184:187], a[40:43], a[168:171], v[184:187]\n"
      "s_mul_i32 s75, s75, 3072\n"
      "buffer_load_dwordx4 v250, s[48:51], s93 offen lds\n"
      "ds_read_b64_tr_b16 a[116:117], v235 offset:16896\n"
      "v_mfma_f32_16x16x32_bf16 v[184:187], a[44:47], a[172:175], v[184:187]\n"
      "s_add_u32 s93, s93, 65536\n"
      "v_exp_f32 v165, v165\n"
      "v_mfma_f32_16x16x32_bf16 v[188:191], a[60:63], a[172:175], v[216:219]\n"
      "s_add_u32 s40, s75, 18874368\n"
      "ds_read_b64_tr_b16 a[118:119], v235 offset:20992\n"
      "v_cvt_pk_bf16_f32 v194, v164, v165\n"
      "v_mfma_f32_16x16x32_bf16 v[188:191], a[56:59], a[168:171], v[188:191]\n"
      "s_add_u32 s75, s94, 2\n"
      "v_exp_f32 v166, v166\n"
      "v_mfma_f32_16x16x32_bf16 v[188:191], a[52:55], a[164:167], v[188:191]\n"
      "s_waitcnt lgkmcnt(2)\n"
      "ds_read_b64_tr_b16 a[120:121], v234 offset:17152\n"
      "ds_read_b64_tr_b16 a[122:123], v234 offset:21248\n"
      "v_mfma_f32_16x16x32_bf16 v[188:191], a[48:51], a[160:163], v[188:191]\n"
      "s_add_u32 m0, s23, 12288\n"
      "v_exp_f32 v167, v167\n"
      "v_mfma_f32_16x16x32_bf16 v[0:3], v[200:203], a[64:67], v[0:3]\n"
      "s_and_b32 s75, s75, 3\n"
      "buffer_load_dwordx4 v250, s[48:51], s93 offen lds\n"
      "v_exp_f32 v168, v168\n"
      "v_mfma_f32_16x16x32_bf16 v[0:3], v[204:207], a[96:99], v[0:3]\n"
      "s_sub_u32 s93, s93, 61440\n"
      "s_add_u32 m0, s23, 123904\n"
      "ds_read_b64_tr_b16 a[124:125], v235 offset:17152\n"
      "v_mfma_f32_16x16x32_bf16 v[4:7], v[200:203], a[68:71], v[4:7]\n"
      "s_add_u32 s64, s74, s75\n"
      "global_load_lds_dwordx4 v237, s[28:29] offset:3072 nt\n"
      "v_mfma_f32_16x16x32_bf16 v[4:7], v[204:207], a[100:103], v[4:7]\n"
      "s_add_u32 m0, s23, 65536\n"
      "v_exp_f32 v169, v169\n"
      "v_mfma_f32_16x16x32_bf16 v[8:11], v[200:203], a[72:75], v[8:11]\n"
      "s_add_u32 s65, s64, s95\n"
      "buffer_load_dwordx4 v250, s[48:51], s93 offen lds\n"
      "ds_read_b64_tr_b16 a[126:127], v235 offset:21248\n"
      "v_cvt_pk_bf16_f32 v195, v166, v167\n"
      "v_mfma_f32_16x16x32_bf16 v[8:11], v[204:207], a[104:107], v[8:11]\n"
      "s_add_u32 s93, s93, 65536\n"
      "v_exp_f32 v170, v170\n"
      "v_mfma_f32_16x16x32_bf16 v[12:15], v[200:203], a[76:79], v[12:15]\n"
      "s_add_u32 m0, s23, 69632\n"
      "v_cvt_pk_bf16_f32 v196, v168, v169\n"
      "v_add_u32 v237, s57, v237\n"
      "v_mfma_f32_16x16x32_bf16 v[12:15], v[204:207], a[108:111], v[12:15]\n"
      "s_lshl_b32 s75, s75, 7\n"
      "buffer_load_dwordx4 v250, s[48:51], s93 offen lds\n"
      "v_exp_f32 v171, v171\n"
      "v_mfma_f32_16x16x32_bf16 v[16:19], v[200:203], a[80:83], v[16:19]\n"
      "s_waitcnt lgkmcnt(1)\n"
      "s_sub_u32 s93, s93, 61440\n"
      "ds_read_b64_tr_b16 a[246:247], v246 offset:20480\n"
      "v_cvt_pk_bf16_f32 v197, v170, v171\n"
      "v_mfma_f32_16x16x32_bf16 v[16:19], v[204:207], a[112:115], v[16:19]\n"
      "s_add_u32 m0, s23, 73728\n"
      "ds_read_b64_tr_b16 a[248:249], v245 offset:24576\n"
      "v_exp_f32 v172, v172\n"
      "v_mfma_f32_16x16x32_bf16 v[20:23], v[200:203], a[84:87], v[20:23]\n"
      "s_add_u32 s75, s75, s73\n"
      "buffer_load_dwordx4 v250, s[48:51], s93 offen lds\n"
      "ds_read_b64_tr_b16 a[250:251], v245 offset:28672\n"
      "v_mfma_f32_16x16x32_bf16 v[20:23], v[204:207], a[116:119], v[20:23]\n"
      "s_add_u32 s93, s93, 65536\n"
      "ds_read_b64_tr_b16 a[252:253], v246 offset:24576\n"
      "v_exp_f32 v173, v173\n"
      "v_mfma_f32_16x16x32_bf16 v[24:27], v[200:203], a[88:91], v[24:27]\n"
      "s_waitcnt lgkmcnt(4)\n"
      "ds_read_b64_tr_b16 a[254:255], v246 offset:28672\n"
      "v_cvt_pk_bf16_f32 v198, v172, v173\n"
      "v_mfma_f32_16x16x32_bf16 v[24:27], v[204:207], a[120:123], v[24:27]\n"
      "s_add_u32 m0, s23, 77824\n"
      "v_exp_f32 v174, v174\n"
      "v_mfma_f32_16x16x32_bf16 v[28:31], v[200:203], a[92:95], v[28:31]\n"
      "s_mul_i32 s75, s75, 3072\n"
      "buffer_load_dwordx4 v250, s[48:51], s93 offen lds\n"
      "v_mfma_f32_16x16x32_bf16 v[28:31], v[204:207], a[124:127], v[28:31]\n"
      "s_add_u32 s93, s93, 53248\n"
      "s_and_b32 s93, s93, 0xfffff\n"
      "v_exp_f32 v175, v175\n"
      "v_smfmac_f32_16x16x64_bf16 a[216:219], a[208:211], v[200:207], v247\n"
      "s_waitcnt vmcnt(16) lgkmcnt(0)\n"
      "v_cvt_pk_bf16_f32 v199, v174, v175\n"
      "v_mfma_f32_16x16x32_bf16 v[160:163], a[0:3], a[176:179], v[220:223]\n"
      "s_barrier\n"
      "v_mfma_f32_16x16x32_bf16 v[160:163], a[4:7], a[180:183], v[160:163]\n"
      "s_add_u32 s41, s75, 18874368\n"
      "v_exp_f32 v176, v176\n"
      "v_mfma_f32_16x16x32_bf16 v[160:163], a[8:11], a[184:187], v[160:163]\n"
      "s_add_u32 s75, s94, 3\n"
      "ds_read_b128 a[0:3], v228 offset:32768\n"
      "v_mfma_f32_16x16x32_bf16 v[160:163], a[12:15], a[188:191], v[160:163]\n"
      "s_and_b32 s75, s75, 3\n"
      "v_exp_f32 v177, v177\n"
      "v_mfma_f32_16x16x32_bf16 v[164:167], a[28:31], a[188:191], v[220:223]\n"
      "s_add_u32 s66, s74, s75\n"
      "v_exp_f32 v178, v178\n"
      "v_mfma_f32_16x16x32_bf16 v[164:167], a[24:27], a[184:187], v[164:167]\n"
      "s_add_u32 m0, s23, 131072\n"
      "v_cvt_pk_bf16_f32 v200, v176, v177\n"
      "v_mfma_f32_16x16x32_bf16 v[164:167], a[20:23], a[180:183], v[164:167]\n"
      "s_add_u32 s67, s66, s95\n"
      "buffer_load_dwordx4 v250, s[48:51], s93 offen lds\n"
      "v_mfma_f32_16x16x32_bf16 v[164:167], a[16:19], a[176:179], v[164:167]\n"
      "s_add_u32 s93, s93, 65536\n"
      "v_exp_f32 v179, v179\n"
      "v_mfma_f32_16x16x32_bf16 v[168:171], a[32:35], a[176:179], v[220:223]\n"
      "s_lshl_b32 s75, s75, 7\n"
      "ds_read_b128 a[4:7], v229 offset:33024\n"
      "v_cvt_pk_bf16_f32 v201, v178, v179\n"
      "v_mfma_f32_16x16x32_bf16 v[168:171], a[36:39], a[180:183], v[168:171]\n"
      "s_add_u32 s75, s75, s73\n"
      "v_exp_f32 v180, v180\n"
      "v_mfma_f32_16x16x32_bf16 v[168:171], a[40:43], a[184:187], v[168:171]\n"
      "s_mul_i32 s75, s75, 3072\n"
      "ds_read_b128 a[8:11], v228 offset:33280\n"
      "v_mfma_f32_16x16x32_bf16 v[168:171], a[44:47], a[188:191], v[168:171]\n"
      "s_add_u32 s42, s75, 18874368\n"
      "v_exp_f32 v181, v181\n"
      "v_mfma_f32_16x16x32_bf16 v[172:175], a[60:63], a[188:191], v[220:223]\n"
      "s_lshl_b32 s75, s22, 7\n"
      "v_cvt_pk_bf16_f32 v202, v180, v181\n"
      "v_mfma_f32_16x16x32_bf16 v[172:175], a[56:59], a[184:187], v[172:175]\n"
      "s_add_u32 s75, s75, s73\n"
      "v_exp_f32 v182, v182\n"
      "v_mfma_f32_16x16x32_bf16 v[172:175], a[52:55], a[180:183], v[172:175]\n"
      "s_mul_i32 s75, s75, 3072\n"
      "ds_read_b128 a[12:15], v229 offset:33536\n"
      "v_mfma_f32_16x16x32_bf16 v[172:175], a[48:51], a[176:179], v[172:175]\n"
      "s_add_u32 s43, s75, 18876416\n"
      "v_exp_f32 v183, v183\n"
      "v_mfma_f32_16x16x32_bf16 v[32:35], v[192:195], a[64:67], v[32:35]\n"
      "s_add_u32 s69, s74, s94\n"
      "v_exp_f32 v184, v184\n"
      "v_mfma_f32_16x16x32_bf16 v[32:35], v[196:199], a[96:99], v[32:35]\n"
      "s_add_u32 m0, s23, 135168\n"
      "v_cvt_pk_bf16_f32 v203, v182, v183\n"
      "v_mfma_f32_16x16x32_bf16 v[36:39], v[192:195], a[68:71], v[36:39]\n"
      "s_add_u32 s70, s69, s95\n"
      "buffer_load_dwordx4 v250, s[48:51], s93 offen lds\n"
      "v_mfma_f32_16x16x32_bf16 v[36:39], v[196:199], a[100:103], v[36:39]\n"
      "s_sub_u32 s93, s93, 61440\n"
      "v_exp_f32 v185, v185\n"
      "v_mfma_f32_16x16x32_bf16 v[40:43], v[192:195], a[72:75], v[40:43]\n"
      "s_bfe_u32 s75, s60, 0x20001\n"
      "ds_read_b128 a[28:31], v229 offset:37632\n"
      "v_cvt_pk_bf16_f32 v204, v184, v185\n"
      "v_mfma_f32_16x16x32_bf16 v[40:43], v[196:199], a[104:107], v[40:43]\n"
      "s_add_u32 m0, s23, 139264\n"
      "v_exp_f32 v186, v186\n"
      "v_mfma_f32_16x16x32_bf16 v[44:47], v[192:195], a[76:79], v[44:47]\n"
      "s_lshl_b32 s75, s75, 2\n"
      "buffer_load_dwordx4 v250, s[48:51], s93 offen lds\n"
      "ds_read_b128 a[24:27], v228 offset:37376\n"
      "v_mfma_f32_16x16x32_bf16 v[44:47], v[196:199], a[108:111], v[44:47]\n"
      "s_add_u32 s93, s93, 65536\n"
      "v_exp_f32 v187, v187\n"
      "v_mfma_f32_16x16x32_bf16 v[48:51], v[192:195], a[80:83], v[48:51]\n"
      "s_add_u32 s75, s75, s22\n"
      "v_cvt_pk_bf16_f32 v205, v186, v187\n"
      "v_mfma_f32_16x16x32_bf16 v[48:51], v[196:199], a[112:115], v[48:51]\n"
      "s_add_u32 m0, s23, 143360\n"
      "v_exp_f32 v188, v188\n"
      "v_mfma_f32_16x16x32_bf16 v[52:55], v[192:195], a[84:87], v[52:55]\n"
      "s_lshl_b32 s75, s75, 6\n"
      "buffer_load_dwordx4 v250, s[48:51], s93 offen lds\n"
      "ds_read_b128 a[20:23], v229 offset:37120\n"
      "v_mfma_f32_16x16x32_bf16 v[52:55], v[196:199], a[116:119], v[52:55]\n"
      "s_sub_u32 s93, s93, 61440\n"
      "v_exp_f32 v189, v189\n"
      "v_mfma_f32_16x16x32_bf16 v[56:59], v[192:195], a[88:91], v[56:59]\n"
      "s_lshl_b32 s44, s69, 14\n"
      "v_exp_f32 v190, v190\n"
      "v_mfma_f32_16x16x32_bf16 v[56:59], v[196:199], a[120:123], v[56:59]\n"
      "s_add_u32 m0, s23, 147456\n"
      "v_cvt_pk_bf16_f32 v206, v188, v189\n"
      "v_mfma_f32_16x16x32_bf16 v[60:63], v[192:195], a[92:95], v[60:63]\n"
      "s_add_u32 s44, s44, s75\n"
      "buffer_load_dwordx4 v250, s[48:51], s93 offen lds\n"
      "v_mfma_f32_16x16x32_bf16 v[60:63], v[196:199], a[124:127], v[60:63]\n"
      "s_add_u32 s93, s93, 65536\n"
      "v_exp_f32 v191, v191\n"
      "v_smfmac_f32_16x16x64_bf16 a[216:219], a[212:215], v[192:199], v247\n"
      "s_lshl_b32 s45, s70, 14\n"
      "v_cvt_pk_bf16_f32 v207, v190, v191\n"
      "v_mfma_f32_16x16x32_bf16 v[176:179], a[32:35], a[192:195], v[224:227]\n"
      "s_add_u32 s45, s45, s75\n"
      "v_exp_f32 v160, v160\n"
      "v_mfma_f32_16x16x32_bf16 v[176:179], a[36:39], a[196:199], v[176:179]\n"
      "s_lshl_b32 s46, s68, 14\n"
      "ds_read_b128 a[16:19], v228 offset:36864\n"
      "ds_read_b128 a[32:35], v230 offset:32768\n"
      "v_mfma_f32_16x16x32_bf16 v[176:179], a[40:43], a[200:203], v[176:179]\n"
      "s_add_u32 s46, s46, s75\n"
      "v_exp_f32 v161, v161\n"
      "v_mfma_f32_16x16x32_bf16 v[176:179], a[44:47], a[204:207], v[176:179]\n"
      "s_lshl_b32 s73, s59, 1\n"
      "ds_read_b128 a[36:39], v231 offset:33024\n"
      "v_cvt_pk_bf16_f32 v192, v160, v161\n"
      "v_mfma_f32_16x16x32_bf16 v[180:183], a[60:63], a[204:207], v[224:227]\n"
      "s_add_u32 m0, s23, 151552\n"
      "v_exp_f32 v162, v162\n"
      "v_mfma_f32_16x16x32_bf16 v[180:183], a[56:59], a[200:203], v[180:183]\n"
      "s_and_b32 s75, s60, 1\n"
      "buffer_load_dwordx4 v250, s[48:51], s93 offen lds\n"
      "v_mfma_f32_16x16x32_bf16 v[180:183], a[52:55], a[196:199], v[180:183]\n"
      "s_sub_u32 s93, s93, 61440\n"
      "v_exp_f32 v163, v163\n"
      "v_mfma_f32_16x16x32_bf16 v[180:183], a[48:51], a[192:195], v[180:183]\n"
      "s_add_u32 m0, s23, 155648\n"
      "ds_read_b128 a[40:43], v230 offset:33280\n"
      "v_cvt_pk_bf16_f32 v193, v162, v163\n"
      "v_mfma_f32_16x16x32_bf16 v[64:67], v[200:203], a[64:67], v[64:67]\n"
      "s_add_u32 s73, s73, s75\n"
      "buffer_load_dwordx4 v250, s[48:51], s93 offen lds\n"
      "v_mfma_f32_16x16x32_bf16 v[64:67], v[204:207], a[96:99], v[64:67]\n"
      "s_add_u32 s93, s93, 65536\n"
      "v_exp_f32 v164, v164\n"
      "v_mfma_f32_16x16x32_bf16 v[68:71], v[200:203], a[68:71], v[68:71]\n"
      "s_add_u32 s73, s73, s22\n"
      "v_exp_f32 v165, v165\n"
      "v_mfma_f32_16x16x32_bf16 v[68:71], v[204:207], a[100:103], v[68:71]\n"
      "s_add_u32 m0, s23, 159744\n"
      "v_cvt_pk_bf16_f32 v194, v164, v165\n"
      "v_mfma_f32_16x16x32_bf16 v[72:75], v[200:203], a[72:75], v[72:75]\n"
      "s_and_b32 s75, s60, 2\n"
      "buffer_load_dwordx4 v250, s[48:51], s93 offen lds\n"
      "v_mfma_f32_16x16x32_bf16 v[72:75], v[204:207], a[104:107], v[72:75]\n"
      "s_add_u32 s93, s93, 53248\n"
      "s_and_b32 s93, s93, 0xfffff\n"
      "v_exp_f32 v166, v166\n"
      "v_mfma_f32_16x16x32_bf16 v[76:79], v[200:203], a[76:79], v[76:79]\n"
      "s_lshl_b32 s75, s75, 1\n"
      "ds_read_b128 a[44:47], v231 offset:33536\n"
      "ds_read_b128 a[60:63], v231 offset:37632\n"
      "v_mfma_f32_16x16x32_bf16 v[76:79], v[204:207], a[108:111], v[76:79]\n"
      "s_add_u32 s73, s73, s75\n"
      "v_exp_f32 v167, v167\n"
      "v_mfma_f32_16x16x32_bf16 v[80:83], v[200:203], a[80:83], v[80:83]\n"
      "s_and_b32 s73, s73, 7\n"
      "ds_read_b128 a[56:59], v230 offset:37376\n"
      "v_cvt_pk_bf16_f32 v195, v166, v167\n"
      "v_mfma_f32_16x16x32_bf16 v[80:83], v[204:207], a[112:115], v[80:83]\n"
      "s_lshl_b32 s75, s94, 3\n"
      "v_exp_f32 v168, v168\n"
      "v_mfma_f32_16x16x32_bf16 v[84:87], v[200:203], a[84:87], v[84:87]\n"
      "s_add_u32 s75, s75, s73\n"
      "ds_read_b128 a[52:55], v231 offset:37120\n"
      "v_mfma_f32_16x16x32_bf16 v[84:87], v[204:207], a[116:119], v[84:87]\n"
      "s_lshl_b32 s75, s75, 12\n"
      "v_exp_f32 v169, v169\n"
      "v_mfma_f32_16x16x32_bf16 v[88:91], v[200:203], a[88:91], v[88:91]\n"
      "s_lshl_b32 s62, s62, 17\n"
      "v_cvt_pk_bf16_f32 v196, v168, v169\n"
      "v_mfma_f32_16x16x32_bf16 v[88:91], v[204:207], a[120:123], v[88:91]\n"
      "s_add_u32 s62, s62, s75\n"
      "v_exp_f32 v170, v170\n"
      "v_mfma_f32_16x16x32_bf16 v[92:95], v[200:203], a[92:95], v[92:95]\n"
      "s_lshl_b32 s63, s63, 17\n"
      "ds_read_b128 a[48:51], v230 offset:36864\n"
      "v_mfma_f32_16x16x32_bf16 v[92:95], v[204:207], a[124:127], v[92:95]\n"
      "s_add_u32 s63, s63, s75\n"
      "v_exp_f32 v171, v171\n"
      "v_smfmac_f32_16x16x64_bf16 a[220:223], a[208:211], v[200:207], v247\n"
      "s_waitcnt lgkmcnt(1)\n"
      "v_cvt_pk_bf16_f32 v197, v170, v171\n"
      "v_mfma_f32_16x16x32_bf16 v[160:163], a[0:3], a[128:131], v[208:211]\n"
      "s_lshl_b32 s64, s64, 17\n"
      "v_exp_f32 v172, v172\n"
      "v_mfma_f32_16x16x32_bf16 v[160:163], a[4:7], a[132:135], v[160:163]\n"
      "s_add_u32 s64, s64, s75\n"
      "v_mfma_f32_16x16x32_bf16 v[160:163], a[8:11], a[136:139], v[160:163]\n"
      "s_lshl_b32 s65, s65, 17\n"
      "v_exp_f32 v173, v173\n"
      "v_mfma_f32_16x16x32_bf16 v[160:163], a[12:15], a[140:143], v[160:163]\n"
      "s_add_u32 s65, s65, s75\n"
      "v_cvt_pk_bf16_f32 v198, v172, v173\n"
      "v_mfma_f32_16x16x32_bf16 v[164:167], a[28:31], a[140:143], v[208:211]\n"
      "s_add_u32 m0, s23, 16384\n"
      "v_exp_f32 v174, v174\n"
      "v_mfma_f32_16x16x32_bf16 v[164:167], a[24:27], a[136:139], v[164:167]\n"
      "s_lshl_b32 s66, s66, 17\n"
      "buffer_load_dwordx4 v250, s[48:51], s93 offen lds\n"
      "v_mfma_f32_16x16x32_bf16 v[164:167], a[20:23], a[132:135], v[164:167]\n"
      "s_add_u32 s93, s93, 65536\n"
      "v_exp_f32 v175, v175\n"
      "v_mfma_f32_16x16x32_bf16 v[164:167], a[16:19], a[128:131], v[164:167]\n"
      "s_add_u32 s66, s66, s75\n"
      "v_cvt_pk_bf16_f32 v199, v174, v175\n"
      "v_mfma_f32_16x16x32_bf16 v[168:171], a[32:35], a[128:131], v[208:211]\n"
      "s_lshl_b32 s67, s67, 17\n"
      "v_exp_f32 v176, v176\n"
      "v_mfma_f32_16x16x32_bf16 v[168:171], a[36:39], a[132:135], v[168:171]\n"
      "s_add_u32 s67, s67, s75\n"
      "v_add_f32 v240, v240, v176\n"
      "v_mfma_f32_16x16x32_bf16 v[168:171], a[40:43], a[136:139], v[168:171]\n"
      "s_lshl_b32 s68, s68, 17\n"
      "v_exp_f32 v177, v177\n"
      "v_mfma_f32_16x16x32_bf16 v[168:171], a[44:47], a[140:143], v[168:171]\n"
      "s_add_u32 s68, s68, s75\n"
      "v_exp_f32 v178, v178\n"
      "v_mfma_f32_16x16x32_bf16 v[172:175], a[60:63], a[140:143], v[208:211]\n"
      "s_waitcnt lgkmcnt(0)\n"
      "s_add_u32 m0, s23, 20480\n"
      "v_add_f32 v240, v240, v177\n"
      "v_mfma_f32_16x16x32_bf16 v[172:175], a[56:59], a[136:139], v[172:175]\n"
      "s_lshl_b32 s69, s69, 17\n"
      "buffer_load_dwordx4 v250, s[48:51], s93 offen lds\n"
      "v_mfma_f32_16x16x32_bf16 v[172:175], a[52:55], a[132:135], v[172:175]\n"
      "s_sub_u32 s93, s93, 61440\n"
      "v_exp_f32 v179, v179\n"
      "v_mfma_f32_16x16x32_bf16 v[172:175], a[48:51], a[128:131], v[172:175]\n"
      "s_add_u32 s69, s69, s75\n"
      "v_cvt_pk_bf16_f32 v200, v176, v177\n"
      "v_add_f32 v240, v240, v178\n"
      "v_mfma_f32_16x16x32_bf16 v[96:99], v[192:195], a[64:67], v[96:99]\n"
      "s_lshl_b32 s70, s70, 17\n"
      "v_exp_f32 v180, v180\n"
      "v_mfma_f32_16x16x32_bf16 v[96:99], v[196:199], a[96:99], v[96:99]\n"
      "s_add_u32 s70, s70, s75\n"
      "v_add_f32 v240, v240, v180\n"
      "v_add_f32 v240, v240, v179\n"
      "v_mfma_f32_16x16x32_bf16 v[100:103], v[192:195], a[68:71], v[100:103]\n"
      "s_lshl_b32 s71, s22, 12\n"
      "v_exp_f32 v181, v181\n"
      "v_mfma_f32_16x16x32_bf16 v[100:103], v[196:199], a[100:103], v[100:103]\n"
      "s_add_u32 s75, s94, 16\n"
      "v_add_f32 v240, v240, v181\n"
      "v_cvt_pk_bf16_f32 v202, v180, v181\n"
      "v_mfma_f32_16x16x32_bf16 v[104:107], v[192:195], a[72:75], v[104:107]\n"
      "v_exp_f32 v182, v182\n"
      "v_mfma_f32_16x16x32_bf16 v[104:107], v[196:199], a[104:107], v[104:107]\n"
      "ds_read_b64_tr_b16 a[64:65], v232 offset:32768\n"
      "v_add_f32 v240, v240, v182\n"
      "v_mfma_f32_16x16x32_bf16 v[108:111], v[192:195], a[76:79], v[108:111]\n"
      "v_exp_f32 v183, v183\n"
      "v_mfma_f32_16x16x32_bf16 v[108:111], v[196:199], a[108:111], v[108:111]\n"
      "v_add_f32 v240, v240, v183\n"
      "v_cvt_pk_bf16_f32 v203, v182, v183\n"
      "v_mfma_f32_16x16x32_bf16 v[112:115], v[192:195], a[80:83], v[112:115]\n"
      "ds_read_b64_tr_b16 a[66:67], v232 offset:36864\n"
      "ds_read_b64_tr_b16 a[68:69], v233 offset:32768\n"
      "v_mfma_f32_16x16x32_bf16 v[112:115], v[196:199], a[112:115], v[112:115]\n"
      "v_exp_f32 v160, v160\n"
      "v_mfma_f32_16x16x32_bf16 v[116:119], v[192:195], a[84:87], v[116:119]\n"
      "ds_read_b64_tr_b16 a[70:71], v233 offset:36864\n"
      "ds_read_b64_tr_b16 a[72:73], v232 offset:33024\n"
      "v_mfma_f32_16x16x32_bf16 v[116:119], v[196:199], a[116:119], v[116:119]\n"
      "v_exp_f32 v161, v161\n"
      "v_mfma_f32_16x16x32_bf16 v[120:123], v[192:195], a[88:91], v[120:123]\n"
      "ds_read_b64_tr_b16 a[74:75], v232 offset:37120\n"
      "v_cvt_pk_bf16_f32 v201, v178, v179\n"
      "v_mfma_f32_16x16x32_bf16 v[120:123], v[196:199], a[120:123], v[120:123]\n"
      "s_add_u32 m0, s23, 24576\n"
      "v_exp_f32 v162, v162\n"
      "v_mfma_f32_16x16x32_bf16 v[124:127], v[192:195], a[92:95], v[124:127]\n"
      "buffer_load_dwordx4 v250, s[48:51], s93 offen lds\n"
      "ds_read_b64_tr_b16 a[76:77], v233 offset:33024\n"
      "v_mfma_f32_16x16x32_bf16 v[124:127], v[196:199], a[124:127], v[124:127]\n"
      "s_add_u32 s93, s93, 65536\n"
      "v_exp_f32 v163, v163\n"
      "v_smfmac_f32_16x16x64_bf16 a[220:223], a[212:215], v[192:199], v247\n"
      "v_cvt_pk_bf16_f32 v192, v160, v161\n"
      "v_mfma_f32_16x16x32_bf16 v[176:179], a[0:3], a[144:147], v[212:215]\n"
      "v_exp_f32 v164, v164\n"
      "v_mfma_f32_16x16x32_bf16 v[176:179], a[4:7], a[148:151], v[176:179]\n"
      "ds_read_b64_tr_b16 a[78:79], v233 offset:37120\n"
      "v_cvt_pk_bf16_f32 v193, v162, v163\n"
      "v_mfma_f32_16x16x32_bf16 v[176:179], a[8:11], a[152:155], v[176:179]\n"
      "v_exp_f32 v165, v165\n"
      "v_mfma_f32_16x16x32_bf16 v[176:179], a[12:15], a[156:159], v[176:179]\n"
      "v_exp_f32 v166, v166\n"
      "v_mfma_f32_16x16x32_bf16 v[180:183], a[28:31], a[156:159], v[212:215]\n"
      "s_add_u32 m0, s23, 28672\n"
      "v_cvt_pk_bf16_f32 v194, v164, v165\n"
      "v_mfma_f32_16x16x32_bf16 v[180:183], a[24:27], a[152:155], v[180:183]\n"
      "buffer_load_dwordx4 v250, s[48:51], s93 offen lds\n"
      "v_mfma_f32_16x16x32_bf16 v[180:183], a[20:23], a[148:151], v[180:183]\n"
      "s_sub_u32 s93, s93, 61440\n"
      "v_exp_f32 v167, v167\n"
      "v_mfma_f32_16x16x32_bf16 v[180:183], a[16:19], a[144:147], v[180:183]\n"
      "ds_read_b64_tr_b16 a[80:81], v232 offset:33280\n"
      "v_cvt_pk_bf16_f32 v195, v166, v167\n"
      "v_mfma_f32_16x16x32_bf16 v[184:187], a[32:35], a[144:147], v[212:215]\n"
      "v_exp_f32 v168, v168\n"
      "v_mfma_f32_16x16x32_bf16 v[184:187], a[36:39], a[148:151], v[184:187]\n"
      "ds_read_b64_tr_b16 a[82:83], v232 offset:37376\n"
      "ds_read_b64_tr_b16 a[84:85], v233 offset:33280\n"
      "v_mfma_f32_16x16x32_bf16 v[184:187], a[40:43], a[152:155], v[184:187]\n"
      "v_exp_f32 v169, v169\n"
      "v_mfma_f32_16x16x32_bf16 v[184:187], a[44:47], a[156:159], v[184:187]\n"
      "ds_read_b64_tr_b16 a[86:87], v233 offset:37376\n"
      "v_cvt_pk_bf16_f32 v196, v168, v169\n"
      "v_mfma_f32_16x16x32_bf16 v[188:191], a[60:63], a[156:159], v[212:215]\n"
      "v_exp_f32 v170, v170\n"
      "v_mfma_f32_16x16x32_bf16 v[188:191], a[56:59], a[152:155], v[188:191]\n"
      "ds_read_b64_tr_b16 a[88:89], v232 offset:33536\n"
      "ds_read_b64_tr_b16 a[90:91], v232 offset:37632\n"
      "v_mfma_f32_16x16x32_bf16 v[188:191], a[52:55], a[148:151], v[188:191]\n"
      "v_exp_f32 v171, v171\n"
      "v_mfma_f32_16x16x32_bf16 v[188:191], a[48:51], a[144:147], v[188:191]\n"
      "ds_read_b64_tr_b16 a[92:93], v233 offset:33536\n"
      "v_cvt_pk_bf16_f32 v197, v170, v171\n"
      "v_mfma_f32_16x16x32_bf16 v[128:131], v[200:203], a[96:99], v[128:131]\n"
      "v_exp_f32 v172, v172\n"
      "v_mfma_f32_16x16x32_bf16 v[132:135], v[200:203], a[100:103], v[132:135]\n"
      "ds_read_b64_tr_b16 a[96:97], v234 offset:32768\n"
      "ds_read_b64_tr_b16 a[98:99], v234 offset:36864\n"
      "v_mfma_f32_16x16x32_bf16 v[136:139], v[200:203], a[104:107], v[136:139]\n"
      "v_exp_f32 v173, v173\n"
      "v_mfma_f32_16x16x32_bf16 v[140:143], v[200:203], a[108:111], v[140:143]\n"
      "ds_read_b64_tr_b16 a[100:101], v235 offset:32768\n"
      "v_cvt_pk_bf16_f32 v198, v172, v173\n"
      "v_mfma_f32_16x16x32_bf16 v[144:147], v[200:203], a[112:115], v[144:147]\n"
      "v_exp_f32 v174, v174\n"
      "v_mfma_f32_16x16x32_bf16 v[148:151], v[200:203], a[116:119], v[148:151]\n"
      "ds_read_b64_tr_b16 a[102:103], v235 offset:36864\n"
      "ds_read_b64_tr_b16 a[94:95], v233 offset:37632\n"
      "v_mfma_f32_16x16x32_bf16 v[152:155], v[200:203], a[120:123], v[152:155]\n"
      "v_exp_f32 v175, v175\n"
      "v_mfma_f32_16x16x32_bf16 v[156:159], v[200:203], a[124:127], v[156:159]\n"
      "s_add_u32 m0, s23, 81920\n"
      "v_cvt_pk_bf16_f32 v199, v174, v175\n"
      "v_mfma_f32_16x16x32_bf16 v[160:163], a[0:3], a[160:163], v[216:219]\n"
      "buffer_load_dwordx4 v250, s[48:51], s93 offen lds\n"
      "v_mfma_f32_16x16x32_bf16 v[160:163], a[4:7], a[164:167], v[160:163]\n"
      "s_add_u32 s93, s93, 65536\n"
      "v_exp_f32 v176, v176\n"
      "v_mfma_f32_16x16x32_bf16 v[160:163], a[8:11], a[168:171], v[160:163]\n"
      "ds_read_b64_tr_b16 a[104:105], v234 offset:33024\n"
      "ds_read_b64_tr_b16 a[106:107], v234 offset:37120\n"
      "v_mfma_f32_16x16x32_bf16 v[160:163], a[12:15], a[172:175], v[160:163]\n"
      "v_exp_f32 v177, v177\n"
      "v_mfma_f32_16x16x32_bf16 v[164:167], a[28:31], a[172:175], v[216:219]\n"
      "ds_read_b64_tr_b16 a[108:109], v235 offset:33024\n"
      "ds_read_b64_tr_b16 a[110:111], v235 offset:37120\n"
      "v_mfma_f32_16x16x32_bf16 v[164:167], a[24:27], a[168:171], v[164:167]\n"
      "v_exp_f32 v178, v178\n"
      "v_mfma_f32_16x16x32_bf16 v[164:167], a[20:23], a[164:167], v[164:167]\n"
      "ds_read_b64_tr_b16 a[112:113], v234 offset:33280\n"
      "v_cvt_pk_bf16_f32 v200, v176, v177\n"
      "v_mfma_f32_16x16x32_bf16 v[164:167], a[16:19], a[160:163], v[164:167]\n"
      "v_exp_f32 v179, v179\n"
      "v_mfma_f32_16x16x32_bf16 v[168:171], a[32:35], a[160:163], v[216:219]\n"
      "ds_read_b64_tr_b16 a[114:115], v234 offset:37376\n"
      "v_cvt_pk_bf16_f32 v201, v178, v179\n"
      "v_mfma_f32_16x16x32_bf16 v[168:171], a[36:39], a[164:167], v[168:171]\n"
      "v_exp_f32 v180, v180\n"
      "v_mfma_f32_16x16x32_bf16 v[168:171], a[40:43], a[168:171], v[168:171]\n"
      "ds_read_b64_tr_b16 a[116:117], v235 offset:33280\n"
      "v_mfma_f32_16x16x32_bf16 v[168:171], a[44:47], a[172:175], v[168:171]\n"
      "v_exp_f32 v181, v181\n"
      "v_mfma_f32_16x16x32_bf16 v[172:175], a[60:63], a[172:175], v[216:219]\n"
      "ds_read_b64_tr_b16 a[118:119], v235 offset:37376\n"
      "v_cvt_pk_bf16_f32 v202, v180, v181\n"
      "v_mfma_f32_16x16x32_bf16 v[172:175], a[56:59], a[168:171], v[172:175]\n"
      "v_exp_f32 v182, v182\n"
      "v_mfma_f32_16x16x32_bf16 v[172:175], a[52:55], a[164:167], v[172:175]\n"
      "ds_read_b64_tr_b16 a[120:121], v234 offset:33536\n"
      "ds_read_b64_tr_b16 a[122:123], v234 offset:37632\n"
      "v_mfma_f32_16x16x32_bf16 v[172:175], a[48:51], a[160:163], v[172:175]\n"
      "s_waitcnt lgkmcnt(4)\n"
      "v_exp_f32 v183, v183\n"
      "v_mfma_f32_16x16x32_bf16 v[0:3], v[192:195], a[64:67], v[0:3]\n"
      "v_exp_f32 v184, v184\n"
      "v_mfma_f32_16x16x32_bf16 v[0:3], v[196:199], a[96:99], v[0:3]\n"
      "s_add_u32 m0, s23, 86016\n"
      "ds_read_b64_tr_b16 a[124:125], v235 offset:33536\n"
      "v_mfma_f32_16x16x32_bf16 v[4:7], v[192:195], a[68:71], v[4:7]\n"
      "buffer_load_dwordx4 v250, s[48:51], s93 offen lds\n"
      "v_mfma_f32_16x16x32_bf16 v[4:7], v[196:199], a[100:103], v[4:7]\n"
      "s_sub_u32 s93, s93, 61440\n"
      "v_exp_f32 v185, v185\n"
      "v_mfma_f32_16x16x32_bf16 v[8:11], v[192:195], a[72:75], v[8:11]\n"
      "ds_read_b64_tr_b16 a[126:127], v235 offset:37632\n"
      "v_cvt_pk_bf16_f32 v203, v182, v183\n"
      "v_mfma_f32_16x16x32_bf16 v[8:11], v[196:199], a[104:107], v[8:11]\n"
      "v_exp_f32 v186, v186\n"
      "v_mfma_f32_16x16x32_bf16 v[12:15], v[192:195], a[76:79], v[12:15]\n"
      "v_cvt_pk_bf16_f32 v204, v184, v185\n"
      "v_readlane_b32 s91, v254, 20\n"
      "v_mfma_f32_16x16x32_bf16 v[12:15], v[196:199], a[108:111], v[12:15]\n"
      "v_exp_f32 v187, v187\n"
      "v_mfma_f32_16x16x32_bf16 v[16:19], v[192:195], a[80:83], v[16:19]\n"
      "s_waitcnt lgkmcnt(1)\n"
      "v_cvt_pk_bf16_f32 v205, v186, v187\n"
      "v_readlane_b32 s92, v254, s75\n"
      "v_mfma_f32_16x16x32_bf16 v[16:19], v[196:199], a[112:115], v[16:19]\n"
      "s_add_u32 m0, s23, 90112\n"
      "v_exp_f32 v188, v188\n"
      "v_mfma_f32_16x16x32_bf16 v[20:23], v[192:195], a[84:87], v[20:23]\n"
      "buffer_load_dwordx4 v250, s[48:51], s93 offen lds\n"
      "v_mfma_f32_16x16x32_bf16 v[20:23], v[196:199], a[116:119], v[20:23]\n"
      "s_add_u32 s93, s93, 65536\n"
      "v_exp_f32 v189, v189\n"
      "v_mfma_f32_16x16x32_bf16 v[24:27], v[192:195], a[88:91], v[24:27]\n"
      "s_waitcnt lgkmcnt(0)\n"
      "v_cvt_pk_bf16_f32 v206, v188, v189\n"
      "v_lshrrev_b32 v237, 4, v255\n"
      "v_mfma_f32_16x16x32_bf16 v[24:27], v[196:199], a[120:123], v[24:27]\n"
      "s_add_u32 m0, s23, 94208\n"
      "v_exp_f32 v190, v190\n"
      "v_mfma_f32_16x16x32_bf16 v[28:31], v[192:195], a[92:95], v[28:31]\n"
      "buffer_load_dwordx4 v250, s[48:51], s93 offen lds\n"
      "v_mfma_f32_16x16x32_bf16 v[28:31], v[196:199], a[124:127], v[28:31]\n"
      "s_add_u32 s93, s93, 53248\n"
      "s_and_b32 s93, s93, 0xfffff\n"
      "v_exp_f32 v191, v191\n"
      "v_smfmac_f32_16x16x64_bf16 a[216:219], a[208:211], v[192:199], v247\n"
      "s_waitcnt vmcnt(8) lgkmcnt(0)\n"
      "v_cvt_pk_bf16_f32 v207, v190, v191\n"
      "v_mfma_f32_16x16x32_bf16 v[176:179], a[0:3], a[176:179], v[220:223]\n"
      "s_barrier\n"
      "v_mul_u32_u24 v237, 0x340, v237\n"
      "v_and_b32 v236, 0xc0, v237\n"
      "v_mfma_f32_16x16x32_bf16 v[176:179], a[4:7], a[180:183], v[176:179]\n"
      "v_exp_f32 v160, v160\n"
      "v_mfma_f32_16x16x32_bf16 v[176:179], a[8:11], a[184:187], v[176:179]\n"
      "ds_read_b128 a[0:3], v228 offset:49152\n"
      "v_xor_b32 v236, v236, v248\n"
      "v_mfma_f32_16x16x32_bf16 v[176:179], a[12:15], a[188:191], v[176:179]\n"
      "v_exp_f32 v161, v161\n"
      "v_mfma_f32_16x16x32_bf16 v[180:183], a[28:31], a[188:191], v[220:223]\n"
      "v_exp_f32 v162, v162\n"
      "v_mfma_f32_16x16x32_bf16 v[180:183], a[24:27], a[184:187], v[180:183]\n"
      "s_add_u32 m0, s23, 32768\n"
      "v_cvt_pk_bf16_f32 v192, v160, v161\n"
      "v_and_b32 v237, 0xf00, v237\n"
      "v_mfma_f32_16x16x32_bf16 v[180:183], a[20:23], a[180:183], v[180:183]\n"
      "buffer_load_dwordx4 v250, s[48:51], s93 offen lds\n"
      "v_mfma_f32_16x16x32_bf16 v[180:183], a[16:19], a[176:179], v[180:183]\n"
      "s_add_u32 s93, s93, 65536\n"
      "v_exp_f32 v163, v163\n"
      "v_mfma_f32_16x16x32_bf16 v[184:187], a[32:35], a[176:179], v[220:223]\n"
      "ds_read_b128 a[4:7], v229 offset:49408\n"
      "v_cvt_pk_bf16_f32 v193, v162, v163\n"
      "v_mfma_f32_16x16x32_bf16 v[184:187], a[36:39], a[180:183], v[184:187]\n"
      "v_exp_f32 v164, v164\n"
      "v_mfma_f32_16x16x32_bf16 v[184:187], a[40:43], a[184:187], v[184:187]\n"
      "ds_read_b128 a[8:11], v228 offset:49664\n"
      "v_add_u32 v236, v236, v237\n"
      "v_mfma_f32_16x16x32_bf16 v[184:187], a[44:47], a[188:191], v[184:187]\n"
      "v_exp_f32 v165, v165\n"
      "v_mfma_f32_16x16x32_bf16 v[188:191], a[60:63], a[188:191], v[220:223]\n"
      "v_cvt_pk_bf16_f32 v194, v164, v165\n"
      "v_mov_b32 v237, 0\n"
      "v_mfma_f32_16x16x32_bf16 v[188:191], a[56:59], a[184:187], v[188:191]\n"
      "v_exp_f32 v166, v166\n"
      "v_mfma_f32_16x16x32_bf16 v[188:191], a[52:55], a[180:183], v[188:191]\n"
      "ds_read_b128 a[12:15], v229 offset:49920\n"
      "v_mfma_f32_16x16x32_bf16 v[188:191], a[48:51], a[176:179], v[188:191]\n"
      "v_exp_f32 v167, v167\n"
      "v_mfma_f32_16x16x32_bf16 v[32:35], v[200:203], a[64:67], v[32:35]\n"
      "v_exp_f32 v168, v168\n"
      "v_mfma_f32_16x16x32_bf16 v[32:35], v[204:207], a[96:99], v[32:35]\n"
      "s_add_u32 m0, s23, 36864\n"
      "v_cvt_pk_bf16_f32 v195, v166, v167\n"
      "v_mfma_f32_16x16x32_bf16 v[36:39], v[200:203], a[68:71], v[36:39]\n"
      "buffer_load_dwordx4 v250, s[48:51], s93 offen lds\n"
      "v_mfma_f32_16x16x32_bf16 v[36:39], v[204:207], a[100:103], v[36:39]\n"
      "s_sub_u32 s93, s93, 61440\n"
      "v_exp_f32 v169, v169\n"
      "v_mfma_f32_16x16x32_bf16 v[40:43], v[200:203], a[72:75], v[40:43]\n"
      "ds_read_b128 a[28:31], v229 offset:54016\n"
      "v_cvt_pk_bf16_f32 v196, v168, v169\n"
      "v_mfma_f32_16x16x32_bf16 v[40:43], v[204:207], a[104:107], v[40:43]\n"
      "s_add_u32 m0, s23, 40960\n"
      "v_exp_f32 v170, v170\n"
      "v_mfma_f32_16x16x32_bf16 v[44:47], v[200:203], a[76:79], v[44:47]\n"
      "buffer_load_dwordx4 v250, s[48:51], s93 offen lds\n"
      "ds_read_b128 a[24:27], v228 offset:53760\n"
      "v_mfma_f32_16x16x32_bf16 v[44:47], v[204:207], a[108:111], v[44:47]\n"
      "s_add_u32 s93, s93, 65536\n"
      "v_exp_f32 v171, v171\n"
      "v_mfma_f32_16x16x32_bf16 v[48:51], v[200:203], a[80:83], v[48:51]\n"
      "v_cvt_pk_bf16_f32 v197, v170, v171\n"
      "v_mfma_f32_16x16x32_bf16 v[48:51], v[204:207], a[112:115], v[48:51]\n"
      "s_add_u32 m0, s23, 45056\n"
      "v_exp_f32 v172, v172\n"
      "v_mfma_f32_16x16x32_bf16 v[52:55], v[200:203], a[84:87], v[52:55]\n"
      "buffer_load_dwordx4 v250, s[48:51], s93 offen lds\n"
      "ds_read_b128 a[20:23], v229 offset:53504\n"
      "v_mfma_f32_16x16x32_bf16 v[52:55], v[204:207], a[116:119], v[52:55]\n"
      "s_sub_u32 s93, s93, 61440\n"
      "v_exp_f32 v173, v173\n"
      "v_mfma_f32_16x16x32_bf16 v[56:59], v[200:203], a[88:91], v[56:59]\n"
      "v_exp_f32 v174, v174\n"
      "v_mfma_f32_16x16x32_bf16 v[56:59], v[204:207], a[120:123], v[56:59]\n"
      "s_add_u32 m0, s23, 98304\n"
      "v_cvt_pk_bf16_f32 v198, v172, v173\n"
      "v_mfma_f32_16x16x32_bf16 v[60:63], v[200:203], a[92:95], v[60:63]\n"
      "buffer_load_dwordx4 v250, s[48:51], s93 offen lds\n"
      "v_mfma_f32_16x16x32_bf16 v[60:63], v[204:207], a[124:127], v[60:63]\n"
      "s_add_u32 s93, s93, 65536\n"
      "v_exp_f32 v175, v175\n"
      "v_smfmac_f32_16x16x64_bf16 a[216:219], a[212:215], v[200:207], v247\n"
      "v_cvt_pk_bf16_f32 v199, v174, v175\n"
      "v_mfma_f32_16x16x32_bf16 v[160:163], a[32:35], a[192:195], v[224:227]\n"
      "v_exp_f32 v176, v176\n"
      "v_mfma_f32_16x16x32_bf16 v[160:163], a[36:39], a[196:199], v[160:163]\n"
      "ds_read_b128 a[16:19], v228 offset:53248\n"
      "ds_read_b128 a[32:35], v230 offset:49152\n"
      "v_mfma_f32_16x16x32_bf16 v[160:163], a[40:43], a[200:203], v[160:163]\n"
      "v_exp_f32 v177, v177\n"
      "v_mfma_f32_16x16x32_bf16 v[160:163], a[44:47], a[204:207], v[160:163]\n"
      "ds_read_b128 a[36:39], v231 offset:49408\n"
      "v_cvt_pk_bf16_f32 v200, v176, v177\n"
      "v_mfma_f32_16x16x32_bf16 v[164:167], a[60:63], a[204:207], v[224:227]\n"
      "s_add_u32 m0, s23, 102400\n"
      "v_exp_f32 v178, v178\n"
      "v_mfma_f32_16x16x32_bf16 v[164:167], a[56:59], a[200:203], v[164:167]\n"
      "buffer_load_dwordx4 v250, s[48:51], s93 offen lds\n"
      "v_mfma_f32_16x16x32_bf16 v[164:167], a[52:55], a[196:199], v[164:167]\n"
      "s_sub_u32 s93, s93, 61440\n"
      "v_exp_f32 v179, v179\n"
      "v_mfma_f32_16x16x32_bf16 v[164:167], a[48:51], a[192:195], v[164:167]\n"
      "s_add_u32 m0, s23, 106496\n"
      "ds_read_b128 a[40:43], v230 offset:49664\n"
      "v_cvt_pk_bf16_f32 v201, v178, v179\n"
      "v_mfma_f32_16x16x32_bf16 v[64:67], v[192:195], a[64:67], v[64:67]\n"
      "buffer_load_dwordx4 v250, s[48:51], s93 offen lds\n"
      "v_mfma_f32_16x16x32_bf16 v[64:67], v[196:199], a[96:99], v[64:67]\n"
      "s_add_u32 s93, s93, 65536\n"
      "v_exp_f32 v180, v180\n"
      "v_mfma_f32_16x16x32_bf16 v[68:71], v[192:195], a[68:71], v[68:71]\n"
      "v_exp_f32 v181, v181\n"
      "v_mfma_f32_16x16x32_bf16 v[68:71], v[196:199], a[100:103], v[68:71]\n"
      "s_add_u32 m0, s23, 110592\n"
      "v_cvt_pk_bf16_f32 v202, v180, v181\n"
      "v_mfma_f32_16x16x32_bf16 v[72:75], v[192:195], a[72:75], v[72:75]\n"
      "buffer_load_dwordx4 v250, s[48:51], s93 offen lds\n"
      "v_mfma_f32_16x16x32_bf16 v[72:75], v[196:199], a[104:107], v[72:75]\n"
      "s_add_u32 s93, s93, 53248\n"
      "s_and_b32 s93, s93, 0xfffff\n"
      "v_exp_f32 v182, v182\n"
      "v_mfma_f32_16x16x32_bf16 v[76:79], v[192:195], a[76:79], v[76:79]\n"
      "ds_read_b128 a[44:47], v231 offset:49920\n"
      "ds_read_b128 a[60:63], v231 offset:54016\n"
      "v_mfma_f32_16x16x32_bf16 v[76:79], v[196:199], a[108:111], v[76:79]\n"
      "v_exp_f32 v183, v183\n"
      "v_mfma_f32_16x16x32_bf16 v[80:83], v[192:195], a[80:83], v[80:83]\n"
      "ds_read_b128 a[56:59], v230 offset:53760\n"
      "v_cvt_pk_bf16_f32 v203, v182, v183\n"
      "v_mfma_f32_16x16x32_bf16 v[80:83], v[196:199], a[112:115], v[80:83]\n"
      "v_exp_f32 v184, v184\n"
      "v_mfma_f32_16x16x32_bf16 v[84:87], v[192:195], a[84:87], v[84:87]\n"
      "ds_read_b128 a[52:55], v231 offset:53504\n"
      "v_mfma_f32_16x16x32_bf16 v[84:87], v[196:199], a[116:119], v[84:87]\n"
      "v_exp_f32 v185, v185\n"
      "v_mfma_f32_16x16x32_bf16 v[88:91], v[192:195], a[88:91], v[88:91]\n"
      "v_cvt_pk_bf16_f32 v204, v184, v185\n"
      "v_mfma_f32_16x16x32_bf16 v[88:91], v[196:199], a[120:123], v[88:91]\n"
      "v_exp_f32 v186, v186\n"
      "v_mfma_f32_16x16x32_bf16 v[92:95], v[192:195], a[92:95], v[92:95]\n"
      "ds_read_b128 a[48:51], v230 offset:53248\n"
      "v_mfma_f32_16x16x32_bf16 v[92:95], v[196:199], a[124:127], v[92:95]\n"
      "v_exp_f32 v187, v187\n"
      "v_smfmac_f32_16x16x64_bf16 a[220:223], a[208:211], v[192:199], v247\n"
      "s_waitcnt lgkmcnt(1)\n"
      "v_cvt_pk_bf16_f32 v205, v186, v187\n"
      "v_mfma_f32_16x16x32_bf16 v[176:179], a[0:3], a[128:131], v[208:211]\n"
      "v_exp_f32 v188, v188\n"
      "v_mfma_f32_16x16x32_bf16 v[176:179], a[4:7], a[132:135], v[176:179]\n"
      "v_mfma_f32_16x16x32_bf16 v[176:179], a[8:11], a[136:139], v[176:179]\n"
      "v_exp_f32 v189, v189\n"
      "v_mfma_f32_16x16x32_bf16 v[176:179], a[12:15], a[140:143], v[176:179]\n"
      "v_cvt_pk_bf16_f32 v206, v188, v189\n"
      "v_xor_b32 v228, 0x0, v236\n"
      "v_mfma_f32_16x16x32_bf16 v[180:183], a[28:31], a[140:143], v[208:211]\n"
      "v_exp_f32 v190, v190\n"
      "v_mfma_f32_16x16x32_bf16 v[180:183], a[24:27], a[136:139], v[180:183]\n"
      "v_xor_b32 v229, 0x110, v236\n"
      "v_xor_b32 v230, 0x220, v236\n"
      "v_mfma_f32_16x16x32_bf16 v[180:183], a[20:23], a[132:135], v[180:183]\n"
      "v_exp_f32 v191, v191\n"
      "v_mfma_f32_16x16x32_bf16 v[180:183], a[16:19], a[128:131], v[180:183]\n"
      "v_cvt_pk_bf16_f32 v207, v190, v191\n"
      "v_xor_b32 v231, 0x330, v236\n"
      "v_mfma_f32_16x16x32_bf16 v[184:187], a[32:35], a[128:131], v[208:211]\n"
      "v_exp_f32 v160, v160\n"
      "v_mfma_f32_16x16x32_bf16 v[184:187], a[36:39], a[132:135], v[184:187]\n"
      "v_add_f32 v240, v240, v160\n"
      "v_mov_b32 v236, 1.0\n"
      "v_mfma_f32_16x16x32_bf16 v[184:187], a[40:43], a[136:139], v[184:187]\n"
      "v_exp_f32 v161, v161\n"
      "v_mfma_f32_16x16x32_bf16 v[184:187], a[44:47], a[140:143], v[184:187]\n"
      "v_exp_f32 v162, v162\n"
      "v_mfma_f32_16x16x32_bf16 v[188:191], a[60:63], a[140:143], v[208:211]\n"
      "v_add_f32 v240, v240, v161\n"
      "v_mfma_f32_16x16x32_bf16 v[188:191], a[56:59], a[136:139], v[188:191]\n"
      "v_mfma_f32_16x16x32_bf16 v[188:191], a[52:55], a[132:135], v[188:191]\n"
      "s_waitcnt lgkmcnt(0)\n"
      "v_exp_f32 v163, v163\n"
      "v_mfma_f32_16x16x32_bf16 v[188:191], a[48:51], a[128:131], v[188:191]\n"
      "v_cvt_pk_bf16_f32 v192, v160, v161\n"
      "v_add_f32 v240, v240, v162\n"
      "v_mfma_f32_16x16x32_bf16 v[96:99], v[200:203], a[64:67], v[96:99]\n"
      "ds_read_b64_tr_b16 a[128:129], v241 offset:0\n"
      "v_exp_f32 v164, v164\n"
      "v_mfma_f32_16x16x32_bf16 v[96:99], v[204:207], a[96:99], v[96:99]\n"
      "ds_read_b64_tr_b16 a[130:131], v241 offset:4096\n"
      "v_add_f32 v240, v240, v164\n"
      "v_add_f32 v240, v240, v163\n"
      "v_mfma_f32_16x16x32_bf16 v[100:103], v[200:203], a[68:71], v[100:103]\n"
      "ds_read_b64_tr_b16 a[132:133], v242 offset:0\n"
      "v_exp_f32 v165, v165\n"
      "v_mfma_f32_16x16x32_bf16 v[100:103], v[204:207], a[100:103], v[100:103]\n"
      "ds_read_b64_tr_b16 a[134:135], v242 offset:4096\n"
      "v_add_f32 v240, v240, v165\n"
      "v_cvt_pk_bf16_f32 v194, v164, v165\n"
      "v_mfma_f32_16x16x32_bf16 v[104:107], v[200:203], a[72:75], v[104:107]\n"
      "ds_read_b64_tr_b16 a[136:137], v241 offset:8192\n"
      "v_exp_f32 v166, v166\n"
      "v_mfma_f32_16x16x32_bf16 v[104:107], v[204:207], a[104:107], v[104:107]\n"
      "ds_read_b64_tr_b16 a[64:65], v232 offset:49152\n"
      "v_add_f32 v240, v240, v166\n"
      "v_mfma_f32_16x16x32_bf16 v[108:111], v[200:203], a[76:79], v[108:111]\n"
      "ds_read_b64_tr_b16 a[138:139], v241 offset:12288\n"
      "v_exp_f32 v167, v167\n"
      "v_mfma_f32_16x16x32_bf16 v[108:111], v[204:207], a[108:111], v[108:111]\n"
      "ds_read_b64_tr_b16 a[140:141], v242 offset:8192\n"
      "v_add_f32 v240, v240, v167\n"
      "v_cvt_pk_bf16_f32 v195, v166, v167\n"
      "v_mfma_f32_16x16x32_bf16 v[112:115], v[200:203], a[80:83], v[112:115]\n"
      "ds_read_b64_tr_b16 a[66:67], v232 offset:53248\n"
      "ds_read_b64_tr_b16 a[68:69], v233 offset:49152\n"
      "ds_read_b64_tr_b16 a[142:143], v242 offset:12288\n"
      "v_mfma_f32_16x16x32_bf16 v[112:115], v[204:207], a[112:115], v[112:115]\n"
      "v_exp_f32 v176, v176\n"
      "v_mfma_f32_16x16x32_bf16 v[116:119], v[200:203], a[84:87], v[116:119]\n"
      "ds_read_b64_tr_b16 a[70:71], v233 offset:53248\n"
      "ds_read_b64_tr_b16 a[72:73], v232 offset:49408\n"
      "v_mfma_f32_16x16x32_bf16 v[116:119], v[204:207], a[116:119], v[116:119]\n"
      "v_exp_f32 v177, v177\n"
      "v_mfma_f32_16x16x32_bf16 v[120:123], v[200:203], a[88:91], v[120:123]\n"
      "ds_read_b64_tr_b16 a[74:75], v232 offset:53504\n"
      "v_cvt_pk_bf16_f32 v193, v162, v163\n"
      "v_mfma_f32_16x16x32_bf16 v[120:123], v[204:207], a[120:123], v[120:123]\n"
      "v_exp_f32 v178, v178\n"
      "v_mfma_f32_16x16x32_bf16 v[124:127], v[200:203], a[92:95], v[124:127]\n"
      "ds_read_b64_tr_b16 a[76:77], v233 offset:49408\n"
      "v_mfma_f32_16x16x32_bf16 v[124:127], v[204:207], a[124:127], v[124:127]\n"
      "v_exp_f32 v179, v179\n"
      "v_smfmac_f32_16x16x64_bf16 a[220:223], a[212:215], v[200:207], v247\n"
      "v_cvt_pk_bf16_f32 v200, v176, v177\n"
      "v_mfma_f32_16x16x32_bf16 v[160:163], a[0:3], a[144:147], v[212:215]\n"
      "s_waitcnt lgkmcnt(4)\n"
      "v_exp_f32 v180, v180\n"
      "v_mfma_f32_16x16x32_bf16 v[160:163], a[4:7], a[148:151], v[160:163]\n"
      "s_barrier\n"
      "s_add_u32 m0, s23, 0\n"
      "ds_read_b64_tr_b16 a[78:79], v233 offset:53504\n"
      "v_cvt_pk_bf16_f32 v201, v178, v179\n"
      "v_mfma_f32_16x16x32_bf16 v[160:163], a[8:11], a[152:155], v[160:163]\n"
      "buffer_load_dwordx4 v250, s[48:51], s93 offen lds\n"
      "v_exp_f32 v181, v181\n"
      "v_mfma_f32_16x16x32_bf16 v[160:163], a[12:15], a[156:159], v[160:163]\n"
      "s_add_u32 s93, s93, 65536\n"
      "v_exp_f32 v182, v182\n"
      "v_mfma_f32_16x16x32_bf16 v[164:167], a[28:31], a[156:159], v[212:215]\n"
      "s_add_u32 m0, s23, 4096\n"
      "v_cvt_pk_bf16_f32 v202, v180, v181\n"
      "v_mfma_f32_16x16x32_bf16 v[164:167], a[24:27], a[152:155], v[164:167]\n"
      "buffer_load_dwordx4 v250, s[48:51], s93 offen lds\n"
      "v_mfma_f32_16x16x32_bf16 v[164:167], a[20:23], a[148:151], v[164:167]\n"
      "s_sub_u32 s93, s93, 61440\n"
      "v_exp_f32 v183, v183\n"
      "v_mfma_f32_16x16x32_bf16 v[164:167], a[16:19], a[144:147], v[164:167]\n"
      "ds_read_b64_tr_b16 a[80:81], v232 offset:49664\n"
      "v_cvt_pk_bf16_f32 v203, v182, v183\n"
      "v_mfma_f32_16x16x32_bf16 v[168:171], a[32:35], a[144:147], v[212:215]\n"
      "s_add_u32 m0, s23, 8192\n"
      "v_exp_f32 v184, v184\n"
      "v_mfma_f32_16x16x32_bf16 v[168:171], a[36:39], a[148:151], v[168:171]\n"
      "buffer_load_dwordx4 v250, s[48:51], s93 offen lds\n"
      "ds_read_b64_tr_b16 a[82:83], v232 offset:53760\n"
      "ds_read_b64_tr_b16 a[84:85], v233 offset:49664\n"
      "v_mfma_f32_16x16x32_bf16 v[168:171], a[40:43], a[152:155], v[168:171]\n"
      "s_add_u32 s93, s93, 65536\n"
      "v_exp_f32 v185, v185\n"
      "v_mfma_f32_16x16x32_bf16 v[168:171], a[44:47], a[156:159], v[168:171]\n"
      "s_add_u32 m0, s23, 12288\n"
      "ds_read_b64_tr_b16 a[86:87], v233 offset:53760\n"
      "v_cvt_pk_bf16_f32 v204, v184, v185\n"
      "v_mfma_f32_16x16x32_bf16 v[172:175], a[60:63], a[156:159], v[212:215]\n"
      "buffer_load_dwordx4 v250, s[48:51], s93 offen lds\n"
      "v_exp_f32 v186, v186\n"
      "v_mfma_f32_16x16x32_bf16 v[172:175], a[56:59], a[152:155], v[172:175]\n"
      "s_sub_u32 s93, s93, 61440\n"
      "ds_read_b64_tr_b16 a[88:89], v232 offset:49920\n"
      "ds_read_b64_tr_b16 a[90:91], v232 offset:54016\n"
      "v_mfma_f32_16x16x32_bf16 v[172:175], a[52:55], a[148:151], v[172:175]\n"
      "s_add_u32 m0, s23, 49152\n"
      "v_exp_f32 v187, v187\n"
      "v_mfma_f32_16x16x32_bf16 v[172:175], a[48:51], a[144:147], v[172:175]\n"
      "buffer_load_dwordx4 v250, s[48:51], s93 offen lds\n"
      "ds_read_b64_tr_b16 a[92:93], v233 offset:49920\n"
      "v_cvt_pk_bf16_f32 v205, v186, v187\n"
      "v_mfma_f32_16x16x32_bf16 v[128:131], v[192:195], a[96:99], v[128:131]\n"
      "s_add_u32 s93, s93, 65536\n"
      "ds_read_b64_tr_b16 a[144:145], v243 offset:0\n"
      "v_exp_f32 v188, v188\n"
      "v_mfma_f32_16x16x32_bf16 v[132:135], v[192:195], a[100:103], v[132:135]\n"
      "s_add_u32 m0, s23, 53248\n"
      "ds_read_b64_tr_b16 a[96:97], v234 offset:49152\n"
      "ds_read_b64_tr_b16 a[98:99], v234 offset:53248\n"
      "ds_read_b64_tr_b16 a[146:147], v243 offset:4096\n"
      "ds_read_b64_tr_b16 a[148:149], v244 offset:0\n"
      "v_mfma_f32_16x16x32_bf16 v[136:139], v[192:195], a[104:107], v[136:139]\n"
      "buffer_load_dwordx4 v250, s[48:51], s93 offen lds\n"
      "ds_read_b64_tr_b16 a[150:151], v244 offset:4096\n"
      "v_exp_f32 v189, v189\n"
      "v_mfma_f32_16x16x32_bf16 v[140:143], v[192:195], a[108:111], v[140:143]\n"
      "s_sub_u32 s93, s93, 61440\n"
      "ds_read_b64_tr_b16 a[100:101], v235 offset:49152\n"
      "v_cvt_pk_bf16_f32 v206, v188, v189\n"
      "v_mfma_f32_16x16x32_bf16 v[144:147], v[192:195], a[112:115], v[144:147]\n"
      "s_add_u32 m0, s23, 57344\n"
      "ds_read_b64_tr_b16 a[152:153], v243 offset:8192\n"
      "v_exp_f32 v190, v190\n"
      "v_mfma_f32_16x16x32_bf16 v[148:151], v[192:195], a[116:119], v[148:151]\n"
      "buffer_load_dwordx4 v250, s[48:51], s93 offen lds\n"
      "ds_read_b64_tr_b16 a[102:103], v235 offset:53248\n"
      "ds_read_b64_tr_b16 a[94:95], v233 offset:54016\n"
      "ds_read_b64_tr_b16 a[154:155], v243 offset:12288\n"
      "v_mfma_f32_16x16x32_bf16 v[152:155], v[192:195], a[120:123], v[152:155]\n"
      "s_add_u32 s93, s93, 65536\n"
      "ds_read_b64_tr_b16 a[156:157], v244 offset:8192\n"
      "v_exp_f32 v191, v191\n"
      "v_mfma_f32_16x16x32_bf16 v[156:159], v[192:195], a[124:127], v[156:159]\n"
      "s_add_u32 m0, s23, 61440\n"
      "ds_read_b64_tr_b16 a[158:159], v244 offset:12288\n"
      "v_cvt_pk_bf16_f32 v207, v190, v191\n"
      "v_mfma_f32_16x16x32_bf16 v[176:179], a[0:3], a[160:163], v[216:219]\n"
      "buffer_load_dwordx4 v250, s[48:51], s93 offen lds\n"
      "v_mfma_f32_16x16x32_bf16 v[176:179], a[4:7], a[164:167], v[176:179]\n"
      "s_add_u32 s93, s93, 53248\n"
      "s_and_b32 s93, s93, 0xfffff\n"
      "v_exp_f32 v160, v160\n"
      "v_mfma_f32_16x16x32_bf16 v[176:179], a[8:11], a[168:171], v[176:179]\n"
      "ds_read_b64_tr_b16 a[104:105], v234 offset:49408\n"
      "ds_read_b64_tr_b16 a[106:107], v234 offset:53504\n"
      "v_mfma_f32_16x16x32_bf16 v[176:179], a[12:15], a[172:175], v[176:179]\n"
      "v_exp_f32 v161, v161\n"
      "v_mfma_f32_16x16x32_bf16 v[180:183], a[28:31], a[172:175], v[216:219]\n"
      "ds_read_b64_tr_b16 a[108:109], v235 offset:49408\n"
      "ds_read_b64_tr_b16 a[110:111], v235 offset:53504\n"
      "v_mfma_f32_16x16x32_bf16 v[180:183], a[24:27], a[168:171], v[180:183]\n"
      "s_waitcnt lgkmcnt(4)\n"
      "v_exp_f32 v162, v162\n"
      "v_mfma_f32_16x16x32_bf16 v[180:183], a[20:23], a[164:167], v[180:183]\n"
      "s_barrier\n"
      "ds_read_b64_tr_b16 a[112:113], v234 offset:49664\n"
      "v_cvt_pk_bf16_f32 v192, v160, v161\n"
      "v_mfma_f32_16x16x32_bf16 v[180:183], a[16:19], a[160:163], v[180:183]\n"
      "v_exp_f32 v163, v163\n"
      "v_mfma_f32_16x16x32_bf16 v[184:187], a[32:35], a[160:163], v[216:219]\n"
      "ds_read_b64_tr_b16 a[114:115], v234 offset:53760\n"
      "v_cvt_pk_bf16_f32 v193, v162, v163\n"
      "v_mfma_f32_16x16x32_bf16 v[184:187], a[36:39], a[164:167], v[184:187]\n"
      "s_add_u32 m0, s23, 65536\n"
      "v_exp_f32 v164, v164\n"
      "v_mfma_f32_16x16x32_bf16 v[184:187], a[40:43], a[168:171], v[184:187]\n"
      "buffer_load_dwordx4 v250, s[48:51], s93 offen lds\n"
      "ds_read_b64_tr_b16 a[116:117], v235 offset:49664\n"
      "v_mfma_f32_16x16x32_bf16 v[184:187], a[44:47], a[172:175], v[184:187]\n"
      "s_add_u32 s93, s93, 65536\n"
      "v_exp_f32 v165, v165\n"
      "v_mfma_f32_16x16x32_bf16 v[188:191], a[60:63], a[172:175], v[216:219]\n"
      "ds_read_b64_tr_b16 a[118:119], v235 offset:53760\n"
      "v_cvt_pk_bf16_f32 v194, v164, v165\n"
      "v_mfma_f32_16x16x32_bf16 v[188:191], a[56:59], a[168:171], v[188:191]\n"
      "v_exp_f32 v166, v166\n"
      "v_mfma_f32_16x16x32_bf16 v[188:191], a[52:55], a[164:167], v[188:191]\n"
      "s_add_u32 m0, s23, 69632\n"
      "ds_read_b64_tr_b16 a[120:121], v234 offset:49920\n"
      "ds_read_b64_tr_b16 a[122:123], v234 offset:54016\n"
      "v_mfma_f32_16x16x32_bf16 v[188:191], a[48:51], a[160:163], v[188:191]\n"
      "buffer_load_dwordx4 v250, s[48:51], s93 offen lds\n"
      "v_exp_f32 v167, v167\n"
      "v_smfmac_f32_16x16x64_bf16 a[216:219], a[208:211], v[200:207], v247\n"
      "s_sub_u32 s93, s93, 61440\n"
      "v_exp_f32 v168, v168\n"
      "v_mfma_f32_16x16x32_bf16 v[0:3], v[200:203], a[64:67], v[0:3]\n"
      "s_add_u32 m0, s23, 73728\n"
      "ds_read_b64_tr_b16 a[124:125], v235 offset:49920\n"
      "ds_read_b64_tr_b16 a[160:161], v245 offset:0\n"
      "v_mfma_f32_16x16x32_bf16 v[0:3], v[204:207], a[96:99], v[0:3]\n"
      "buffer_load_dwordx4 v250, s[48:51], s93 offen lds\n"
      "v_mfma_f32_16x16x32_bf16 v[4:7], v[200:203], a[68:71], v[4:7]\n"
      "s_add_u32 s93, s93, 65536\n"
      "v_exp_f32 v169, v169\n"
      "v_mfma_f32_16x16x32_bf16 v[4:7], v[204:207], a[100:103], v[4:7]\n"
      "s_waitcnt lgkmcnt(2)\n"
      "ds_read_b64_tr_b16 a[126:127], v235 offset:54016\n"
      "v_cvt_pk_bf16_f32 v195, v166, v167\n"
      "v_accvgpr_read_b32 v212, a216\n"
      "v_accvgpr_read_b32 v213, a217\n"
      "v_mfma_f32_16x16x32_bf16 v[8:11], v[200:203], a[72:75], v[8:11]\n"
      "s_add_u32 m0, s23, 77824\n"
      "v_exp_f32 v170, v170\n"
      "v_add_f32 v212, v212, v213\n"
      "v_subrev_u32 v213, 0x12800000, v212\n"
      "v_mfma_f32_16x16x32_bf16 v[8:11], v[204:207], a[104:107], v[8:11]\n"
      "buffer_load_dwordx4 v250, s[48:51], s93 offen lds\n"
      "v_cvt_pk_bf16_f32 v196, v168, v169\n"
      "v_max_u32 v237, v237, v213\n"
      "v_mfma_f32_16x16x32_bf16 v[12:15], v[200:203], a[76:79], v[12:15]\n"
      "s_sub_u32 s93, s93, 61440\n"
      "ds_read_b64_tr_b16 a[162:163], v245 offset:4096\n"
      "v_exp_f32 v171, v171\n"
      "v_mfma_f32_16x16x4_f32 v[208:211], v212, v236, 0\n"
      "v_mfma_f32_16x16x32_bf16 v[12:15], v[204:207], a[108:111], v[12:15]\n"
      "ds_read_b64_tr_b16 a[164:165], v246 offset:0\n"
      "v_cvt_pk_bf16_f32 v197, v170, v171\n"
      "v_mfma_f32_16x16x32_bf16 v[16:19], v[200:203], a[80:83], v[16:19]\n"
      "ds_read_b64_tr_b16 a[166:167], v246 offset:4096\n"
      "v_exp_f32 v172, v172\n"
      "v_mfma_f32_16x16x32_bf16 v[16:19], v[204:207], a[112:115], v[16:19]\n"
      "ds_read_b64_tr_b16 a[168:169], v245 offset:8192\n"
      "ds_read_b64_tr_b16 a[170:171], v245 offset:12288\n"
      "v_mfma_f32_16x16x32_bf16 v[20:23], v[200:203], a[84:87], v[20:23]\n"
      "ds_read_b64_tr_b16 a[172:173], v246 offset:8192\n"
      "v_exp_f32 v173, v173\n"
      "v_mfma_f32_16x16x32_bf16 v[20:23], v[204:207], a[116:119], v[20:23]\n"
      "ds_read_b64_tr_b16 a[174:175], v246 offset:12288\n"
      "v_cvt_pk_bf16_f32 v198, v172, v173\n"
      "v_rcp_f32 v208, v208\n"
      "v_mfma_f32_16x16x32_bf16 v[24:27], v[200:203], a[88:91], v[24:27]\n"
      "v_exp_f32 v174, v174\n"
      "v_rcp_f32 v209, v209\n"
      "v_mfma_f32_16x16x32_bf16 v[24:27], v[204:207], a[120:123], v[24:27]\n"
      "v_exp_f32 v175, v175\n"
      "s_nop 0\n"
      "v_cvt_pk_bf16_f32 v199, v174, v175\n"
      "v_mfma_f32_16x16x32_bf16 v[28:31], v[200:203], a[92:95], v[28:31]\n"
      "s_waitcnt lgkmcnt(4)\n"
      "v_rcp_f32 v210, v210\n"
      "v_rcp_f32 v211, v211\n"
      "v_mfma_f32_16x16x32_bf16 v[28:31], v[204:207], a[124:127], v[28:31]\n"
      "s_waitcnt vmcnt(12) lgkmcnt(0)\n"
      "v_pk_mul_f32 v[0:1], v[0:1], v[208:209]\n"
      "v_pk_mul_f32 v[2:3], v[2:3], v[210:211]\n"
      "v_pk_mul_f32 v[4:5], v[4:5], v[208:209]\n"
      "v_pk_mul_f32 v[6:7], v[6:7], v[210:211]\n"
      "v_pk_mul_f32 v[8:9], v[8:9], v[208:209]\n"
      "v_pk_mul_f32 v[10:11], v[10:11], v[210:211]\n"
      "v_pk_mul_f32 v[12:13], v[12:13], v[208:209]\n"
      "v_pk_mul_f32 v[14:15], v[14:15], v[210:211]\n"
      "v_pk_mul_f32 v[16:17], v[16:17], v[208:209]\n"
      "v_pk_mul_f32 v[18:19], v[18:19], v[210:211]\n"
      "v_pk_mul_f32 v[20:21], v[20:21], v[208:209]\n"
      "v_pk_mul_f32 v[22:23], v[22:23], v[210:211]\n"
      "v_pk_mul_f32 v[24:25], v[24:25], v[208:209]\n"
      "v_pk_mul_f32 v[26:27], v[26:27], v[210:211]\n"
      "v_pk_mul_f32 v[28:29], v[28:29], v[208:209]\n"
      "v_pk_mul_f32 v[30:31], v[30:31], v[210:211]\n"
      "v_smfmac_f32_16x16x64_bf16 a[216:219], a[212:215], v[192:199], v247\n"
      "s_barrier\n"
      "v_cvt_pk_bf16_f32 v212, v0, v4\n"
      "v_cvt_pk_bf16_f32 v213, v8, v12\n"
      "v_cvt_pk_bf16_f32 v214, v16, v20\n"
      "v_cvt_pk_bf16_f32 v215, v24, v28\n"
      "v_mfma_f32_16x16x32_bf16 v[32:35], v[192:195], a[64:67], v[32:35]\n"
      "buffer_store_dwordx4 v[212:215], v228, s[36:39], s84 offen sc1\n"
      "v_cvt_pk_bf16_f32 v216, v1, v5\n"
      "v_cvt_pk_bf16_f32 v217, v9, v13\n"
      "v_cvt_pk_bf16_f32 v218, v17, v21\n"
      "v_cvt_pk_bf16_f32 v219, v25, v29\n"
      "v_mfma_f32_16x16x32_bf16 v[32:35], v[196:199], a[96:99], v[32:35]\n"
      "buffer_store_dwordx4 v[216:219], v229, s[36:39], s84 offen sc1\n"
      "v_cvt_pk_bf16_f32 v212, v2, v6\n"
      "v_cvt_pk_bf16_f32 v213, v10, v14\n"
      "v_cvt_pk_bf16_f32 v214, v18, v22\n"
      "v_cvt_pk_bf16_f32 v215, v26, v30\n"
      "v_mfma_f32_16x16x32_bf16 v[36:39], v[192:195], a[68:71], v[36:39]\n"
      "buffer_store_dwordx4 v[212:215], v230, s[36:39], s84 offen sc1\n"
      "v_cvt_pk_bf16_f32 v216, v3, v7\n"
      "v_cvt_pk_bf16_f32 v217, v11, v15\n"
      "v_cvt_pk_bf16_f32 v218, v19, v23\n"
      "v_cvt_pk_bf16_f32 v219, v27, v31\n"
      "v_mfma_f32_16x16x32_bf16 v[36:39], v[196:199], a[100:103], v[36:39]\n"
      "buffer_store_dwordx4 v[216:219], v231, s[36:39], s84 offen sc1\n"
      "v_exp_f32 v176, v176\n"
      "v_mfma_f32_16x16x32_bf16 v[40:43], v[192:195], a[72:75], v[40:43]\n"
      "v_accvgpr_read_b32 v212, a218\n"
      "v_accvgpr_read_b32 v213, a219\n"
      "v_add_f32 v212, v212, v213\n"
      "v_subrev_u32 v213, 0x12800000, v212\n"
      "v_mfma_f32_16x16x32_bf16 v[40:43], v[196:199], a[104:107], v[40:43]\n"
      "v_max_u32 v237, v237, v213\n"
      "v_cmp_le_u32 vcc, 0x3b000000, v237\n"
      "v_exp_f32 v177, v177\n"
      "v_mfma_f32_16x16x32_bf16 v[44:47], v[192:195], a[76:79], v[44:47]\n"
      "s_or_b32 s73, vcc_lo, vcc_hi\n"
      "s_cselect_b32 s20, 1, 3\n"
      "s_lshr_b32 s21, s20, 1\n"
      "v_mfma_f32_16x16x4_f32 v[208:211], v212, v236, 0\n"
      "v_exp_f32 v178, v178\n"
      "v_mfma_f32_16x16x32_bf16 v[44:47], v[196:199], a[108:111], v[44:47]\n"
      "v_cvt_pk_bf16_f32 v200, v176, v177\n"
      "v_exp_f32 v179, v179\n"
      "v_mfma_f32_16x16x32_bf16 v[48:51], v[192:195], a[80:83], v[48:51]\n"
      "v_exp_f32 v180, v180\n"
      "v_cvt_pk_bf16_f32 v201, v178, v179\n"
      "v_mfma_f32_16x16x32_bf16 v[48:51], v[196:199], a[112:115], v[48:51]\n"
      "v_exp_f32 v181, v181\n"
      "v_exp_f32 v182, v182\n"
      "v_mfma_f32_16x16x32_bf16 v[52:55], v[192:195], a[84:87], v[52:55]\n"
      "s_nop 0\n"
      "v_rcp_f32 v208, v208\n"
      "v_rcp_f32 v209, v209\n"
      "v_mfma_f32_16x16x32_bf16 v[52:55], v[196:199], a[116:119], v[52:55]\n"
      "v_rcp_f32 v210, v210\n"
      "v_rcp_f32 v211, v211\n"
      "v_mfma_f32_16x16x32_bf16 v[56:59], v[192:195], a[88:91], v[56:59]\n"
      "v_cvt_pk_bf16_f32 v202, v180, v181\n"
      "v_exp_f32 v183, v183\n"
      "v_mfma_f32_16x16x32_bf16 v[56:59], v[196:199], a[120:123], v[56:59]\n"
      "v_exp_f32 v184, v184\n"
      "v_cvt_pk_bf16_f32 v203, v182, v183\n"
      "v_mfma_f32_16x16x32_bf16 v[60:63], v[192:195], a[92:95], v[60:63]\n"
      "v_exp_f32 v185, v185\n"
      "v_exp_f32 v186, v186\n"
      "v_mfma_f32_16x16x32_bf16 v[60:63], v[196:199], a[124:127], v[60:63]\n"
      "v_pk_mul_f32 v[32:33], v[32:33], v[208:209]\n"
      "v_pk_mul_f32 v[34:35], v[34:35], v[210:211]\n"
      "v_pk_mul_f32 v[36:37], v[36:37], v[208:209]\n"
      "v_pk_mul_f32 v[38:39], v[38:39], v[210:211]\n"
      "v_pk_mul_f32 v[40:41], v[40:41], v[208:209]\n"
      "v_pk_mul_f32 v[42:43], v[42:43], v[210:211]\n"
      "v_pk_mul_f32 v[44:45], v[44:45], v[208:209]\n"
      "v_pk_mul_f32 v[46:47], v[46:47], v[210:211]\n"
      "v_pk_mul_f32 v[48:49], v[48:49], v[208:209]\n"
      "v_pk_mul_f32 v[50:51], v[50:51], v[210:211]\n"
      "v_pk_mul_f32 v[52:53], v[52:53], v[208:209]\n"
      "v_pk_mul_f32 v[54:55], v[54:55], v[210:211]\n"
      "v_pk_mul_f32 v[56:57], v[56:57], v[208:209]\n"
      "v_pk_mul_f32 v[58:59], v[58:59], v[210:211]\n"
      "v_pk_mul_f32 v[60:61], v[60:61], v[208:209]\n"
      "v_pk_mul_f32 v[62:63], v[62:63], v[210:211]\n"
      "v_mfma_f32_16x16x32_bf16 v[160:163], a[32:35], a[192:195], v[224:227]\n"
      "v_cvt_pk_bf16_f32 v212, v32, v36\n"
      "v_cvt_pk_bf16_f32 v213, v40, v44\n"
      "v_cvt_pk_bf16_f32 v214, v48, v52\n"
      "v_cvt_pk_bf16_f32 v215, v56, v60\n"
      "v_mfma_f32_16x16x32_bf16 v[160:163], a[36:39], a[196:199], v[160:163]\n"
      "buffer_store_dwordx4 v[212:215], v228, s[36:39], s85 offen sc1\n"
      "v_cvt_pk_bf16_f32 v216, v33, v37\n"
      "v_cvt_pk_bf16_f32 v217, v41, v45\n"
      "v_cvt_pk_bf16_f32 v218, v49, v53\n"
      "v_cvt_pk_bf16_f32 v219, v57, v61\n"
      "v_mfma_f32_16x16x32_bf16 v[160:163], a[40:43], a[200:203], v[160:163]\n"
      "buffer_store_dwordx4 v[216:219], v229, s[36:39], s85 offen sc1\n"
      "v_cvt_pk_bf16_f32 v212, v34, v38\n"
      "v_cvt_pk_bf16_f32 v213, v42, v46\n"
      "v_cvt_pk_bf16_f32 v214, v50, v54\n"
      "v_cvt_pk_bf16_f32 v215, v58, v62\n"
      "v_mfma_f32_16x16x32_bf16 v[160:163], a[44:47], a[204:207], v[160:163]\n"
      "buffer_store_dwordx4 v[212:215], v230, s[36:39], s85 offen sc1\n"
      "v_cvt_pk_bf16_f32 v216, v35, v39\n"
      "v_cvt_pk_bf16_f32 v217, v43, v47\n"
      "v_cvt_pk_bf16_f32 v218, v51, v55\n"
      "v_cvt_pk_bf16_f32 v219, v59, v63\n"
      "v_mfma_f32_16x16x32_bf16 v[164:167], a[60:63], a[204:207], v[224:227]\n"
      "buffer_store_dwordx4 v[216:219], v231, s[36:39], s85 offen sc1\n"
      "v_cvt_pk_bf16_f32 v204, v184, v185\n"
      "v_exp_f32 v187, v187\n"
      "v_mfma_f32_16x16x32_bf16 v[164:167], a[56:59], a[200:203], v[164:167]\n"
      "s_add_u32 m0, s23, 131072\n"
      "v_exp_f32 v188, v188\n"
      "v_cvt_pk_bf16_f32 v205, v186, v187\n"
      "v_mfma_f32_16x16x32_bf16 v[164:167], a[52:55], a[196:199], v[164:167]\n"
      "buffer_load_dwordx4 v250, s[48:51], s93 offen lds\n"
      "v_exp_f32 v189, v189\n"
      "v_mfma_f32_16x16x32_bf16 v[164:167], a[48:51], a[192:195], v[164:167]\n"
      "s_add_u32 s93, s93, 65536\n"
      "s_add_u32 m0, s23, 135168\n"
      "v_exp_f32 v190, v190\n"
      "v_cvt_pk_bf16_f32 v206, v188, v189\n"
      "v_mfma_f32_16x16x32_bf16 v[176:179], a[0:3], a[176:179], v[220:223]\n"
      "buffer_load_dwordx4 v250, s[48:51], s93 offen lds\n"
      "v_exp_f32 v191, v191\n"
      "v_mfma_f32_16x16x32_bf16 v[176:179], a[4:7], a[180:183], v[176:179]\n"
      "s_sub_u32 s93, s93, 61440\n"
      "s_add_u32 m0, s23, 139264\n"
      "v_cvt_pk_bf16_f32 v207, v190, v191\n"
      "v_mfma_f32_16x16x32_bf16 v[176:179], a[8:11], a[184:187], v[176:179]\n"
      "buffer_load_dwordx4 v250, s[48:51], s93 offen lds\n"
      "v_mfma_f32_16x16x32_bf16 v[176:179], a[12:15], a[188:191], v[176:179]\n"
      "s_add_u32 s93, s93, 65536\n"
      "s_add_u32 m0, s23, 143360\n"
      "v_exp_f32 v160, v160\n"
      "v_mfma_f32_16x16x32_bf16 v[180:183], a[28:31], a[188:191], v[220:223]\n"
      "buffer_load_dwordx4 v250, s[48:51], s93 offen lds\n"
      "v_mfma_f32_16x16x32_bf16 v[180:183], a[24:27], a[184:187], v[180:183]\n"
      "s_add_u32 s93, s93, 53248\n"
      "s_and_b32 s93, s93, 0xfffff\n"
      "v_exp_f32 v161, v161\n"
      "v_mfma_f32_16x16x32_bf16 v[180:183], a[20:23], a[180:183], v[180:183]\n"
      "v_add_f32 v240, v240, v160\n"
      "v_mfma_f32_16x16x32_bf16 v[180:183], a[16:19], a[176:179], v[180:183]\n"
      "v_exp_f32 v162, v162\n"
      "v_mfma_f32_16x16x32_bf16 v[184:187], a[32:35], a[176:179], v[220:223]\n"
      "ds_read_b64_tr_b16 a[192:193], v245 offset:16384\n"
      "v_add_f32 v240, v240, v161\n"
      "v_cvt_pk_bf16_f32 v192, v160, v161\n"
      "v_mfma_f32_16x16x32_bf16 v[184:187], a[36:39], a[180:183], v[184:187]\n"
      "ds_read_b64_tr_b16 a[194:195], v245 offset:20480\n"
      "v_exp_f32 v163, v163\n"
      "v_mfma_f32_16x16x32_bf16 v[184:187], a[40:43], a[184:187], v[184:187]\n"
      "ds_read_b64_tr_b16 a[196:197], v246 offset:16384\n"
      "v_add_f32 v240, v240, v162\n"
      "v_mfma_f32_16x16x32_bf16 v[184:187], a[44:47], a[188:191], v[184:187]\n"
      "ds_read_b64_tr_b16 a[198:199], v246 offset:20480\n"
      "v_exp_f32 v164, v164\n"
      "v_mfma_f32_16x16x32_bf16 v[188:191], a[60:63], a[188:191], v[220:223]\n"
      "ds_read_b64_tr_b16 a[200:201], v245 offset:24576\n"
      "v_add_f32 v240, v240, v163\n"
      "v_cvt_pk_bf16_f32 v193, v162, v163\n"
      "v_mfma_f32_16x16x32_bf16 v[188:191], a[56:59], a[184:187], v[188:191]\n"
      "v_exp_f32 v165, v165\n"
      "v_mfma_f32_16x16x32_bf16 v[188:191], a[52:55], a[180:183], v[188:191]\n"
      "ds_read_b64_tr_b16 a[202:203], v245 offset:28672\n"
      "v_add_f32 v240, v240, v164\n"
      "v_mfma_f32_16x16x32_bf16 v[188:191], a[48:51], a[176:179], v[188:191]\n"
      "v_exp_f32 v166, v166\n"
      "v_smfmac_f32_16x16x64_bf16 a[220:223], a[208:211], v[200:207], v247\n"
      "v_add_f32 v240, v240, v165\n"
      "v_mfma_f32_16x16x32_bf16 v[64:67], v[200:203], a[64:67], v[64:67]\n"
      "ds_read_b64_tr_b16 a[204:205], v246 offset:24576\n"
      "v_cvt_pk_bf16_f32 v194, v164, v165\n"
      "v_mfma_f32_16x16x32_bf16 v[64:67], v[204:207], a[96:99], v[64:67]\n"
      "v_exp_f32 v167, v167\n"
      "v_mfma_f32_16x16x32_bf16 v[68:71], v[200:203], a[68:71], v[68:71]\n"
      "ds_read_b64_tr_b16 a[206:207], v246 offset:28672\n"
      "v_add_f32 v240, v240, v166\n"
      "v_mfma_f32_16x16x32_bf16 v[68:71], v[204:207], a[100:103], v[68:71]\n"
      "v_add_f32 v240, v240, v167\n"
      "v_cvt_pk_bf16_f32 v195, v166, v167\n"
      "v_exp_f32 v176, v176\n"
      "v_mfma_f32_16x16x32_bf16 v[72:75], v[200:203], a[72:75], v[72:75]\n"
      "v_exp_f32 v177, v177\n"
      "v_exp_f32 v178, v178\n"
      "v_mfma_f32_16x16x32_bf16 v[72:75], v[204:207], a[104:107], v[72:75]\n"
      "v_exp_f32 v179, v179\n"
      "v_exp_f32 v180, v180\n"
      "v_mfma_f32_16x16x32_bf16 v[76:79], v[200:203], a[76:79], v[76:79]\n"
      "v_exp_f32 v181, v181\n"
      "v_exp_f32 v182, v182\n"
      "v_mfma_f32_16x16x32_bf16 v[76:79], v[204:207], a[108:111], v[76:79]\n"
      "v_exp_f32 v183, v183\n"
      "v_exp_f32 v184, v184\n"
      "v_mfma_f32_16x16x32_bf16 v[80:83], v[200:203], a[80:83], v[80:83]\n"
      "v_exp_f32 v185, v185\n"
      "v_exp_f32 v186, v186\n"
      "v_mfma_f32_16x16x32_bf16 v[80:83], v[204:207], a[112:115], v[80:83]\n"
      "v_exp_f32 v187, v187\n"
      "v_exp_f32 v188, v188\n"
      "v_mfma_f32_16x16x32_bf16 v[84:87], v[200:203], a[84:87], v[84:87]\n"
      "v_exp_f32 v189, v189\n"
      "v_exp_f32 v190, v190\n"
      "v_mfma_f32_16x16x32_bf16 v[84:87], v[204:207], a[116:119], v[84:87]\n"
      "v_exp_f32 v191, v191\n"
      "v_accvgpr_read_b32 v212, a220\n"
      "v_accvgpr_read_b32 v213, a221\n"
      "v_mfma_f32_16x16x32_bf16 v[88:91], v[200:203], a[88:91], v[88:91]\n"
      "ds_read_b64_tr_b16 a[16:17], v243 offset:16384\n"
      "v_add_f32 v212, v212, v213\n"
      "v_subrev_u32 v213, 0x12800000, v212\n"
      "v_max_u32 v237, v237, v213\n"
      "s_waitcnt vmcnt(4)\n"
      "s_add_u32 s73, s91, s21\n"
      "v_mov_b32 v234, s73\n"
      "buffer_store_dword v234, v249, s[36:39], s89 offen sc1\n"
      "v_mfma_f32_16x16x32_bf16 v[88:91], v[204:207], a[120:123], v[88:91]\n"
      "ds_read_b64_tr_b16 a[0:1], v241 offset:16384\n"
      "v_mfma_f32_16x16x4_f32 v[208:211], v212, v236, 0\n"
      "v_mfma_f32_16x16x32_bf16 v[92:95], v[200:203], a[92:95], v[92:95]\n"
      "ds_read_b64_tr_b16 a[2:3], v241 offset:20480\n"
      "ds_read_b64_tr_b16 a[4:5], v242 offset:16384\n"
      "ds_read_b64_tr_b16 a[18:19], v243 offset:20480\n"
      "v_mfma_f32_16x16x32_bf16 v[92:95], v[204:207], a[124:127], v[92:95]\n"
      "ds_read_b64_tr_b16 a[20:21], v244 offset:16384\n"
      "v_cvt_pk_bf16_f32 v200, v176, v177\n"
      "v_cvt_pk_bf16_f32 v201, v178, v179\n"
      "v_cvt_pk_bf16_f32 v202, v180, v181\n"
      "v_cvt_pk_bf16_f32 v203, v182, v183\n"
      "v_mfma_f32_16x16x32_bf16 v[128:131], v[192:195], a[96:99], v[128:131]\n"
      "ds_read_b64_tr_b16 a[22:23], v244 offset:20480\n"
      "v_cvt_pk_bf16_f32 v204, v184, v185\n"
      "v_cvt_pk_bf16_f32 v205, v186, v187\n"
      "v_cvt_pk_bf16_f32 v206, v188, v189\n"
      "v_cvt_pk_bf16_f32 v207, v190, v191\n"
      "v_mfma_f32_16x16x32_bf16 v[132:135], v[192:195], a[100:103], v[132:135]\n"
      "ds_read_b64_tr_b16 a[24:25], v243 offset:24576\n"
      "v_rcp_f32 v208, v208\n"
      "v_rcp_f32 v209, v209\n"
      "v_mfma_f32_16x16x32_bf16 v[136:139], v[192:195], a[104:107], v[136:139]\n"
      "ds_read_b64_tr_b16 a[26:27], v243 offset:28672\n"
      "v_rcp_f32 v210, v210\n"
      "v_rcp_f32 v211, v211\n"
      "v_mfma_f32_16x16x32_bf16 v[140:143], v[192:195], a[108:111], v[140:143]\n"
      "ds_read_b64_tr_b16 a[28:29], v244 offset:24576\n"
      "v_pk_mul_f32 v[64:65], v[64:65], v[208:209]\n"
      "v_pk_mul_f32 v[66:67], v[66:67], v[210:211]\n"
      "v_pk_mul_f32 v[68:69], v[68:69], v[208:209]\n"
      "v_pk_mul_f32 v[70:71], v[70:71], v[210:211]\n"
      "v_pk_mul_f32 v[72:73], v[72:73], v[208:209]\n"
      "v_pk_mul_f32 v[74:75], v[74:75], v[210:211]\n"
      "v_pk_mul_f32 v[76:77], v[76:77], v[208:209]\n"
      "v_pk_mul_f32 v[78:79], v[78:79], v[210:211]\n"
      "v_pk_mul_f32 v[80:81], v[80:81], v[208:209]\n"
      "v_pk_mul_f32 v[82:83], v[82:83], v[210:211]\n"
      "v_pk_mul_f32 v[84:85], v[84:85], v[208:209]\n"
      "v_pk_mul_f32 v[86:87], v[86:87], v[210:211]\n"
      "v_pk_mul_f32 v[88:89], v[88:89], v[208:209]\n"
      "v_pk_mul_f32 v[90:91], v[90:91], v[210:211]\n"
      "v_pk_mul_f32 v[92:93], v[92:93], v[208:209]\n"
      "v_pk_mul_f32 v[94:95], v[94:95], v[210:211]\n"
      "v_mfma_f32_16x16x32_bf16 v[144:147], v[192:195], a[112:115], v[144:147]\n"
      "ds_read_b64_tr_b16 a[30:31], v244 offset:28672\n"
      "v_cvt_pk_bf16_f32 v212, v64, v68\n"
      "v_cvt_pk_bf16_f32 v213, v72, v76\n"
      "v_cvt_pk_bf16_f32 v214, v80, v84\n"
      "v_cvt_pk_bf16_f32 v215, v88, v92\n"
      "v_mfma_f32_16x16x32_bf16 v[148:151], v[192:195], a[116:119], v[148:151]\n"
      "buffer_store_dwordx4 v[212:215], v228, s[36:39], s86 offen sc1\n"
      "ds_read_b64_tr_b16 a[32:33], v241 offset:32768\n"
      "v_cvt_pk_bf16_f32 v216, v65, v69\n"
      "v_cvt_pk_bf16_f32 v217, v73, v77\n"
      "v_cvt_pk_bf16_f32 v218, v81, v85\n"
      "v_cvt_pk_bf16_f32 v219, v89, v93\n"
      "v_mfma_f32_16x16x4_f32 v[24:27], v240, v236, 0\n"
      "v_mfma_f32_16x16x32_bf16 v[152:155], v[192:195], a[120:123], v[152:155]\n"
      "buffer_store_dwordx4 v[216:219], v229, s[36:39], s86 offen sc1\n"
      "ds_read_b64_tr_b16 a[6:7], v242 offset:20480\n"
      "v_cvt_pk_bf16_f32 v212, v66, v70\n"
      "v_cvt_pk_bf16_f32 v213, v74, v78\n"
      "v_cvt_pk_bf16_f32 v214, v82, v86\n"
      "v_cvt_pk_bf16_f32 v215, v90, v94\n"
      "v_mfma_f32_16x16x32_bf16 v[156:159], v[192:195], a[124:127], v[156:159]\n"
      "buffer_store_dwordx4 v[212:215], v230, s[36:39], s86 offen sc1\n"
      "ds_read_b64_tr_b16 a[8:9], v241 offset:24576\n"
      "v_cvt_pk_bf16_f32 v216, v67, v71\n"
      "v_cvt_pk_bf16_f32 v217, v75, v79\n"
      "v_cvt_pk_bf16_f32 v218, v83, v87\n"
      "v_cvt_pk_bf16_f32 v219, v91, v95\n"
      "v_smfmac_f32_16x16x64_bf16 a[220:223], a[212:215], v[200:207], v247\n"
      "buffer_store_dwordx4 v[216:219], v231, s[36:39], s86 offen sc1\n"
      "ds_read_b64_tr_b16 a[10:11], v241 offset:28672\n"
      "v_mfma_f32_16x16x32_bf16 v[96:99], v[200:203], a[64:67], v[96:99]\n"
      "s_mul_i32 s74, s61, 0x12800000\n"
      "ds_read_b64_tr_b16 a[12:13], v242 offset:24576\n"
      "ds_read_b64_tr_b16 a[14:15], v242 offset:28672\n"
      "ds_read_b64_tr_b16 a[34:35], v241 offset:36864\n"
      "v_max_u32 v24, s74, v24\n"
      "v_subrev_u32 v24, 0x12800000, v24\n"
      "v_mfma_f32_16x16x32_bf16 v[96:99], v[204:207], a[96:99], v[96:99]\n"
      "ds_read_b64_tr_b16 a[36:37], v242 offset:32768\n"
      "v_max_u32 v25, s74, v25\n"
      "v_subrev_u32 v25, 0x12800000, v25\n"
      "v_max_u32 v26, s74, v26\n"
      "v_subrev_u32 v26, 0x12800000, v26\n"
      "v_mfma_f32_16x16x32_bf16 v[100:103], v[200:203], a[68:71], v[100:103]\n"
      "ds_read_b64_tr_b16 a[38:39], v242 offset:36864\n"
      "v_max_u32 v27, s74, v27\n"
      "v_subrev_u32 v27, 0x12800000, v27\n"
      "v_max3_u32 v237, v237, v24, v25\n"
      "v_max3_u32 v237, v237, v26, v27\n"
      "v_mfma_f32_16x16x32_bf16 v[100:103], v[204:207], a[100:103], v[100:103]\n"
      "ds_read_b64_tr_b16 a[40:41], v241 offset:40960\n"
      "ds_read_b64_tr_b16 a[42:43], v241 offset:45056\n"
      "v_mfma_f32_16x16x32_bf16 v[104:107], v[200:203], a[72:75], v[104:107]\n"
      "ds_read_b64_tr_b16 a[44:45], v242 offset:40960\n"
      "ds_read_b64_tr_b16 a[46:47], v242 offset:45056\n"
      "v_mfma_f32_16x16x32_bf16 v[104:107], v[204:207], a[104:107], v[104:107]\n"
      "v_accvgpr_read_b32 v224, a222\n"
      "v_accvgpr_read_b32 v225, a223\n"
      "v_add_f32 v224, v224, v225\n"
      "v_subrev_u32 v225, 0x12800000, v224\n"
      "v_max_u32 v237, v237, v225\n"
      "v_mfma_f32_16x16x32_bf16 v[108:111], v[200:203], a[76:79], v[108:111]\n"
      "v_cmp_le_u32 vcc, 0x3b000000, v237\n"
      "v_add_u32 v0, s23, v248\n"
      "v_mfma_f32_16x16x32_bf16 v[108:111], v[204:207], a[108:111], v[108:111]\n"
      "s_or_b32 s73, vcc_lo, vcc_hi\n"
      "v_lshrrev_b32 v1, 1, v0\n"
      "v_mov_b32 v3, s73\n"
      "v_mov_b32 v2, v240\n"
      "v_add_u32 v0, 0x20000, v1\n"
      "v_mfma_f32_16x16x32_bf16 v[112:115], v[200:203], a[80:83], v[112:115]\n"
      "ds_write_b64 v0, v[2:3] offset:16384\n"
      "v_mfma_f32_16x16x32_bf16 v[112:115], v[204:207], a[112:115], v[112:115]\n"
      "ds_write_b64 v1, v[130:131] offset:16384\n"
      "v_mfma_f32_16x16x32_bf16 v[116:119], v[200:203], a[84:87], v[116:119]\n"
      "ds_write_b64 v1, v[134:135] offset:18432\n"
      "v_mfma_f32_16x16x32_bf16 v[116:119], v[204:207], a[116:119], v[116:119]\n"
      "ds_write_b64 v1, v[138:139] offset:20480\n"
      "v_mfma_f32_16x16x32_bf16 v[120:123], v[200:203], a[88:91], v[120:123]\n"
      "ds_write_b64 v1, v[142:143] offset:22528\n"
      "v_mfma_f32_16x16x32_bf16 v[120:123], v[204:207], a[120:123], v[120:123]\n"
      "ds_write_b64 v1, v[146:147] offset:24576\n"
      "v_mfma_f32_16x16x32_bf16 v[124:127], v[200:203], a[92:95], v[124:127]\n"
      "ds_write_b64 v1, v[150:151] offset:26624\n"
      "v_mfma_f32_16x16x32_bf16 v[124:127], v[204:207], a[124:127], v[124:127]\n"
      "ds_write_b64 v1, v[154:155] offset:28672\n"
      "ds_write_b64 v1, v[158:159] offset:30720\n"
      "v_lshrrev_b32 v4, 4, v255\n"
      "v_lshlrev_b32 v4, 9, v4\n"
      "v_add_u32 v4, 0x20000, v4\n"
      "v_xor_b32 v0, 0x210, v0\n"
      "v_xor_b32 v1, 0x200, v1\n"
      "v_mfma_f32_16x16x4_f32 v[220:223], v224, v236, 0\n"
      "buffer_load_dword v235, v253, s[36:39], 0 offen sc1\n"
      "s_waitcnt vmcnt(6) lgkmcnt(0)\n"
      "s_barrier\n"
      "ds_read_b32 v4, v4 offset:16388\n"
      "ds_read_b32 v2, v0 offset:16384\n"
      "ds_read_b64 v[8:9], v1 offset:16384\n"
      "ds_read_b64 v[10:11], v1 offset:18432\n"
      "ds_read_b64 v[12:13], v1 offset:20480\n"
      "ds_read_b64 v[14:15], v1 offset:22528\n"
      "ds_read_b64 v[16:17], v1 offset:24576\n"
      "ds_read_b64 v[18:19], v1 offset:26624\n"
      "ds_read_b64 v[20:21], v1 offset:28672\n"
      "ds_read_b64 v[22:23], v1 offset:30720\n"
      "s_waitcnt lgkmcnt(9)\n"
      "v_cmp_ne_u32 vcc, 0, v4\n"
      "s_or_b32 s73, vcc_lo, vcc_hi\n"
      "s_cselect_b32 s72, s20, 0\n"
      "s_cbranch_scc1 END_%=\n"
      "v_rcp_f32 v220, v220\n"
      "v_rcp_f32 v221, v221\n"
      "v_rcp_f32 v222, v222\n"
      "v_rcp_f32 v223, v223\n"
      "v_pk_mul_f32 v[96:97], v[96:97], v[220:221]\n"
      "v_pk_mul_f32 v[100:101], v[100:101], v[220:221]\n"
      "v_pk_mul_f32 v[104:105], v[104:105], v[220:221]\n"
      "v_pk_mul_f32 v[108:109], v[108:109], v[220:221]\n"
      "v_pk_mul_f32 v[112:113], v[112:113], v[220:221]\n"
      "v_pk_mul_f32 v[116:117], v[116:117], v[220:221]\n"
      "v_pk_mul_f32 v[120:121], v[120:121], v[220:221]\n"
      "v_pk_mul_f32 v[124:125], v[124:125], v[220:221]\n"
      "v_cvt_pk_bf16_f32 v176, v96, v100\n"
      "v_cvt_pk_bf16_f32 v177, v104, v108\n"
      "v_cvt_pk_bf16_f32 v178, v112, v116\n"
      "v_cvt_pk_bf16_f32 v179, v120, v124\n"
      "v_cvt_pk_bf16_f32 v180, v97, v101\n"
      "v_cvt_pk_bf16_f32 v181, v105, v109\n"
      "v_cvt_pk_bf16_f32 v182, v113, v117\n"
      "v_cvt_pk_bf16_f32 v183, v121, v125\n"
      "v_pk_mul_f32 v[98:99], v[98:99], v[222:223]\n"
      "v_pk_mul_f32 v[102:103], v[102:103], v[222:223]\n"
      "v_pk_mul_f32 v[106:107], v[106:107], v[222:223]\n"
      "v_pk_mul_f32 v[110:111], v[110:111], v[222:223]\n"
      "v_pk_mul_f32 v[114:115], v[114:115], v[222:223]\n"
      "v_pk_mul_f32 v[118:119], v[118:119], v[222:223]\n"
      "v_pk_mul_f32 v[122:123], v[122:123], v[222:223]\n"
      "v_pk_mul_f32 v[126:127], v[126:127], v[222:223]\n"
      "v_cvt_pk_bf16_f32 v184, v98, v102\n"
      "v_cvt_pk_bf16_f32 v185, v106, v110\n"
      "v_cvt_pk_bf16_f32 v186, v114, v118\n"
      "v_cvt_pk_bf16_f32 v187, v122, v126\n"
      "v_cvt_pk_bf16_f32 v188, v99, v103\n"
      "v_cvt_pk_bf16_f32 v189, v107, v111\n"
      "v_cvt_pk_bf16_f32 v190, v115, v119\n"
      "v_cvt_pk_bf16_f32 v191, v123, v127\n"
      "buffer_store_dwordx4 v[176:179], v228, s[36:39], s87 offen sc1\n"
      "buffer_store_dwordx4 v[180:183], v229, s[36:39], s87 offen sc1\n"
      "buffer_store_dwordx4 v[184:187], v230, s[36:39], s87 offen sc1\n"
      "buffer_store_dwordx4 v[188:191], v231, s[36:39], s87 offen sc1\n"
      "ds_read_b64_tr_b16 a[48:49], v243 offset:32768\n"
      "ds_read_b64_tr_b16 a[50:51], v243 offset:36864\n"
      "ds_read_b64_tr_b16 a[52:53], v244 offset:32768\n"
      "ds_read_b64_tr_b16 a[54:55], v244 offset:36864\n"
      "ds_read_b64_tr_b16 a[56:57], v243 offset:40960\n"
      "ds_read_b64_tr_b16 a[58:59], v243 offset:45056\n"
      "ds_read_b64_tr_b16 a[60:61], v244 offset:40960\n"
      "ds_read_b64_tr_b16 a[62:63], v244 offset:45056\n"
      "ds_read_b64_tr_b16 a[176:177], v241 offset:0\n"
      "ds_read_b64_tr_b16 a[178:179], v241 offset:4096\n"
      "ds_read_b64_tr_b16 a[180:181], v242 offset:0\n"
      "ds_read_b64_tr_b16 a[182:183], v242 offset:4096\n"
      "ds_read_b64_tr_b16 a[184:185], v241 offset:8192\n"
      "ds_read_b64_tr_b16 a[186:187], v241 offset:12288\n"
      "ds_read_b64_tr_b16 a[188:189], v242 offset:8192\n"
      "ds_read_b64_tr_b16 a[190:191], v242 offset:12288\n"
      "ds_read_b64_tr_b16 a[208:209], v241 offset:49152\n"
      "ds_read_b64_tr_b16 a[210:211], v241 offset:53248\n"
      "ds_read_b64_tr_b16 a[212:213], v242 offset:49152\n"
      "ds_read_b64_tr_b16 a[214:215], v242 offset:53248\n"
      "ds_read_b64_tr_b16 a[216:217], v241 offset:57344\n"
      "ds_read_b64_tr_b16 a[218:219], v241 offset:61440\n"
      "ds_read_b64_tr_b16 a[220:221], v242 offset:57344\n"
      "ds_read_b64_tr_b16 a[222:223], v242 offset:61440\n"
      "s_waitcnt lgkmcnt(0)\n"
      "v_add_f32 v2, v2, v240\n"
      "s_mul_i32 s73, s61, 0x220\n"
      "s_nop 0\n"
      "v_mfma_f32_16x16x4_f32 v[208:211], v2, v236, 0\n"
      "v_pk_add_f32 v[128:129], v[128:129], v[8:9]\n"
      "v_pk_add_f32 v[132:133], v[132:133], v[10:11]\n"
      "v_pk_add_f32 v[136:137], v[136:137], v[12:13]\n"
      "v_pk_add_f32 v[140:141], v[140:141], v[14:15]\n"
      "v_pk_add_f32 v[144:145], v[144:145], v[16:17]\n"
      "v_pk_add_f32 v[148:149], v[148:149], v[18:19]\n"
      "v_pk_add_f32 v[152:153], v[152:153], v[20:21]\n"
      "v_pk_add_f32 v[156:157], v[156:157], v[22:23]\n"
      "v_xor_b32 v232, s73, v228\n"
      "v_xor_b32 v233, s73, v229\n"
      "s_nop 1\n"
      "v_rcp_f32 v208, v208\n"
      "v_rcp_f32 v209, v209\n"
      "s_nop 0\n"
      "v_pk_mul_f32 v[128:129], v[128:129], v[208:209]\n"
      "v_pk_mul_f32 v[132:133], v[132:133], v[208:209]\n"
      "v_pk_mul_f32 v[136:137], v[136:137], v[208:209]\n"
      "v_pk_mul_f32 v[140:141], v[140:141], v[208:209]\n"
      "v_pk_mul_f32 v[144:145], v[144:145], v[208:209]\n"
      "v_pk_mul_f32 v[148:149], v[148:149], v[208:209]\n"
      "v_pk_mul_f32 v[152:153], v[152:153], v[208:209]\n"
      "v_pk_mul_f32 v[156:157], v[156:157], v[208:209]\n"
      "v_cvt_pk_bf16_f32 v212, v128, v132\n"
      "v_cvt_pk_bf16_f32 v213, v136, v140\n"
      "v_cvt_pk_bf16_f32 v214, v144, v148\n"
      "v_cvt_pk_bf16_f32 v215, v152, v156\n"
      "v_cvt_pk_bf16_f32 v216, v129, v133\n"
      "v_cvt_pk_bf16_f32 v217, v137, v141\n"
      "v_cvt_pk_bf16_f32 v218, v145, v149\n"
      "v_cvt_pk_bf16_f32 v219, v153, v157\n"
      "buffer_store_dwordx4 v[212:215], v232, s[36:39], s88 offen sc1\n"
      "buffer_store_dwordx4 v[216:219], v233, s[36:39], s88 offen sc1\n"
      "v_and_b32 v172, 15, v255\n"
      "v_lshrrev_b32 v173, 4, v255\n"
      "v_xor_b32 v160, v172, v173\n"
      "v_lshlrev_b32 v160, 4, v160\n"
      "v_lshl_add_u32 v160, v172, 8, v160\n"
      "v_xor_b32 v161, 64, v160\n"
      "v_xor_b32 v162, 128, v160\n"
      "v_xor_b32 v163, 192, v160\n"
      "v_add_u32 v164, 0x10000, v160\n"
      "v_add_u32 v165, 0x10000, v161\n"
      "v_add_u32 v166, 0x10000, v162\n"
      "v_add_u32 v167, 0x10000, v163\n"
      "v_add_u32 v168, 0x20000, v160\n"
      "v_add_u32 v169, 0x20000, v161\n"
      "v_add_u32 v170, 0x20000, v162\n"
      "v_add_u32 v171, 0x20000, v163\n"
      "v_lshlrev_b32 v172, 10, v172\n"
      "v_bfrev_b32 v173, v173\n"
      "v_lshrrev_b32 v173, 26, v173\n"
      "v_add_u32 v172, v173, v172\n"
      "s_waitcnt vmcnt(6)\n"
      "v_sub_u32 v235, v235, v254\n"
      "v_cmp_gt_u32 vcc, 1, v235\n"
      "s_and_b32 s73, vcc_lo, 0xffff\n"
      "s_cbranch_scc0 POLLED0_%=\n"
      "s_mov_b32 s95, 0x80000\n"
      "POLL0_%=:\n"
      "buffer_load_dword v235, v253, s[36:39], 0 offen sc1\n"
      "s_waitcnt vmcnt(0)\n"
      "v_sub_u32 v235, v235, v254\n"
      "v_cmp_gt_u32 vcc, 1, v235\n"
      "s_and_b32 s73, vcc_lo, 0xffff\n"
      "s_cbranch_scc0 POLLED0_%=\n"
      "s_sleep 1\n"
      "s_sub_u32 s95, s95, 1\n"
      "s_cbranch_scc0 POLL0_%=\n"
      "POLLED0_%=:\n"
      "s_add_u32 m0, s71, 81920\n"
      "s_add_u32 s8, s36, s62\n"
      "s_addc_u32 s9, s37, 0\n"
      "s_nop 0\n"
      "global_load_lds_dwordx4 v248, s[8:9] sc1\n"
      "global_load_lds_dwordx4 v248, s[8:9] offset:1024 sc1\n"
      "global_load_lds_dwordx4 v248, s[8:9] offset:2048 sc1\n"
      "global_load_lds_dwordx4 v248, s[8:9] offset:3072 sc1\n"
      "s_add_u32 m0, s71, 32768\n"
      "s_add_u32 s8, s36, s63\n"
      "s_addc_u32 s9, s37, 0\n"
      "s_nop 0\n"
      "global_load_lds_dwordx4 v248, s[8:9] sc1\n"
      "global_load_lds_dwordx4 v248, s[8:9] offset:1024 sc1\n"
      "global_load_lds_dwordx4 v248, s[8:9] offset:2048 sc1\n"
      "global_load_lds_dwordx4 v248, s[8:9] offset:3072 sc1\n"
      "s_waitcnt vmcnt(0) lgkmcnt(0)\n"
      "s_barrier\n"
      "ds_read_b128 v[0:3], v164 offset:16384\n"
      "s_add_u32 m0, s71, 147456\n"
      "s_xor_b32 s62, s62, 0x4000\n"
      "s_add_u32 s8, s36, s62\n"
      "s_addc_u32 s9, s37, 0\n"
      "ds_read_b128 v[4:7], v165 offset:16384\n"
      "global_load_lds_dwordx4 v248, s[8:9] sc1\n"
      "ds_read_b128 v[8:11], v166 offset:16384\n"
      "global_load_lds_dwordx4 v248, s[8:9] offset:1024 sc1\n"
      "ds_read_b128 v[12:15], v167 offset:16384\n"
      "global_load_lds_dwordx4 v248, s[8:9] offset:2048 sc1\n"
      "ds_read_b128 v[16:19], v160 offset:32768\n"
      "global_load_lds_dwordx4 v248, s[8:9] offset:3072 sc1\n"
      "s_add_u32 m0, s71, 16384\n"
      "s_xor_b32 s63, s63, 0x4000\n"
      "s_add_u32 s8, s36, s63\n"
      "s_addc_u32 s9, s37, 0\n"
      "ds_read_b128 v[20:23], v161 offset:32768\n"
      "global_load_lds_dwordx4 v248, s[8:9] sc1\n"
      "ds_read_b128 v[24:27], v162 offset:32768\n"
      "global_load_lds_dwordx4 v248, s[8:9] offset:1024 sc1\n"
      "ds_read_b128 v[28:31], v163 offset:32768\n"
      "global_load_lds_dwordx4 v248, s[8:9] offset:2048 sc1\n"
      "ds_read_b128 v[32:35], v164 offset:20480\n"
      "global_load_lds_dwordx4 v248, s[8:9] offset:3072 sc1\n"
      "ds_read_b128 v[36:39], v165 offset:20480\n"
      "s_waitcnt lgkmcnt(3)\n"
      "v_mfma_f32_16x16x32_bf16 v[192:195], a[224:227], v[0:3], 0\n"
      "ds_read_b128 v[40:43], v166 offset:20480\n"
      "ds_read_b64_tr_b16 a[64:65], v243 offset:0\n"
      "s_waitcnt vmcnt(16)\n"
      "s_add_u32 s73, s91, 2\n"
      "v_mov_b32 v234, s73\n"
      "buffer_store_dword v234, v249, s[36:39], s89 offen sc1\n"
      "v_mfma_f32_16x16x32_bf16 v[192:195], a[232:235], v[4:7], v[192:195]\n"
      "ds_read_b64_tr_b16 a[66:67], v243 offset:4096\n"
      "ds_read_b64_tr_b16 a[68:69], v244 offset:0\n"
      "v_mfma_f32_16x16x32_bf16 v[192:195], a[240:243], v[8:11], v[192:195]\n"
      "ds_read_b128 v[44:47], v167 offset:20480\n"
      "ds_read_b64_tr_b16 a[70:71], v244 offset:4096\n"
      "v_mfma_f32_16x16x32_bf16 v[192:195], a[248:251], v[12:15], v[192:195]\n"
      "ds_read_b64_tr_b16 a[72:73], v243 offset:8192\n"
      "ds_read_b64_tr_b16 a[74:75], v243 offset:12288\n"
      "v_mfma_f32_16x16x32_bf16 v[196:199], a[228:231], v[0:3], 0\n"
      "s_add_u32 m0, s23, 114688\n"
      "ds_read_b128 v[48:51], v160 offset:36864\n"
      "ds_read_b64_tr_b16 a[76:77], v244 offset:8192\n"
      "v_mfma_f32_16x16x32_bf16 v[196:199], a[236:239], v[4:7], v[196:199]\n"
      "buffer_load_dwordx4 v250, s[48:51], s93 offen lds\n"
      "ds_read_b64_tr_b16 a[78:79], v244 offset:12288\n"
      "v_mfma_f32_16x16x32_bf16 v[196:199], a[244:247], v[8:11], v[196:199]\n"
      "s_add_u32 s93, s93, 65536\n"
      "s_add_u32 m0, s23, 118784\n"
      "ds_read_b128 v[52:55], v161 offset:36864\n"
      "ds_read_b64_tr_b16 a[80:81], v245 offset:0\n"
      "v_mfma_f32_16x16x32_bf16 v[196:199], a[252:255], v[12:15], v[196:199]\n"
      "buffer_load_dwordx4 v250, s[48:51], s93 offen lds\n"
      "ds_read_b64_tr_b16 a[82:83], v245 offset:4096\n"
      "v_mfma_f32_16x16x32_bf16 v[200:203], a[224:227], v[16:19], 0\n"
      "s_waitcnt lgkmcnt(8)\n"
      "s_sub_u32 s93, s93, 61440\n"
      "s_add_u32 m0, s23, 122880\n"
      "ds_read_b128 v[56:59], v162 offset:36864\n"
      "ds_read_b64_tr_b16 a[84:85], v246 offset:0\n"
      "v_mfma_f32_16x16x32_bf16 v[200:203], a[232:235], v[20:23], v[200:203]\n"
      "buffer_load_dwordx4 v250, s[48:51], s93 offen lds\n"
      "ds_read_b64_tr_b16 a[86:87], v246 offset:4096\n"
      "v_mfma_f32_16x16x32_bf16 v[200:203], a[240:243], v[24:27], v[200:203]\n"
      "s_add_u32 s93, s93, 65536\n"
      "s_add_u32 m0, s23, 126976\n"
      "ds_read_b128 v[60:63], v163 offset:36864\n"
      "ds_read_b64_tr_b16 a[88:89], v245 offset:8192\n"
      "v_mfma_f32_16x16x32_bf16 v[200:203], a[248:251], v[28:31], v[200:203]\n"
      "buffer_load_dwordx4 v250, s[48:51], s93 offen lds\n"
      "ds_read_b64_tr_b16 a[90:91], v245 offset:12288\n"
      "v_mfma_f32_16x16x32_bf16 v[204:207], a[228:231], v[16:19], 0\n"
      "s_sub_u32 s93, s93, 61440\n"
      "s_add_u32 m0, s23, 98304\n"
      "ds_read_b128 v[0:3], v164 offset:24576\n"
      "ds_read_b64_tr_b16 a[92:93], v246 offset:8192\n"
      "v_mfma_f32_16x16x32_bf16 v[204:207], a[236:239], v[20:23], v[204:207]\n"
      "buffer_load_dwordx4 v250, s[48:51], s93 offen lds\n"
      "ds_read_b64_tr_b16 a[94:95], v246 offset:12288\n"
      "v_mfma_f32_16x16x32_bf16 v[204:207], a[244:247], v[24:27], v[204:207]\n"
      "s_add_u32 s93, s93, 65536\n"
      "s_add_u32 m0, s23, 102400\n"
      "ds_read_b128 v[4:7], v165 offset:24576\n"
      "v_mfma_f32_16x16x32_bf16 v[204:207], a[252:255], v[28:31], v[204:207]\n"
      "buffer_load_dwordx4 v250, s[48:51], s93 offen lds\n"
      "v_mfma_f32_16x16x32_bf16 v[192:195], a[128:131], v[32:35], v[192:195]\n"
      "s_sub_u32 s93, s93, 61440\n"
      "s_add_u32 m0, s23, 106496\n"
      "ds_read_b128 v[8:11], v166 offset:24576\n"
      "v_mfma_f32_16x16x32_bf16 v[192:195], a[136:139], v[36:39], v[192:195]\n"
      "buffer_load_dwordx4 v250, s[48:51], s93 offen lds\n"
      "v_mfma_f32_16x16x32_bf16 v[192:195], a[144:147], v[40:43], v[192:195]\n"
      "s_add_u32 s93, s93, 65536\n"
      "s_add_u32 m0, s23, 110592\n"
      "ds_read_b128 v[12:15], v167 offset:24576\n"
      "v_mfma_f32_16x16x32_bf16 v[192:195], a[152:155], v[44:47], v[192:195]\n"
      "buffer_load_dwordx4 v250, s[48:51], s93 offen lds\n"
      "v_mfma_f32_16x16x32_bf16 v[196:199], a[132:135], v[32:35], v[196:199]\n"
      "s_add_u32 s93, s93, 53248\n"
      "s_and_b32 s93, s93, 0xfffff\n"
      "ds_read_b128 v[16:19], v160 offset:40960\n"
      "v_mfma_f32_16x16x32_bf16 v[196:199], a[140:143], v[36:39], v[196:199]\n"
      "v_mfma_f32_16x16x32_bf16 v[196:199], a[148:151], v[40:43], v[196:199]\n"
      "ds_read_b128 v[20:23], v161 offset:40960\n"
      "v_mfma_f32_16x16x32_bf16 v[196:199], a[156:159], v[44:47], v[196:199]\n"
      "s_waitcnt lgkmcnt(3)\n"
      "v_mfma_f32_16x16x32_bf16 v[200:203], a[128:131], v[48:51], v[200:203]\n"
      "ds_read_b128 v[24:27], v162 offset:40960\n"
      "v_mfma_f32_16x16x32_bf16 v[200:203], a[136:139], v[52:55], v[200:203]\n"
      "v_mfma_f32_16x16x32_bf16 v[200:203], a[144:147], v[56:59], v[200:203]\n"
      "ds_read_b128 v[28:31], v163 offset:40960\n"
      "v_mfma_f32_16x16x32_bf16 v[200:203], a[152:155], v[60:63], v[200:203]\n"
      "v_mfma_f32_16x16x32_bf16 v[204:207], a[132:135], v[48:51], v[204:207]\n"
      "ds_read_b128 v[32:35], v164 offset:28672\n"
      "v_mfma_f32_16x16x32_bf16 v[204:207], a[140:143], v[52:55], v[204:207]\n"
      "v_mfma_f32_16x16x32_bf16 v[204:207], a[148:151], v[56:59], v[204:207]\n"
      "ds_read_b128 v[36:39], v165 offset:28672\n"
      "v_mfma_f32_16x16x32_bf16 v[204:207], a[156:159], v[60:63], v[204:207]\n"
      "s_add_u32 s8, s36, s64\n"
      "s_addc_u32 s9, s37, 0\n"
      "v_mfma_f32_16x16x32_bf16 v[192:195], a[160:163], v[0:3], v[192:195]\n"
      "s_add_u32 m0, s71, 0\n"
      "ds_read_b128 v[40:43], v166 offset:28672\n"
      "v_mfma_f32_16x16x32_bf16 v[192:195], a[168:171], v[4:7], v[192:195]\n"
      "global_load_lds_dwordx4 v248, s[8:9] sc1\n"
      "v_mfma_f32_16x16x32_bf16 v[192:195], a[192:195], v[8:11], v[192:195]\n"
      "s_waitcnt lgkmcnt(2)\n"
      "ds_read_b128 v[44:47], v167 offset:28672\n"
      "v_mfma_f32_16x16x32_bf16 v[192:195], a[200:203], v[12:15], v[192:195]\n"
      "v_mfma_f32_16x16x32_bf16 v[196:199], a[164:167], v[0:3], v[196:199]\n"
      "ds_read_b128 v[48:51], v160 offset:45056\n"
      "global_load_lds_dwordx4 v248, s[8:9] offset:1024 sc1\n"
      "v_mfma_f32_16x16x32_bf16 v[196:199], a[172:175], v[4:7], v[196:199]\n"
      "v_mfma_f32_16x16x32_bf16 v[196:199], a[196:199], v[8:11], v[196:199]\n"
      "ds_read_b128 v[52:55], v161 offset:45056\n"
      "v_mfma_f32_16x16x32_bf16 v[196:199], a[204:207], v[12:15], v[196:199]\n"
      "global_load_lds_dwordx4 v248, s[8:9] offset:2048 sc1\n"
      "v_mfma_f32_16x16x32_bf16 v[200:203], a[160:163], v[16:19], v[200:203]\n"
      "ds_read_b128 v[56:59], v162 offset:45056\n"
      "v_mfma_f32_16x16x32_bf16 v[200:203], a[168:171], v[20:23], v[200:203]\n"
      "v_mfma_f32_16x16x32_bf16 v[200:203], a[192:195], v[24:27], v[200:203]\n"
      "s_waitcnt vmcnt(12) lgkmcnt(2)\n"
      "ds_read_b128 v[60:63], v163 offset:45056\n"
      "global_load_lds_dwordx4 v248, s[8:9] offset:3072 sc1\n"
      "v_mfma_f32_16x16x32_bf16 v[200:203], a[200:203], v[28:31], v[200:203]\n"
      "s_barrier\n"
      "s_xor_b32 s64, s64, 0x4000\n"
      "s_add_u32 s8, s36, s64\n"
      "s_addc_u32 s9, s37, 0\n"
      "v_mfma_f32_16x16x32_bf16 v[204:207], a[164:167], v[16:19], v[204:207]\n"
      "s_add_u32 m0, s71, 49152\n"
      "ds_read_b128 v[0:3], v168 offset:16384\n"
      "v_mfma_f32_16x16x32_bf16 v[204:207], a[172:175], v[20:23], v[204:207]\n"
      "global_load_lds_dwordx4 v248, s[8:9] sc1\n"
      "v_mfma_f32_16x16x32_bf16 v[204:207], a[196:199], v[24:27], v[204:207]\n"
      "ds_read_b128 v[4:7], v169 offset:16384\n"
      "v_mfma_f32_16x16x32_bf16 v[204:207], a[204:207], v[28:31], v[204:207]\n"
      "v_mfma_f32_16x16x32_bf16 v[192:195], a[0:3], v[32:35], v[192:195]\n"
      "ds_read_b128 v[8:11], v170 offset:16384\n"
      "global_load_lds_dwordx4 v248, s[8:9] offset:1024 sc1\n"
      "v_mfma_f32_16x16x32_bf16 v[192:195], a[8:11], v[36:39], v[192:195]\n"
      "v_mfma_f32_16x16x32_bf16 v[192:195], a[16:19], v[40:43], v[192:195]\n"
      "ds_read_b128 v[12:15], v171 offset:16384\n"
      "v_mfma_f32_16x16x32_bf16 v[192:195], a[24:27], v[44:47], v[192:195]\n"
      "global_load_lds_dwordx4 v248, s[8:9] offset:2048 sc1\n"
      "v_mfma_f32_16x16x32_bf16 v[196:199], a[4:7], v[32:35], v[196:199]\n"
      "ds_read_b128 v[16:19], v160 offset:16384\n"
      "v_mfma_f32_16x16x32_bf16 v[196:199], a[12:15], v[36:39], v[196:199]\n"
      "v_mfma_f32_16x16x32_bf16 v[196:199], a[20:23], v[40:43], v[196:199]\n"
      "ds_read_b128 v[20:23], v161 offset:16384\n"
      "global_load_lds_dwordx4 v248, s[8:9] offset:3072 sc1\n"
      "v_mfma_f32_16x16x32_bf16 v[196:199], a[28:31], v[44:47], v[196:199]\n"
      "v_mfma_f32_16x16x32_bf16 v[200:203], a[0:3], v[48:51], v[200:203]\n"
      "s_waitcnt lgkmcnt(2)\n"
      "ds_read_b128 v[24:27], v162 offset:16384\n"
      "v_mfma_f32_16x16x32_bf16 v[200:203], a[8:11], v[52:55], v[200:203]\n"
      "v_mfma_f32_16x16x32_bf16 v[200:203], a[16:19], v[56:59], v[200:203]\n"
      "ds_read_b128 v[28:31], v163 offset:16384\n"
      "v_mfma_f32_16x16x32_bf16 v[200:203], a[24:27], v[60:63], v[200:203]\n"
      "v_mfma_f32_16x16x32_bf16 v[204:207], a[4:7], v[48:51], v[204:207]\n"
      "ds_read_b128 v[32:35], v168 offset:20480\n"
      "v_mfma_f32_16x16x32_bf16 v[204:207], a[12:15], v[52:55], v[204:207]\n"
      "v_mfma_f32_16x16x32_bf16 v[204:207], a[20:23], v[56:59], v[204:207]\n"
      "ds_read_b128 v[36:39], v169 offset:20480\n"
      "v_mfma_f32_16x16x32_bf16 v[204:207], a[28:31], v[60:63], v[204:207]\n"
      "v_mfma_f32_16x16x32_bf16 v[192:195], a[32:35], v[0:3], v[192:195]\n"
      "ds_read_b128 v[40:43], v170 offset:20480\n"
      "v_mfma_f32_16x16x32_bf16 v[192:195], a[40:43], v[4:7], v[192:195]\n"
      "v_mfma_f32_16x16x32_bf16 v[192:195], a[48:51], v[8:11], v[192:195]\n"
      "s_waitcnt vmcnt(8)\n"
      "ds_read_b128 v[44:47], v171 offset:20480\n"
      "v_mfma_f32_16x16x32_bf16 v[192:195], a[56:59], v[12:15], v[192:195]\n"
      "s_barrier\n"
      "v_mfma_f32_16x16x32_bf16 v[196:199], a[36:39], v[0:3], v[196:199]\n"
      "ds_read_b128 v[48:51], v160 offset:20480\n"
      "ds_read_b64_tr_b16 a[96:97], v243 offset:49152\n"
      "v_mfma_f32_16x16x32_bf16 v[196:199], a[44:47], v[4:7], v[196:199]\n"
      "ds_read_b64_tr_b16 a[98:99], v243 offset:53248\n"
      "ds_read_b64_tr_b16 a[100:101], v244 offset:49152\n"
      "v_mfma_f32_16x16x32_bf16 v[196:199], a[52:55], v[8:11], v[196:199]\n"
      "ds_read_b128 v[52:55], v161 offset:20480\n"
      "ds_read_b64_tr_b16 a[102:103], v244 offset:53248\n"
      "v_mfma_f32_16x16x32_bf16 v[196:199], a[60:63], v[12:15], v[196:199]\n"
      "s_waitcnt lgkmcnt(7)\n"
      "ds_read_b64_tr_b16 a[104:105], v243 offset:57344\n"
      "ds_read_b64_tr_b16 a[106:107], v243 offset:61440\n"
      "v_mfma_f32_16x16x32_bf16 v[200:203], a[32:35], v[16:19], v[200:203]\n"
      "ds_read_b128 v[56:59], v162 offset:20480\n"
      "ds_read_b64_tr_b16 a[108:109], v244 offset:57344\n"
      "v_mfma_f32_16x16x32_bf16 v[200:203], a[40:43], v[20:23], v[200:203]\n"
      "s_add_u32 s8, s36, s65\n"
      "s_addc_u32 s9, s37, 0\n"
      "ds_read_b64_tr_b16 a[110:111], v244 offset:61440\n"
      "ds_read_b64_tr_b16 a[112:113], v243 offset:32768\n"
      "v_mfma_f32_16x16x32_bf16 v[200:203], a[48:51], v[24:27], v[200:203]\n"
      "s_add_u32 m0, s71, 81920\n"
      "ds_read_b128 v[60:63], v163 offset:20480\n"
      "ds_read_b64_tr_b16 a[114:115], v243 offset:36864\n"
      "v_mfma_f32_16x16x32_bf16 v[200:203], a[56:59], v[28:31], v[200:203]\n"
      "global_load_lds_dwordx4 v248, s[8:9] sc1\n"
      "ds_read_b64_tr_b16 a[116:117], v244 offset:32768\n"
      "v_mfma_f32_16x16x32_bf16 v[204:207], a[36:39], v[16:19], v[204:207]\n"
      "ds_read_b128 v[0:3], v168 offset:24576\n"
      "ds_read_b64_tr_b16 a[118:119], v244 offset:36864\n"
      "v_mfma_f32_16x16x32_bf16 v[204:207], a[44:47], v[20:23], v[204:207]\n"
      "ds_read_b64_tr_b16 a[120:121], v243 offset:40960\n"
      "ds_read_b64_tr_b16 a[122:123], v243 offset:45056\n"
      "v_mfma_f32_16x16x32_bf16 v[204:207], a[52:55], v[24:27], v[204:207]\n"
      "ds_read_b128 v[4:7], v169 offset:24576\n"
      "ds_read_b64_tr_b16 a[124:125], v244 offset:40960\n"
      "v_mfma_f32_16x16x32_bf16 v[204:207], a[60:63], v[28:31], v[204:207]\n"
      "global_load_lds_dwordx4 v248, s[8:9] offset:1024 sc1\n"
      "ds_read_b64_tr_b16 a[126:127], v244 offset:45056\n"
      "v_mfma_f32_16x16x32_bf16 v[192:195], a[176:179], v[32:35], v[192:195]\n"
      "ds_read_b128 v[8:11], v170 offset:24576\n"
      "v_mfma_f32_16x16x32_bf16 v[192:195], a[184:187], v[36:39], v[192:195]\n"
      "v_mfma_f32_16x16x32_bf16 v[192:195], a[208:211], v[40:43], v[192:195]\n"
      "s_waitcnt lgkmcnt(6)\n"
      "ds_read_b128 v[12:15], v171 offset:24576\n"
      "v_mfma_f32_16x16x32_bf16 v[192:195], a[216:219], v[44:47], v[192:195]\n"
      "global_load_lds_dwordx4 v248, s[8:9] offset:2048 sc1\n"
      "v_mfma_f32_16x16x32_bf16 v[196:199], a[180:183], v[32:35], v[196:199]\n"
      "ds_read_b128 v[16:19], v160 offset:24576\n"
      "v_mfma_f32_16x16x32_bf16 v[196:199], a[188:191], v[36:39], v[196:199]\n"
      "v_mfma_f32_16x16x32_bf16 v[196:199], a[212:215], v[40:43], v[196:199]\n"
      "ds_read_b128 v[20:23], v161 offset:24576\n"
      "v_mfma_f32_16x16x32_bf16 v[196:199], a[220:223], v[44:47], v[196:199]\n"
      "global_load_lds_dwordx4 v248, s[8:9] offset:3072 sc1\n"
      "v_mfma_f32_16x16x32_bf16 v[200:203], a[176:179], v[48:51], v[200:203]\n"
      "ds_read_b128 v[24:27], v162 offset:24576\n"
      "v_mfma_f32_16x16x32_bf16 v[200:203], a[184:187], v[52:55], v[200:203]\n"
      "s_xor_b32 s65, s65, 0x4000\n"
      "s_add_u32 s8, s36, s65\n"
      "s_addc_u32 s9, s37, 0\n"
      "v_mfma_f32_16x16x32_bf16 v[200:203], a[208:211], v[56:59], v[200:203]\n"
      "s_add_u32 m0, s71, 32768\n"
      "ds_read_b128 v[28:31], v163 offset:24576\n"
      "v_mfma_f32_16x16x32_bf16 v[200:203], a[216:219], v[60:63], v[200:203]\n"
      "global_load_lds_dwordx4 v248, s[8:9] sc1\n"
      "v_mfma_f32_16x16x32_bf16 v[204:207], a[180:183], v[48:51], v[204:207]\n"
      "ds_read_b128 v[32:35], v168 offset:28672\n"
      "v_mfma_f32_16x16x32_bf16 v[204:207], a[188:191], v[52:55], v[204:207]\n"
      "v_mfma_f32_16x16x32_bf16 v[204:207], a[212:215], v[56:59], v[204:207]\n"
      "ds_read_b128 v[36:39], v169 offset:28672\n"
      "v_mfma_f32_16x16x32_bf16 v[204:207], a[220:223], v[60:63], v[204:207]\n"
      "global_load_lds_dwordx4 v248, s[8:9] offset:1024 sc1\n"
      "v_mfma_f32_16x16x32_bf16 v[192:195], a[64:67], v[0:3], v[192:195]\n"
      "s_waitcnt lgkmcnt(2)\n"
      "ds_read_b128 v[40:43], v170 offset:28672\n"
      "v_mfma_f32_16x16x32_bf16 v[192:195], a[72:75], v[4:7], v[192:195]\n"
      "buffer_load_dword v235, v253, s[36:39], 0 offen sc1\n"
      "v_mfma_f32_16x16x32_bf16 v[192:195], a[80:83], v[8:11], v[192:195]\n"
      "ds_read_b128 v[44:47], v171 offset:28672\n"
      "v_mfma_f32_16x16x32_bf16 v[192:195], a[88:91], v[12:15], v[192:195]\n"
      "global_load_lds_dwordx4 v248, s[8:9] offset:2048 sc1\n"
      "v_mfma_f32_16x16x32_bf16 v[196:199], a[68:71], v[0:3], v[196:199]\n"
      "ds_read_b128 v[48:51], v160 offset:28672\n"
      "v_mfma_f32_16x16x32_bf16 v[196:199], a[76:79], v[4:7], v[196:199]\n"
      "v_mfma_f32_16x16x32_bf16 v[196:199], a[84:87], v[8:11], v[196:199]\n"
      "ds_read_b128 v[52:55], v161 offset:28672\n"
      "v_mfma_f32_16x16x32_bf16 v[196:199], a[92:95], v[12:15], v[196:199]\n"
      "global_load_lds_dwordx4 v248, s[8:9] offset:3072 sc1\n"
      "v_mfma_f32_16x16x32_bf16 v[200:203], a[64:67], v[16:19], v[200:203]\n"
      "ds_read_b128 v[56:59], v162 offset:28672\n"
      "v_mfma_f32_16x16x32_bf16 v[200:203], a[72:75], v[20:23], v[200:203]\n"
      "v_mfma_f32_16x16x32_bf16 v[200:203], a[80:83], v[24:27], v[200:203]\n"
      "s_waitcnt vmcnt(9) lgkmcnt(3)\n"
      "ds_read_b128 v[60:63], v163 offset:28672\n"
      "v_mfma_f32_16x16x32_bf16 v[200:203], a[88:91], v[28:31], v[200:203]\n"
      "s_barrier\n"
      "v_mfma_f32_16x16x32_bf16 v[204:207], a[68:71], v[16:19], v[204:207]\n"
      "ds_read_b128 v[0:3], v160 offset:0\n"
      "v_mfma_f32_16x16x32_bf16 v[204:207], a[76:79], v[20:23], v[204:207]\n"
      "v_mfma_f32_16x16x32_bf16 v[204:207], a[84:87], v[24:27], v[204:207]\n"
      "ds_read_b128 v[4:7], v161 offset:0\n"
      "v_mfma_f32_16x16x32_bf16 v[204:207], a[92:95], v[28:31], v[204:207]\n"
      "v_mfma_f32_16x16x32_bf16 v[192:195], a[96:99], v[32:35], v[192:195]\n"
      "ds_read_b128 v[8:11], v162 offset:0\n"
      "v_mfma_f32_16x16x32_bf16 v[192:195], a[104:107], v[36:39], v[192:195]\n"
      "v_mfma_f32_16x16x32_bf16 v[192:195], a[112:115], v[40:43], v[192:195]\n"
      "ds_read_b128 v[12:15], v163 offset:0\n"
      "v_mfma_f32_16x16x32_bf16 v[192:195], a[120:123], v[44:47], v[192:195]\n"
      "v_mfma_f32_16x16x32_bf16 v[196:199], a[100:103], v[32:35], v[196:199]\n"
      "ds_read_b128 v[16:19], v160 offset:4096\n"
      "v_mfma_f32_16x16x32_bf16 v[196:199], a[108:111], v[36:39], v[196:199]\n"
      "v_mfma_f32_16x16x32_bf16 v[196:199], a[116:119], v[40:43], v[196:199]\n"
      "ds_read_b128 v[20:23], v161 offset:4096\n"
      "v_mfma_f32_16x16x32_bf16 v[196:199], a[124:127], v[44:47], v[196:199]\n"
      "s_waitcnt lgkmcnt(3)\n"
      "v_mfma_f32_16x16x32_bf16 v[200:203], a[96:99], v[48:51], v[200:203]\n"
      "ds_read_b128 v[24:27], v162 offset:4096\n"
      "v_mfma_f32_16x16x32_bf16 v[200:203], a[104:107], v[52:55], v[200:203]\n"
      "v_mfma_f32_16x16x32_bf16 v[200:203], a[112:115], v[56:59], v[200:203]\n"
      "ds_read_b128 v[28:31], v163 offset:4096\n"
      "v_mfma_f32_16x16x32_bf16 v[200:203], a[120:123], v[60:63], v[200:203]\n"
      "v_mfma_f32_16x16x32_bf16 v[204:207], a[100:103], v[48:51], v[204:207]\n"
      "ds_read_b128 v[32:35], v160 offset:8192\n"
      "v_mfma_f32_16x16x32_bf16 v[204:207], a[108:111], v[52:55], v[204:207]\n"
      "v_cvt_pk_bf16_f32 v192, v192, v193\n"
      "v_cvt_pk_bf16_f32 v193, v194, v195\n"
      "v_mfma_f32_16x16x32_bf16 v[204:207], a[116:119], v[56:59], v[204:207]\n"
      "ds_read_b128 v[36:39], v161 offset:8192\n"
      "v_mfma_f32_16x16x32_bf16 v[204:207], a[124:127], v[60:63], v[204:207]\n"
      "v_cvt_pk_bf16_f32 v194, v196, v197\n"
      "v_cvt_pk_bf16_f32 v195, v198, v199\n"
      "v_mfma_f32_16x16x32_bf16 v[176:179], a[224:227], v[0:3], 0\n"
      "s_waitcnt lgkmcnt(2)\n"
      "ds_read_b128 v[40:43], v162 offset:8192\n"
      "s_waitcnt vmcnt(2)\n"
      "v_sub_u32 v235, v235, v254\n"
      "v_cmp_gt_u32 vcc, 2, v235\n"
      "s_and_b32 s73, vcc_lo, 0xffff\n"
      "s_cbranch_scc0 POLLED1_%=\n"
      "s_mov_b32 s95, 0x80000\n"
      "POLL1_%=:\n"
      "buffer_load_dword v235, v253, s[36:39], 0 offen sc1\n"
      "s_waitcnt vmcnt(0)\n"
      "v_sub_u32 v235, v235, v254\n"
      "v_cmp_gt_u32 vcc, 2, v235\n"
      "s_and_b32 s73, vcc_lo, 0xffff\n"
      "s_cbranch_scc0 POLLED1_%=\n"
      "s_sleep 1\n"
      "s_sub_u32 s95, s95, 1\n"
      "s_cbranch_scc0 POLL1_%=\n"
      "POLLED1_%=:\n"
      "s_add_u32 s73, s91, 4\n"
      "v_mov_b32 v234, s73\n"
      "buffer_store_dword v234, v249, s[36:39], s89 offen sc1\n"
      "v_mfma_f32_16x16x32_bf16 v[176:179], a[232:235], v[4:7], v[176:179]\n"
      "s_add_u32 s8, s36, s66\n"
      "s_addc_u32 s9, s37, 0\n"
      "buffer_store_dwordx4 v[192:195], v248, s[36:39], s40 offen offset:0 sc1\n"
      "v_mfma_f32_16x16x32_bf16 v[176:179], a[240:243], v[8:11], v[176:179]\n"
      "s_add_u32 m0, s71, 65536\n"
      "ds_read_b128 v[44:47], v163 offset:8192\n"
      "v_mfma_f32_16x16x32_bf16 v[176:179], a[248:251], v[12:15], v[176:179]\n"
      "global_load_lds_dwordx4 v248, s[8:9] sc1\n"
      "v_mfma_f32_16x16x32_bf16 v[180:183], a[228:231], v[0:3], 0\n"
      "ds_read_b128 v[48:51], v160 offset:12288\n"
      "v_mfma_f32_16x16x32_bf16 v[180:183], a[236:239], v[4:7], v[180:183]\n"
      "v_cvt_pk_bf16_f32 v200, v200, v201\n"
      "v_cvt_pk_bf16_f32 v201, v202, v203\n"
      "v_mfma_f32_16x16x32_bf16 v[180:183], a[244:247], v[8:11], v[180:183]\n"
      "ds_read_b128 v[52:55], v161 offset:12288\n"
      "v_mfma_f32_16x16x32_bf16 v[180:183], a[252:255], v[12:15], v[180:183]\n"
      "global_load_lds_dwordx4 v248, s[8:9] offset:1024 sc1\n"
      "v_cvt_pk_bf16_f32 v202, v204, v205\n"
      "v_cvt_pk_bf16_f32 v203, v206, v207\n"
      "v_mfma_f32_16x16x32_bf16 v[176:179], a[128:131], v[16:19], v[176:179]\n"
      "ds_read_b128 v[56:59], v162 offset:12288\n"
      "v_mfma_f32_16x16x32_bf16 v[176:179], a[136:139], v[20:23], v[176:179]\n"
      "buffer_store_dwordx4 v[200:203], v248, s[36:39], s40 offen offset:1024 sc1\n"
      "v_mfma_f32_16x16x32_bf16 v[176:179], a[144:147], v[24:27], v[176:179]\n"
      "ds_read_b128 v[60:63], v163 offset:12288\n"
      "v_mfma_f32_16x16x32_bf16 v[176:179], a[152:155], v[28:31], v[176:179]\n"
      "global_load_lds_dwordx4 v248, s[8:9] offset:2048 sc1\n"
      "v_mfma_f32_16x16x32_bf16 v[180:183], a[132:135], v[16:19], v[180:183]\n"
      "ds_read_b128 v[0:3], v160 offset:49152\n"
      "v_mfma_f32_16x16x32_bf16 v[180:183], a[140:143], v[20:23], v[180:183]\n"
      "v_mfma_f32_16x16x32_bf16 v[180:183], a[148:151], v[24:27], v[180:183]\n"
      "s_waitcnt lgkmcnt(2)\n"
      "ds_read_b128 v[4:7], v161 offset:49152\n"
      "v_mfma_f32_16x16x32_bf16 v[180:183], a[156:159], v[28:31], v[180:183]\n"
      "global_load_lds_dwordx4 v248, s[8:9] offset:3072 sc1\n"
      "v_mfma_f32_16x16x32_bf16 v[176:179], a[160:163], v[32:35], v[176:179]\n"
      "ds_read_b128 v[8:11], v162 offset:49152\n"
      "v_mfma_f32_16x16x32_bf16 v[176:179], a[168:171], v[36:39], v[176:179]\n"
      "s_xor_b32 s66, s66, 0x4000\n"
      "s_add_u32 s8, s36, s66\n"
      "s_addc_u32 s9, s37, 0\n"
      "v_mfma_f32_16x16x32_bf16 v[176:179], a[192:195], v[40:43], v[176:179]\n"
      "s_add_u32 m0, s71, 131072\n"
      "ds_read_b128 v[12:15], v163 offset:49152\n"
      "v_mfma_f32_16x16x32_bf16 v[176:179], a[200:203], v[44:47], v[176:179]\n"
      "global_load_lds_dwordx4 v248, s[8:9] sc1\n"
      "v_mfma_f32_16x16x32_bf16 v[180:183], a[164:167], v[32:35], v[180:183]\n"
      "ds_read_b128 v[16:19], v160 offset:53248\n"
      "v_mfma_f32_16x16x32_bf16 v[180:183], a[172:175], v[36:39], v[180:183]\n"
      "v_mfma_f32_16x16x32_bf16 v[180:183], a[196:199], v[40:43], v[180:183]\n"
      "ds_read_b128 v[20:23], v161 offset:53248\n"
      "v_mfma_f32_16x16x32_bf16 v[180:183], a[204:207], v[44:47], v[180:183]\n"
      "global_load_lds_dwordx4 v248, s[8:9] offset:1024 sc1\n"
      "v_mfma_f32_16x16x32_bf16 v[176:179], a[0:3], v[48:51], v[176:179]\n"
      "ds_read_b128 v[24:27], v162 offset:53248\n"
      "v_mfma_f32_16x16x32_bf16 v[176:179], a[8:11], v[52:55], v[176:179]\n"
      "v_mfma_f32_16x16x32_bf16 v[176:179], a[16:19], v[56:59], v[176:179]\n"
      "s_waitcnt lgkmcnt(2)\n"
      "ds_read_b128 v[28:31], v163 offset:53248\n"
      "v_mfma_f32_16x16x32_bf16 v[176:179], a[24:27], v[60:63], v[176:179]\n"
      "global_load_lds_dwordx4 v248, s[8:9] offset:2048 sc1\n"
      "v_mfma_f32_16x16x32_bf16 v[180:183], a[4:7], v[48:51], v[180:183]\n"
      "ds_read_b128 v[32:35], v160 offset:57344\n"
      "v_mfma_f32_16x16x32_bf16 v[180:183], a[12:15], v[52:55], v[180:183]\n"
      "v_mfma_f32_16x16x32_bf16 v[180:183], a[20:23], v[56:59], v[180:183]\n"
      "ds_read_b128 v[36:39], v161 offset:57344\n"
      "v_mfma_f32_16x16x32_bf16 v[180:183], a[28:31], v[60:63], v[180:183]\n"
      "global_load_lds_dwordx4 v248, s[8:9] offset:3072 sc1\n"
      "v_mfma_f32_16x16x32_bf16 v[176:179], a[32:35], v[0:3], v[176:179]\n"
      "ds_read_b128 v[40:43], v162 offset:57344\n"
      "v_mfma_f32_16x16x32_bf16 v[176:179], a[40:43], v[4:7], v[176:179]\n"
      "v_mfma_f32_16x16x32_bf16 v[176:179], a[48:51], v[8:11], v[176:179]\n"
      "ds_read_b128 v[44:47], v163 offset:57344\n"
      "v_mfma_f32_16x16x32_bf16 v[176:179], a[56:59], v[12:15], v[176:179]\n"
      "v_mfma_f32_16x16x32_bf16 v[180:183], a[36:39], v[0:3], v[180:183]\n"
      "ds_read_b128 v[48:51], v160 offset:61440\n"
      "v_mfma_f32_16x16x32_bf16 v[180:183], a[44:47], v[4:7], v[180:183]\n"
      "v_mfma_f32_16x16x32_bf16 v[180:183], a[52:55], v[8:11], v[180:183]\n"
      "ds_read_b128 v[52:55], v161 offset:61440\n"
      "v_mfma_f32_16x16x32_bf16 v[180:183], a[60:63], v[12:15], v[180:183]\n"
      "v_mfma_f32_16x16x32_bf16 v[176:179], a[176:179], v[16:19], v[176:179]\n"
      "s_waitcnt lgkmcnt(2)\n"
      "ds_read_b128 v[56:59], v162 offset:61440\n"
      "v_mfma_f32_16x16x32_bf16 v[176:179], a[184:187], v[20:23], v[176:179]\n"
      "v_mfma_f32_16x16x32_bf16 v[176:179], a[208:211], v[24:27], v[176:179]\n"
      "s_waitcnt vmcnt(11)\n"
      "ds_read_b128 v[60:63], v163 offset:61440\n"
      "v_mfma_f32_16x16x32_bf16 v[176:179], a[216:219], v[28:31], v[176:179]\n"
      "s_barrier\n"
      "v_mfma_f32_16x16x32_bf16 v[180:183], a[180:183], v[16:19], v[180:183]\n"
      "ds_read_b128 v[0:3], v164 offset:16384\n"
      "v_mfma_f32_16x16x32_bf16 v[180:183], a[188:191], v[20:23], v[180:183]\n"
      "v_mfma_f32_16x16x32_bf16 v[180:183], a[212:215], v[24:27], v[180:183]\n"
      "ds_read_b128 v[4:7], v165 offset:16384\n"
      "v_mfma_f32_16x16x32_bf16 v[180:183], a[220:223], v[28:31], v[180:183]\n"
      "v_mfma_f32_16x16x32_bf16 v[176:179], a[64:67], v[32:35], v[176:179]\n"
      "ds_read_b128 v[8:11], v166 offset:16384\n"
      "v_mfma_f32_16x16x32_bf16 v[176:179], a[72:75], v[36:39], v[176:179]\n"
      "v_mfma_f32_16x16x32_bf16 v[176:179], a[80:83], v[40:43], v[176:179]\n"
      "ds_read_b128 v[12:15], v167 offset:16384\n"
      "v_mfma_f32_16x16x32_bf16 v[176:179], a[88:91], v[44:47], v[176:179]\n"
      "v_mfma_f32_16x16x32_bf16 v[180:183], a[68:71], v[32:35], v[180:183]\n"
      "ds_read_b128 v[16:19], v164 offset:20480\n"
      "v_mfma_f32_16x16x32_bf16 v[180:183], a[76:79], v[36:39], v[180:183]\n"
      "v_mfma_f32_16x16x32_bf16 v[180:183], a[84:87], v[40:43], v[180:183]\n"
      "ds_read_b128 v[20:23], v165 offset:20480\n"
      "v_mfma_f32_16x16x32_bf16 v[180:183], a[92:95], v[44:47], v[180:183]\n"
      "s_waitcnt lgkmcnt(3)\n"
      "v_mfma_f32_16x16x32_bf16 v[176:179], a[96:99], v[48:51], v[176:179]\n"
      "ds_read_b128 v[24:27], v166 offset:20480\n"
      "v_mfma_f32_16x16x32_bf16 v[176:179], a[104:107], v[52:55], v[176:179]\n"
      "v_mfma_f32_16x16x32_bf16 v[176:179], a[112:115], v[56:59], v[176:179]\n"
      "ds_read_b128 v[28:31], v167 offset:20480\n"
      "v_mfma_f32_16x16x32_bf16 v[176:179], a[120:123], v[60:63], v[176:179]\n"
      "v_mfma_f32_16x16x32_bf16 v[180:183], a[100:103], v[48:51], v[180:183]\n"
      "ds_read_b128 v[32:35], v164 offset:24576\n"
      "v_mfma_f32_16x16x32_bf16 v[180:183], a[108:111], v[52:55], v[180:183]\n"
      "v_mfma_f32_16x16x32_bf16 v[180:183], a[116:119], v[56:59], v[180:183]\n"
      "ds_read_b128 v[36:39], v165 offset:24576\n"
      "v_mfma_f32_16x16x32_bf16 v[180:183], a[124:127], v[60:63], v[180:183]\n"
      "v_mfma_f32_16x16x32_bf16 v[184:187], a[224:227], v[0:3], 0\n"
      "ds_read_b128 v[40:43], v166 offset:24576\n"
      "v_mfma_f32_16x16x32_bf16 v[184:187], a[232:235], v[4:7], v[184:187]\n"
      "v_mfma_f32_16x16x32_bf16 v[184:187], a[240:243], v[8:11], v[184:187]\n"
      "s_waitcnt lgkmcnt(2)\n"
      "ds_read_b128 v[44:47], v167 offset:24576\n"
      "v_mfma_f32_16x16x32_bf16 v[184:187], a[248:251], v[12:15], v[184:187]\n"
      "v_mfma_f32_16x16x32_bf16 v[188:191], a[228:231], v[0:3], 0\n"
      "ds_read_b128 v[48:51], v164 offset:28672\n"
      "v_mfma_f32_16x16x32_bf16 v[188:191], a[236:239], v[4:7], v[188:191]\n"
      "v_cvt_pk_bf16_f32 v176, v176, v177\n"
      "v_cvt_pk_bf16_f32 v177, v178, v179\n"
      "v_mfma_f32_16x16x32_bf16 v[188:191], a[244:247], v[8:11], v[188:191]\n"
      "ds_read_b128 v[52:55], v165 offset:28672\n"
      "v_mfma_f32_16x16x32_bf16 v[188:191], a[252:255], v[12:15], v[188:191]\n"
      "v_cvt_pk_bf16_f32 v178, v180, v181\n"
      "v_cvt_pk_bf16_f32 v179, v182, v183\n"
      "v_mfma_f32_16x16x32_bf16 v[184:187], a[128:131], v[16:19], v[184:187]\n"
      "ds_read_b128 v[56:59], v166 offset:28672\n"
      "v_mfma_f32_16x16x32_bf16 v[184:187], a[136:139], v[20:23], v[184:187]\n"
      "s_add_u32 s8, s36, s67\n"
      "s_addc_u32 s9, s37, 0\n"
      "buffer_store_dwordx4 v[176:179], v248, s[36:39], s41 offen offset:0 sc1\n"
      "v_mfma_f32_16x16x32_bf16 v[184:187], a[144:147], v[24:27], v[184:187]\n"
      "s_add_u32 m0, s71, 114688\n"
      "ds_read_b128 v[60:63], v167 offset:28672\n"
      "v_mfma_f32_16x16x32_bf16 v[184:187], a[152:155], v[28:31], v[184:187]\n"
      "global_load_lds_dwordx4 v248, s[8:9] sc1\n"
      "v_mfma_f32_16x16x32_bf16 v[188:191], a[132:135], v[16:19], v[188:191]\n"
      "ds_read_b128 v[0:3], v160 offset:32768\n"
      "v_mfma_f32_16x16x32_bf16 v[188:191], a[140:143], v[20:23], v[188:191]\n"
      "v_mfma_f32_16x16x32_bf16 v[188:191], a[148:151], v[24:27], v[188:191]\n"
      "ds_read_b128 v[4:7], v161 offset:32768\n"
      "v_mfma_f32_16x16x32_bf16 v[188:191], a[156:159], v[28:31], v[188:191]\n"
      "v_mfma_f32_16x16x32_bf16 v[184:187], a[160:163], v[32:35], v[184:187]\n"
      "s_waitcnt lgkmcnt(2)\n"
      "ds_read_b128 v[8:11], v162 offset:32768\n"
      "v_mfma_f32_16x16x32_bf16 v[184:187], a[168:171], v[36:39], v[184:187]\n"
      "global_load_lds_dwordx4 v248, s[8:9] offset:1024 sc1\n"
      "v_mfma_f32_16x16x32_bf16 v[184:187], a[192:195], v[40:43], v[184:187]\n"
      "ds_read_b128 v[12:15], v163 offset:32768\n"
      "v_mfma_f32_16x16x32_bf16 v[184:187], a[200:203], v[44:47], v[184:187]\n"
      "v_mfma_f32_16x16x32_bf16 v[188:191], a[164:167], v[32:35], v[188:191]\n"
      "ds_read_b128 v[16:19], v160 offset:36864\n"
      "v_mfma_f32_16x16x32_bf16 v[188:191], a[172:175], v[36:39], v[188:191]\n"
      "v_mfma_f32_16x16x32_bf16 v[188:191], a[196:199], v[40:43], v[188:191]\n"
      "ds_read_b128 v[20:23], v161 offset:36864\n"
      "v_mfma_f32_16x16x32_bf16 v[188:191], a[204:207], v[44:47], v[188:191]\n"
      "global_load_lds_dwordx4 v248, s[8:9] offset:2048 sc1\n"
      "v_mfma_f32_16x16x32_bf16 v[184:187], a[0:3], v[48:51], v[184:187]\n"
      "ds_read_b128 v[24:27], v162 offset:36864\n"
      "v_mfma_f32_16x16x32_bf16 v[184:187], a[8:11], v[52:55], v[184:187]\n"
      "v_mfma_f32_16x16x32_bf16 v[184:187], a[16:19], v[56:59], v[184:187]\n"
      "ds_read_b128 v[28:31], v163 offset:36864\n"
      "v_mfma_f32_16x16x32_bf16 v[184:187], a[24:27], v[60:63], v[184:187]\n"
      "v_mfma_f32_16x16x32_bf16 v[188:191], a[4:7], v[48:51], v[188:191]\n"
      "ds_read_b128 v[32:35], v160 offset:40960\n"
      "v_mfma_f32_16x16x32_bf16 v[188:191], a[12:15], v[52:55], v[188:191]\n"
      "global_load_lds_dwordx4 v248, s[8:9] offset:3072 sc1\n"
      "v_mfma_f32_16x16x32_bf16 v[188:191], a[20:23], v[56:59], v[188:191]\n"
      "ds_read_b128 v[36:39], v161 offset:40960\n"
      "v_mfma_f32_16x16x32_bf16 v[188:191], a[28:31], v[60:63], v[188:191]\n"
      "s_waitcnt lgkmcnt(3)\n"
      "v_mfma_f32_16x16x32_bf16 v[184:187], a[32:35], v[0:3], v[184:187]\n"
      "ds_read_b128 v[40:43], v162 offset:40960\n"
      "v_mfma_f32_16x16x32_bf16 v[184:187], a[40:43], v[4:7], v[184:187]\n"
      "s_xor_b32 s67, s67, 0x4000\n"
      "s_add_u32 s8, s36, s67\n"
      "s_addc_u32 s9, s37, 0\n"
      "v_mfma_f32_16x16x32_bf16 v[184:187], a[48:51], v[8:11], v[184:187]\n"
      "s_add_u32 m0, s71, 98304\n"
      "ds_read_b128 v[44:47], v163 offset:40960\n"
      "v_mfma_f32_16x16x32_bf16 v[184:187], a[56:59], v[12:15], v[184:187]\n"
      "global_load_lds_dwordx4 v248, s[8:9] sc1\n"
      "v_mfma_f32_16x16x32_bf16 v[188:191], a[36:39], v[0:3], v[188:191]\n"
      "ds_read_b128 v[48:51], v160 offset:45056\n"
      "v_mfma_f32_16x16x32_bf16 v[188:191], a[44:47], v[4:7], v[188:191]\n"
      "v_mfma_f32_16x16x32_bf16 v[188:191], a[52:55], v[8:11], v[188:191]\n"
      "ds_read_b128 v[52:55], v161 offset:45056\n"
      "v_mfma_f32_16x16x32_bf16 v[188:191], a[60:63], v[12:15], v[188:191]\n"
      "v_mfma_f32_16x16x32_bf16 v[184:187], a[176:179], v[16:19], v[184:187]\n"
      "ds_read_b128 v[56:59], v162 offset:45056\n"
      "v_mfma_f32_16x16x32_bf16 v[184:187], a[184:187], v[20:23], v[184:187]\n"
      "global_load_lds_dwordx4 v248, s[8:9] offset:1024 sc1\n"
      "v_mfma_f32_16x16x32_bf16 v[184:187], a[208:211], v[24:27], v[184:187]\n"
      "s_waitcnt vmcnt(7) lgkmcnt(2)\n"
      "ds_read_b128 v[60:63], v163 offset:45056\n"
      "v_mfma_f32_16x16x32_bf16 v[184:187], a[216:219], v[28:31], v[184:187]\n"
      "s_barrier\n"
      "v_mfma_f32_16x16x32_bf16 v[188:191], a[180:183], v[16:19], v[188:191]\n"
      "ds_read_b128 v[0:3], v164 offset:0\n"
      "v_mfma_f32_16x16x32_bf16 v[188:191], a[188:191], v[20:23], v[188:191]\n"
      "v_mfma_f32_16x16x32_bf16 v[188:191], a[212:215], v[24:27], v[188:191]\n"
      "ds_read_b128 v[4:7], v165 offset:0\n"
      "v_mfma_f32_16x16x32_bf16 v[188:191], a[220:223], v[28:31], v[188:191]\n"
      "global_load_lds_dwordx4 v248, s[8:9] offset:2048 sc1\n"
      "v_mfma_f32_16x16x32_bf16 v[184:187], a[64:67], v[32:35], v[184:187]\n"
      "ds_read_b128 v[8:11], v166 offset:0\n"
      "v_mfma_f32_16x16x32_bf16 v[184:187], a[72:75], v[36:39], v[184:187]\n"
      "v_mfma_f32_16x16x32_bf16 v[184:187], a[80:83], v[40:43], v[184:187]\n"
      "ds_read_b128 v[12:15], v167 offset:0\n"
      "v_mfma_f32_16x16x32_bf16 v[184:187], a[88:91], v[44:47], v[184:187]\n"
      "v_mfma_f32_16x16x32_bf16 v[188:191], a[68:71], v[32:35], v[188:191]\n"
      "ds_read_b128 v[16:19], v164 offset:4096\n"
      "v_mfma_f32_16x16x32_bf16 v[188:191], a[76:79], v[36:39], v[188:191]\n"
      "global_load_lds_dwordx4 v248, s[8:9] offset:3072 sc1\n"
      "v_mfma_f32_16x16x32_bf16 v[188:191], a[84:87], v[40:43], v[188:191]\n"
      "ds_read_b128 v[20:23], v165 offset:4096\n"
      "v_mfma_f32_16x16x32_bf16 v[188:191], a[92:95], v[44:47], v[188:191]\n"
      "v_mfma_f32_16x16x32_bf16 v[184:187], a[96:99], v[48:51], v[184:187]\n"
      "s_waitcnt lgkmcnt(2)\n"
      "ds_read_b128 v[24:27], v166 offset:4096\n"
      "v_mfma_f32_16x16x32_bf16 v[184:187], a[104:107], v[52:55], v[184:187]\n"
      "v_mfma_f32_16x16x32_bf16 v[184:187], a[112:115], v[56:59], v[184:187]\n"
      "ds_read_b128 v[28:31], v167 offset:4096\n"
      "v_mfma_f32_16x16x32_bf16 v[184:187], a[120:123], v[60:63], v[184:187]\n"
      "v_mfma_f32_16x16x32_bf16 v[188:191], a[100:103], v[48:51], v[188:191]\n"
      "ds_read_b128 v[32:35], v164 offset:8192\n"
      "v_mfma_f32_16x16x32_bf16 v[188:191], a[108:111], v[52:55], v[188:191]\n"
      "v_mfma_f32_16x16x32_bf16 v[188:191], a[116:119], v[56:59], v[188:191]\n"
      "ds_read_b128 v[36:39], v165 offset:8192\n"
      "v_mfma_f32_16x16x32_bf16 v[188:191], a[124:127], v[60:63], v[188:191]\n"
      "v_mfma_f32_16x16x32_bf16 v[176:179], a[224:227], v[0:3], 0\n"
      "ds_read_b128 v[40:43], v166 offset:8192\n"
      "v_mfma_f32_16x16x32_bf16 v[176:179], a[232:235], v[4:7], v[176:179]\n"
      "v_mfma_f32_16x16x32_bf16 v[176:179], a[240:243], v[8:11], v[176:179]\n"
      "ds_read_b128 v[44:47], v167 offset:8192\n"
      "v_mfma_f32_16x16x32_bf16 v[176:179], a[248:251], v[12:15], v[176:179]\n"
      "v_mfma_f32_16x16x32_bf16 v[180:183], a[228:231], v[0:3], 0\n"
      "ds_read_b128 v[48:51], v164 offset:12288\n"
      "v_mfma_f32_16x16x32_bf16 v[180:183], a[236:239], v[4:7], v[180:183]\n"
      "v_cvt_pk_bf16_f32 v184, v184, v185\n"
      "v_cvt_pk_bf16_f32 v185, v186, v187\n"
      "v_mfma_f32_16x16x32_bf16 v[180:183], a[244:247], v[8:11], v[180:183]\n"
      "ds_read_b128 v[52:55], v165 offset:12288\n"
      "v_mfma_f32_16x16x32_bf16 v[180:183], a[252:255], v[12:15], v[180:183]\n"
      "s_waitcnt lgkmcnt(3)\n"
      "v_cvt_pk_bf16_f32 v186, v188, v189\n"
      "v_cvt_pk_bf16_f32 v187, v190, v191\n"
      "v_mfma_f32_16x16x32_bf16 v[176:179], a[128:131], v[16:19], v[176:179]\n"
      "ds_read_b128 v[56:59], v166 offset:12288\n"
      "v_mfma_f32_16x16x32_bf16 v[176:179], a[136:139], v[20:23], v[176:179]\n"
      "s_add_u32 s8, s36, s68\n"
      "s_addc_u32 s9, s37, 0\n"
      "buffer_store_dwordx4 v[184:187], v248, s[36:39], s41 offen offset:1024 sc1\n"
      "v_mfma_f32_16x16x32_bf16 v[176:179], a[144:147], v[24:27], v[176:179]\n"
      "s_add_u32 m0, s71, 147456\n"
      "ds_read_b128 v[60:63], v167 offset:12288\n"
      "v_mfma_f32_16x16x32_bf16 v[176:179], a[152:155], v[28:31], v[176:179]\n"
      "global_load_lds_dwordx4 v248, s[8:9] sc1\n"
      "v_mfma_f32_16x16x32_bf16 v[180:183], a[132:135], v[16:19], v[180:183]\n"
      "ds_read_b128 v[0:3], v168 offset:0\n"
      "v_mfma_f32_16x16x32_bf16 v[180:183], a[140:143], v[20:23], v[180:183]\n"
      "v_mfma_f32_16x16x32_bf16 v[180:183], a[148:151], v[24:27], v[180:183]\n"
      "ds_read_b128 v[4:7], v169 offset:0\n"
      "v_mfma_f32_16x16x32_bf16 v[180:183], a[156:159], v[28:31], v[180:183]\n"
      "v_mfma_f32_16x16x32_bf16 v[176:179], a[160:163], v[32:35], v[176:179]\n"
      "ds_read_b128 v[8:11], v170 offset:0\n"
      "v_mfma_f32_16x16x32_bf16 v[176:179], a[168:171], v[36:39], v[176:179]\n"
      "global_load_lds_dwordx4 v248, s[8:9] offset:1024 sc1\n"
      "v_mfma_f32_16x16x32_bf16 v[176:179], a[192:195], v[40:43], v[176:179]\n"
      "s_waitcnt lgkmcnt(2)\n"
      "ds_read_b128 v[12:15], v171 offset:0\n"
      "v_mfma_f32_16x16x32_bf16 v[176:179], a[200:203], v[44:47], v[176:179]\n"
      "v_mfma_f32_16x16x32_bf16 v[180:183], a[164:167], v[32:35], v[180:183]\n"
      "ds_read_b128 v[16:19], v168 offset:4096\n"
      "v_mfma_f32_16x16x32_bf16 v[180:183], a[172:175], v[36:39], v[180:183]\n"
      "v_mfma_f32_16x16x32_bf16 v[180:183], a[196:199], v[40:43], v[180:183]\n"
      "ds_read_b128 v[20:23], v169 offset:4096\n"
      "v_mfma_f32_16x16x32_bf16 v[180:183], a[204:207], v[44:47], v[180:183]\n"
      "global_load_lds_dwordx4 v248, s[8:9] offset:2048 sc1\n"
      "v_mfma_f32_16x16x32_bf16 v[176:179], a[0:3], v[48:51], v[176:179]\n"
      "ds_read_b128 v[24:27], v170 offset:4096\n"
      "v_mfma_f32_16x16x32_bf16 v[176:179], a[8:11], v[52:55], v[176:179]\n"
      "v_mfma_f32_16x16x32_bf16 v[176:179], a[16:19], v[56:59], v[176:179]\n"
      "ds_read_b128 v[28:31], v171 offset:4096\n"
      "v_mfma_f32_16x16x32_bf16 v[176:179], a[24:27], v[60:63], v[176:179]\n"
      "v_mfma_f32_16x16x32_bf16 v[180:183], a[4:7], v[48:51], v[180:183]\n"
      "ds_read_b128 v[32:35], v168 offset:8192\n"
      "v_mfma_f32_16x16x32_bf16 v[180:183], a[12:15], v[52:55], v[180:183]\n"
      "global_load_lds_dwordx4 v248, s[8:9] offset:3072 sc1\n"
      "v_mfma_f32_16x16x32_bf16 v[180:183], a[20:23], v[56:59], v[180:183]\n"
      "ds_read_b128 v[36:39], v169 offset:8192\n"
      "v_mfma_f32_16x16x32_bf16 v[180:183], a[28:31], v[60:63], v[180:183]\n"
      "v_mfma_f32_16x16x32_bf16 v[176:179], a[32:35], v[0:3], v[176:179]\n"
      "s_waitcnt lgkmcnt(2)\n"
      "ds_read_b128 v[40:43], v170 offset:8192\n"
      "v_mfma_f32_16x16x32_bf16 v[176:179], a[40:43], v[4:7], v[176:179]\n"
      "s_xor_b32 s68, s68, 0x4000\n"
      "s_add_u32 s8, s36, s68\n"
      "s_addc_u32 s9, s37, 0\n"
      "v_mfma_f32_16x16x32_bf16 v[176:179], a[48:51], v[8:11], v[176:179]\n"
      "s_add_u32 m0, s71, 16384\n"
      "ds_read_b128 v[44:47], v171 offset:8192\n"
      "v_mfma_f32_16x16x32_bf16 v[176:179], a[56:59], v[12:15], v[176:179]\n"
      "global_load_lds_dwordx4 v248, s[8:9] sc1\n"
      "v_mfma_f32_16x16x32_bf16 v[180:183], a[36:39], v[0:3], v[180:183]\n"
      "ds_read_b128 v[48:51], v168 offset:12288\n"
      "v_mfma_f32_16x16x32_bf16 v[180:183], a[44:47], v[4:7], v[180:183]\n"
      "v_mfma_f32_16x16x32_bf16 v[180:183], a[52:55], v[8:11], v[180:183]\n"
      "ds_read_b128 v[52:55], v169 offset:12288\n"
      "v_mfma_f32_16x16x32_bf16 v[180:183], a[60:63], v[12:15], v[180:183]\n"
      "v_mfma_f32_16x16x32_bf16 v[176:179], a[176:179], v[16:19], v[176:179]\n"
      "ds_read_b128 v[56:59], v170 offset:12288\n"
      "v_mfma_f32_16x16x32_bf16 v[176:179], a[184:187], v[20:23], v[176:179]\n"
      "global_load_lds_dwordx4 v248, s[8:9] offset:1024 sc1\n"
      "v_mfma_f32_16x16x32_bf16 v[176:179], a[208:211], v[24:27], v[176:179]\n"
      "s_waitcnt vmcnt(7)\n"
      "ds_read_b128 v[60:63], v171 offset:12288\n"
      "v_mfma_f32_16x16x32_bf16 v[176:179], a[216:219], v[28:31], v[176:179]\n"
      "s_barrier\n"
      "v_mfma_f32_16x16x32_bf16 v[180:183], a[180:183], v[16:19], v[180:183]\n"
      "ds_read_b128 v[0:3], v164 offset:49152\n"
      "v_mfma_f32_16x16x32_bf16 v[180:183], a[188:191], v[20:23], v[180:183]\n"
      "v_mfma_f32_16x16x32_bf16 v[180:183], a[212:215], v[24:27], v[180:183]\n"
      "s_waitcnt lgkmcnt(2)\n"
      "ds_read_b128 v[4:7], v165 offset:49152\n"
      "v_mfma_f32_16x16x32_bf16 v[180:183], a[220:223], v[28:31], v[180:183]\n"
      "global_load_lds_dwordx4 v248, s[8:9] offset:2048 sc1\n"
      "v_mfma_f32_16x16x32_bf16 v[176:179], a[64:67], v[32:35], v[176:179]\n"
      "ds_read_b128 v[8:11], v166 offset:49152\n"
      "v_mfma_f32_16x16x32_bf16 v[176:179], a[72:75], v[36:39], v[176:179]\n"
      "v_mfma_f32_16x16x32_bf16 v[176:179], a[80:83], v[40:43], v[176:179]\n"
      "ds_read_b128 v[12:15], v167 offset:49152\n"
      "v_mfma_f32_16x16x32_bf16 v[176:179], a[88:91], v[44:47], v[176:179]\n"
      "v_mfma_f32_16x16x32_bf16 v[180:183], a[68:71], v[32:35], v[180:183]\n"
      "ds_read_b128 v[16:19], v164 offset:53248\n"
      "v_mfma_f32_16x16x32_bf16 v[180:183], a[76:79], v[36:39], v[180:183]\n"
      "global_load_lds_dwordx4 v248, s[8:9] offset:3072 sc1\n"
      "v_mfma_f32_16x16x32_bf16 v[180:183], a[84:87], v[40:43], v[180:183]\n"
      "ds_read_b128 v[20:23], v165 offset:53248\n"
      "v_mfma_f32_16x16x32_bf16 v[180:183], a[92:95], v[44:47], v[180:183]\n"
      "v_mfma_f32_16x16x32_bf16 v[176:179], a[96:99], v[48:51], v[176:179]\n"
      "ds_read_b128 v[24:27], v166 offset:53248\n"
      "v_mfma_f32_16x16x32_bf16 v[176:179], a[104:107], v[52:55], v[176:179]\n"
      "v_mfma_f32_16x16x32_bf16 v[176:179], a[112:115], v[56:59], v[176:179]\n"
      "s_waitcnt lgkmcnt(2)\n"
      "ds_read_b128 v[28:31], v167 offset:53248\n"
      "v_mfma_f32_16x16x32_bf16 v[176:179], a[120:123], v[60:63], v[176:179]\n"
      "v_mfma_f32_16x16x32_bf16 v[180:183], a[100:103], v[48:51], v[180:183]\n"
      "ds_read_b128 v[32:35], v164 offset:57344\n"
      "v_mfma_f32_16x16x32_bf16 v[180:183], a[108:111], v[52:55], v[180:183]\n"
      "v_mfma_f32_16x16x32_bf16 v[180:183], a[116:119], v[56:59], v[180:183]\n"
      "ds_read_b128 v[36:39], v165 offset:57344\n"
      "v_mfma_f32_16x16x32_bf16 v[180:183], a[124:127], v[60:63], v[180:183]\n"
      "v_mfma_f32_16x16x32_bf16 v[184:187], a[224:227], v[0:3], 0\n"
      "ds_read_b128 v[40:43], v166 offset:57344\n"
      "v_mfma_f32_16x16x32_bf16 v[184:187], a[232:235], v[4:7], v[184:187]\n"
      "v_mfma_f32_16x16x32_bf16 v[184:187], a[240:243], v[8:11], v[184:187]\n"
      "ds_read_b128 v[44:47], v167 offset:57344\n"
      "v_mfma_f32_16x16x32_bf16 v[184:187], a[248:251], v[12:15], v[184:187]\n"
      "v_mfma_f32_16x16x32_bf16 v[188:191], a[228:231], v[0:3], 0\n"
      "ds_read_b128 v[48:51], v164 offset:61440\n"
      "v_mfma_f32_16x16x32_bf16 v[188:191], a[236:239], v[4:7], v[188:191]\n"
      "v_cvt_pk_bf16_f32 v176, v176, v177\n"
      "v_cvt_pk_bf16_f32 v177, v178, v179\n"
      "v_mfma_f32_16x16x32_bf16 v[188:191], a[244:247], v[8:11], v[188:191]\n"
      "ds_read_b128 v[52:55], v165 offset:61440\n"
      "v_mfma_f32_16x16x32_bf16 v[188:191], a[252:255], v[12:15], v[188:191]\n"
      "v_cvt_pk_bf16_f32 v178, v180, v181\n"
      "v_cvt_pk_bf16_f32 v179, v182, v183\n"
      "v_mfma_f32_16x16x32_bf16 v[184:187], a[128:131], v[16:19], v[184:187]\n"
      "s_waitcnt lgkmcnt(2)\n"
      "ds_read_b128 v[56:59], v166 offset:61440\n"
      "v_mfma_f32_16x16x32_bf16 v[184:187], a[136:139], v[20:23], v[184:187]\n"
      "s_add_u32 s8, s36, s69\n"
      "s_addc_u32 s9, s37, 0\n"
      "buffer_store_dwordx4 v[176:179], v248, s[36:39], s42 offen offset:0 sc1\n"
      "v_mfma_f32_16x16x32_bf16 v[184:187], a[144:147], v[24:27], v[184:187]\n"
      "s_add_u32 m0, s71, 0\n"
      "ds_read_b128 v[60:63], v167 offset:61440\n"
      "v_mfma_f32_16x16x32_bf16 v[184:187], a[152:155], v[28:31], v[184:187]\n"
      "global_load_lds_dwordx4 v248, s[8:9] sc1\n"
      "v_mfma_f32_16x16x32_bf16 v[188:191], a[132:135], v[16:19], v[188:191]\n"
      "ds_read_b128 v[0:3], v164 offset:32768\n"
      "v_mfma_f32_16x16x32_bf16 v[188:191], a[140:143], v[20:23], v[188:191]\n"
      "v_mfma_f32_16x16x32_bf16 v[188:191], a[148:151], v[24:27], v[188:191]\n"
      "ds_read_b128 v[4:7], v165 offset:32768\n"
      "v_mfma_f32_16x16x32_bf16 v[188:191], a[156:159], v[28:31], v[188:191]\n"
      "v_mfma_f32_16x16x32_bf16 v[184:187], a[160:163], v[32:35], v[184:187]\n"
      "ds_read_b128 v[8:11], v166 offset:32768\n"
      "v_mfma_f32_16x16x32_bf16 v[184:187], a[168:171], v[36:39], v[184:187]\n"
      "global_load_lds_dwordx4 v248, s[8:9] offset:1024 sc1\n"
      "v_mfma_f32_16x16x32_bf16 v[184:187], a[192:195], v[40:43], v[184:187]\n"
      "ds_read_b128 v[12:15], v167 offset:32768\n"
      "v_mfma_f32_16x16x32_bf16 v[184:187], a[200:203], v[44:47], v[184:187]\n"
      "v_mfma_f32_16x16x32_bf16 v[188:191], a[164:167], v[32:35], v[188:191]\n"
      "ds_read_b128 v[16:19], v164 offset:36864\n"
      "v_mfma_f32_16x16x32_bf16 v[188:191], a[172:175], v[36:39], v[188:191]\n"
      "v_mfma_f32_16x16x32_bf16 v[188:191], a[196:199], v[40:43], v[188:191]\n"
      "s_waitcnt lgkmcnt(2)\n"
      "ds_read_b128 v[20:23], v165 offset:36864\n"
      "v_mfma_f32_16x16x32_bf16 v[188:191], a[204:207], v[44:47], v[188:191]\n"
      "global_load_lds_dwordx4 v248, s[8:9] offset:2048 sc1\n"
      "v_mfma_f32_16x16x32_bf16 v[184:187], a[0:3], v[48:51], v[184:187]\n"
      "ds_read_b128 v[24:27], v166 offset:36864\n"
      "v_mfma_f32_16x16x32_bf16 v[184:187], a[8:11], v[52:55], v[184:187]\n"
      "v_mfma_f32_16x16x32_bf16 v[184:187], a[16:19], v[56:59], v[184:187]\n"
      "ds_read_b128 v[28:31], v167 offset:36864\n"
      "v_mfma_f32_16x16x32_bf16 v[184:187], a[24:27], v[60:63], v[184:187]\n"
      "v_mfma_f32_16x16x32_bf16 v[188:191], a[4:7], v[48:51], v[188:191]\n"
      "ds_read_b128 v[32:35], v164 offset:40960\n"
      "v_mfma_f32_16x16x32_bf16 v[188:191], a[12:15], v[52:55], v[188:191]\n"
      "global_load_lds_dwordx4 v248, s[8:9] offset:3072 sc1\n"
      "v_mfma_f32_16x16x32_bf16 v[188:191], a[20:23], v[56:59], v[188:191]\n"
      "ds_read_b128 v[36:39], v165 offset:40960\n"
      "v_mfma_f32_16x16x32_bf16 v[188:191], a[28:31], v[60:63], v[188:191]\n"
      "v_mfma_f32_16x16x32_bf16 v[184:187], a[32:35], v[0:3], v[184:187]\n"
      "s_waitcnt lgkmcnt(2)\n"
      "ds_read_b128 v[40:43], v166 offset:40960\n"
      "v_mfma_f32_16x16x32_bf16 v[184:187], a[40:43], v[4:7], v[184:187]\n"
      "s_xor_b32 s69, s69, 0x4000\n"
      "s_add_u32 s8, s36, s69\n"
      "s_addc_u32 s9, s37, 0\n"
      "v_mfma_f32_16x16x32_bf16 v[184:187], a[48:51], v[8:11], v[184:187]\n"
      "s_add_u32 m0, s71, 49152\n"
      "ds_read_b128 v[44:47], v167 offset:40960\n"
      "v_mfma_f32_16x16x32_bf16 v[184:187], a[56:59], v[12:15], v[184:187]\n"
      "global_load_lds_dwordx4 v248, s[8:9] sc1\n"
      "v_mfma_f32_16x16x32_bf16 v[188:191], a[36:39], v[0:3], v[188:191]\n"
      "ds_read_b128 v[48:51], v164 offset:45056\n"
      "v_mfma_f32_16x16x32_bf16 v[188:191], a[44:47], v[4:7], v[188:191]\n"
      "v_mfma_f32_16x16x32_bf16 v[188:191], a[52:55], v[8:11], v[188:191]\n"
      "ds_read_b128 v[52:55], v165 offset:45056\n"
      "v_mfma_f32_16x16x32_bf16 v[188:191], a[60:63], v[12:15], v[188:191]\n"
      "v_mfma_f32_16x16x32_bf16 v[184:187], a[176:179], v[16:19], v[184:187]\n"
      "ds_read_b128 v[56:59], v166 offset:45056\n"
      "v_mfma_f32_16x16x32_bf16 v[184:187], a[184:187], v[20:23], v[184:187]\n"
      "global_load_lds_dwordx4 v248, s[8:9] offset:1024 sc1\n"
      "v_mfma_f32_16x16x32_bf16 v[184:187], a[208:211], v[24:27], v[184:187]\n"
      "s_waitcnt vmcnt(7)\n"
      "ds_read_b128 v[60:63], v167 offset:45056\n"
      "v_mfma_f32_16x16x32_bf16 v[184:187], a[216:219], v[28:31], v[184:187]\n"
      "s_barrier\n"
      "v_mfma_f32_16x16x32_bf16 v[188:191], a[180:183], v[16:19], v[188:191]\n"
      "ds_read_b128 v[0:3], v168 offset:16384\n"
      "v_mfma_f32_16x16x32_bf16 v[188:191], a[188:191], v[20:23], v[188:191]\n"
      "v_mfma_f32_16x16x32_bf16 v[188:191], a[212:215], v[24:27], v[188:191]\n"
      "s_waitcnt lgkmcnt(2)\n"
      "ds_read_b128 v[4:7], v169 offset:16384\n"
      "v_mfma_f32_16x16x32_bf16 v[188:191], a[220:223], v[28:31], v[188:191]\n"
      "global_load_lds_dwordx4 v248, s[8:9] offset:2048 sc1\n"
      "v_mfma_f32_16x16x32_bf16 v[184:187], a[64:67], v[32:35], v[184:187]\n"
      "ds_read_b128 v[8:11], v170 offset:16384\n"
      "v_mfma_f32_16x16x32_bf16 v[184:187], a[72:75], v[36:39], v[184:187]\n"
      "v_mfma_f32_16x16x32_bf16 v[184:187], a[80:83], v[40:43], v[184:187]\n"
      "ds_read_b128 v[12:15], v171 offset:16384\n"
      "v_mfma_f32_16x16x32_bf16 v[184:187], a[88:91], v[44:47], v[184:187]\n"
      "v_mfma_f32_16x16x32_bf16 v[188:191], a[68:71], v[32:35], v[188:191]\n"
      "ds_read_b128 v[16:19], v168 offset:20480\n"
      "v_mfma_f32_16x16x32_bf16 v[188:191], a[76:79], v[36:39], v[188:191]\n"
      "global_load_lds_dwordx4 v248, s[8:9] offset:3072 sc1\n"
      "v_mfma_f32_16x16x32_bf16 v[188:191], a[84:87], v[40:43], v[188:191]\n"
      "ds_read_b128 v[20:23], v169 offset:20480\n"
      "v_mfma_f32_16x16x32_bf16 v[188:191], a[92:95], v[44:47], v[188:191]\n"
      "v_mfma_f32_16x16x32_bf16 v[184:187], a[96:99], v[48:51], v[184:187]\n"
      "ds_read_b128 v[24:27], v170 offset:20480\n"
      "v_mfma_f32_16x16x32_bf16 v[184:187], a[104:107], v[52:55], v[184:187]\n"
      "v_mfma_f32_16x16x32_bf16 v[184:187], a[112:115], v[56:59], v[184:187]\n"
      "s_waitcnt lgkmcnt(2)\n"
      "ds_read_b128 v[28:31], v171 offset:20480\n"
      "v_mfma_f32_16x16x32_bf16 v[184:187], a[120:123], v[60:63], v[184:187]\n"
      "v_mfma_f32_16x16x32_bf16 v[188:191], a[100:103], v[48:51], v[188:191]\n"
      "ds_read_b128 v[32:35], v168 offset:24576\n"
      "v_mfma_f32_16x16x32_bf16 v[188:191], a[108:111], v[52:55], v[188:191]\n"
      "v_mfma_f32_16x16x32_bf16 v[188:191], a[116:119], v[56:59], v[188:191]\n"
      "ds_read_b128 v[36:39], v169 offset:24576\n"
      "v_mfma_f32_16x16x32_bf16 v[188:191], a[124:127], v[60:63], v[188:191]\n"
      "v_mfma_f32_16x16x32_bf16 v[176:179], a[224:227], v[0:3], 0\n"
      "ds_read_b128 v[40:43], v170 offset:24576\n"
      "v_mfma_f32_16x16x32_bf16 v[176:179], a[232:235], v[4:7], v[176:179]\n"
      "v_mfma_f32_16x16x32_bf16 v[176:179], a[240:243], v[8:11], v[176:179]\n"
      "ds_read_b128 v[44:47], v171 offset:24576\n"
      "v_mfma_f32_16x16x32_bf16 v[176:179], a[248:251], v[12:15], v[176:179]\n"
      "v_mfma_f32_16x16x32_bf16 v[180:183], a[228:231], v[0:3], 0\n"
      "ds_read_b128 v[48:51], v168 offset:28672\n"
      "v_mfma_f32_16x16x32_bf16 v[180:183], a[236:239], v[4:7], v[180:183]\n"
      "v_cvt_pk_bf16_f32 v184, v184, v185\n"
      "v_cvt_pk_bf16_f32 v185, v186, v187\n"
      "v_mfma_f32_16x16x32_bf16 v[180:183], a[244:247], v[8:11], v[180:183]\n"
      "ds_read_b128 v[52:55], v169 offset:28672\n"
      "v_mfma_f32_16x16x32_bf16 v[180:183], a[252:255], v[12:15], v[180:183]\n"
      "v_cvt_pk_bf16_f32 v186, v188, v189\n"
      "v_cvt_pk_bf16_f32 v187, v190, v191\n"
      "v_mfma_f32_16x16x32_bf16 v[176:179], a[128:131], v[16:19], v[176:179]\n"
      "s_waitcnt lgkmcnt(2)\n"
      "ds_read_b128 v[56:59], v170 offset:28672\n"
      "v_mfma_f32_16x16x32_bf16 v[176:179], a[136:139], v[20:23], v[176:179]\n"
      "s_add_u32 s8, s36, s70\n"
      "s_addc_u32 s9, s37, 0\n"
      "buffer_store_dwordx4 v[184:187], v248, s[36:39], s42 offen offset:1024 sc1\n"
      "v_mfma_f32_16x16x32_bf16 v[176:179], a[144:147], v[24:27], v[176:179]\n"
      "s_add_u32 m0, s71, 81920\n"
      "ds_read_b128 v[60:63], v171 offset:28672\n"
      "v_mfma_f32_16x16x32_bf16 v[176:179], a[152:155], v[28:31], v[176:179]\n"
      "global_load_lds_dwordx4 v248, s[8:9] sc1\n"
      "v_mfma_f32_16x16x32_bf16 v[180:183], a[132:135], v[16:19], v[180:183]\n"
      "ds_read_b128 v[0:3], v160 offset:16384\n"
      "v_mfma_f32_16x16x32_bf16 v[180:183], a[140:143], v[20:23], v[180:183]\n"
      "v_mfma_f32_16x16x32_bf16 v[180:183], a[148:151], v[24:27], v[180:183]\n"
      "ds_read_b128 v[4:7], v161 offset:16384\n"
      "v_mfma_f32_16x16x32_bf16 v[180:183], a[156:159], v[28:31], v[180:183]\n"
      "v_mfma_f32_16x16x32_bf16 v[176:179], a[160:163], v[32:35], v[176:179]\n"
      "ds_read_b128 v[8:11], v162 offset:16384\n"
      "v_bfe_u32 v110, v255, 2, 2\n"
      "v_mfma_f32_16x16x32_bf16 v[176:179], a[168:171], v[36:39], v[176:179]\n"
      "global_load_lds_dwordx4 v248, s[8:9] offset:1024 sc1\n"
      "v_lshrrev_b32 v111, 4, v255\n"
      "v_mfma_f32_16x16x32_bf16 v[176:179], a[192:195], v[40:43], v[176:179]\n"
      "ds_read_b128 v[12:15], v163 offset:16384\n"
      "v_cmp_eq_u32 vcc, v110, v111\n"
      "v_mfma_f32_16x16x32_bf16 v[176:179], a[200:203], v[44:47], v[176:179]\n"
      "v_and_b32 v110, 1, v255\n"
      "v_lshlrev_b32 v110, 4, v110\n"
      "v_mfma_f32_16x16x32_bf16 v[180:183], a[164:167], v[32:35], v[180:183]\n"
      "ds_read_b128 v[16:19], v160 offset:20480\n"
      "v_mov_b32 v111, 0x3f80\n"
      "v_mfma_f32_16x16x32_bf16 v[180:183], a[172:175], v[36:39], v[180:183]\n"
      "v_lshlrev_b32 v110, v110, v111\n"
      "v_cndmask_b32 v110, 0, v110, vcc\n"
      "v_mfma_f32_16x16x32_bf16 v[180:183], a[196:199], v[40:43], v[180:183]\n"
      "s_waitcnt lgkmcnt(2)\n"
      "ds_read_b128 v[20:23], v161 offset:20480\n"
      "v_and_b32 v111, 2, v255\n"
      "v_mfma_f32_16x16x32_bf16 v[180:183], a[204:207], v[44:47], v[180:183]\n"
      "global_load_lds_dwordx4 v248, s[8:9] offset:2048 sc1\n"
      "v_cmp_eq_u32 vcc, 0, v111\n"
      "v_mfma_f32_16x16x32_bf16 v[176:179], a[0:3], v[48:51], v[176:179]\n"
      "ds_read_b128 v[24:27], v162 offset:20480\n"
      "v_mov_b32 v106, 0\n"
      "v_mfma_f32_16x16x32_bf16 v[176:179], a[8:11], v[52:55], v[176:179]\n"
      "v_mov_b32 v107, 0\n"
      "v_cndmask_b32 v104, 0, v110, vcc\n"
      "v_mfma_f32_16x16x32_bf16 v[176:179], a[16:19], v[56:59], v[176:179]\n"
      "ds_read_b128 v[28:31], v163 offset:20480\n"
      "v_sub_u32 v105, v110, v104\n"
      "v_mfma_f32_16x16x32_bf16 v[176:179], a[24:27], v[60:63], v[176:179]\n"
      "v_mov_b32 v108, v104\n"
      "v_mov_b32 v109, v105\n"
      "v_mfma_f32_16x16x32_bf16 v[180:183], a[4:7], v[48:51], v[180:183]\n"
      "ds_read_b128 v[32:35], v160 offset:24576\n"
      "v_mfma_f32_16x16x32_bf16 v[180:183], a[12:15], v[52:55], v[180:183]\n"
      "global_load_lds_dwordx4 v248, s[8:9] offset:3072 sc1\n"
      "v_mov_b32 v234, 0x7ffffff0\n"
      "v_mfma_f32_16x16x32_bf16 v[180:183], a[20:23], v[56:59], v[180:183]\n"
      "s_mov_b64 vcc, 0xf0000\n"
      "ds_read_b128 v[36:39], v161 offset:24576\n"
      "v_cndmask_b32 v253, v234, v253, vcc\n"
      "v_mfma_f32_16x16x32_bf16 v[180:183], a[28:31], v[60:63], v[180:183]\n"
      "v_mfma_f32_16x16x32_bf16 v[176:179], a[32:35], v[0:3], v[176:179]\n"
      "s_waitcnt lgkmcnt(2)\n"
      "ds_read_b128 v[40:43], v162 offset:24576\n"
      "v_mfma_f32_16x16x32_bf16 v[176:179], a[40:43], v[4:7], v[176:179]\n"
      "s_xor_b32 s70, s70, 0x4000\n"
      "s_add_u32 s8, s36, s70\n"
      "s_addc_u32 s9, s37, 0\n"
      "v_mfma_f32_16x16x32_bf16 v[176:179], a[48:51], v[8:11], v[176:179]\n"
      "s_add_u32 m0, s71, 32768\n"
      "ds_read_b128 v[44:47], v163 offset:24576\n"
      "v_mfma_f32_16x16x32_bf16 v[176:179], a[56:59], v[12:15], v[176:179]\n"
      "global_load_lds_dwordx4 v248, s[8:9] sc1\n"
      "v_mfma_f32_16x16x32_bf16 v[180:183], a[36:39], v[0:3], v[180:183]\n"
      "ds_read_b128 v[48:51], v160 offset:28672\n"
      "v_mfma_f32_16x16x32_bf16 v[180:183], a[44:47], v[4:7], v[180:183]\n"
      "v_mfma_f32_16x16x32_bf16 v[180:183], a[52:55], v[8:11], v[180:183]\n"
      "ds_read_b128 v[52:55], v161 offset:28672\n"
      "v_mfma_f32_16x16x32_bf16 v[180:183], a[60:63], v[12:15], v[180:183]\n"
      "v_mfma_f32_16x16x32_bf16 v[176:179], a[176:179], v[16:19], v[176:179]\n"
      "ds_read_b128 v[56:59], v162 offset:28672\n"
      "v_mfma_f32_16x16x32_bf16 v[176:179], a[184:187], v[20:23], v[176:179]\n"
      "global_load_lds_dwordx4 v248, s[8:9] offset:1024 sc1\n"
      "v_mfma_f32_16x16x32_bf16 v[176:179], a[208:211], v[24:27], v[176:179]\n"
      "s_waitcnt vmcnt(7)\n"
      "ds_read_b128 v[60:63], v163 offset:28672\n"
      "v_mfma_f32_16x16x32_bf16 v[176:179], a[216:219], v[28:31], v[176:179]\n"
      "s_barrier\n"
      "v_mfma_f32_16x16x32_bf16 v[180:183], a[180:183], v[16:19], v[180:183]\n"
      "ds_read_b128 v[0:3], v160 offset:0\n"
      "v_mfma_f32_16x16x32_bf16 v[180:183], a[188:191], v[20:23], v[180:183]\n"
      "v_mfma_f32_16x16x32_bf16 v[180:183], a[212:215], v[24:27], v[180:183]\n"
      "s_waitcnt lgkmcnt(2)\n"
      "ds_read_b128 v[4:7], v161 offset:0\n"
      "v_mfma_f32_16x16x32_bf16 v[180:183], a[220:223], v[28:31], v[180:183]\n"
      "global_load_lds_dwordx4 v248, s[8:9] offset:2048 sc1\n"
      "v_mfma_f32_16x16x32_bf16 v[176:179], a[64:67], v[32:35], v[176:179]\n"
      "ds_read_b128 v[8:11], v162 offset:0\n"
      "v_mfma_f32_16x16x32_bf16 v[176:179], a[72:75], v[36:39], v[176:179]\n"
      "v_mfma_f32_16x16x32_bf16 v[176:179], a[80:83], v[40:43], v[176:179]\n"
      "ds_read_b128 v[12:15], v163 offset:0\n"
      "v_mfma_f32_16x16x32_bf16 v[176:179], a[88:91], v[44:47], v[176:179]\n"
      "v_mfma_f32_16x16x32_bf16 v[180:183], a[68:71], v[32:35], v[180:183]\n"
      "ds_read_b128 v[16:19], v160 offset:4096\n"
      "v_mfma_f32_16x16x32_bf16 v[180:183], a[76:79], v[36:39], v[180:183]\n"
      "global_load_lds_dwordx4 v248, s[8:9] offset:3072 sc1\n"
      "v_mfma_f32_16x16x32_bf16 v[180:183], a[84:87], v[40:43], v[180:183]\n"
      "ds_read_b128 v[20:23], v161 offset:4096\n"
      "v_mfma_f32_16x16x32_bf16 v[180:183], a[92:95], v[44:47], v[180:183]\n"
      "v_mfma_f32_16x16x32_bf16 v[176:179], a[96:99], v[48:51], v[176:179]\n"
      "ds_read_b128 v[24:27], v162 offset:4096\n"
      "v_mfma_f32_16x16x32_bf16 v[176:179], a[104:107], v[52:55], v[176:179]\n"
      "v_mfma_f32_16x16x32_bf16 v[176:179], a[112:115], v[56:59], v[176:179]\n"
      "s_waitcnt lgkmcnt(2)\n"
      "ds_read_b128 v[28:31], v163 offset:4096\n"
      "v_mfma_f32_16x16x32_bf16 v[176:179], a[120:123], v[60:63], v[176:179]\n"
      "v_mfma_f32_16x16x32_bf16 v[180:183], a[100:103], v[48:51], v[180:183]\n"
      "ds_read_b128 v[32:35], v160 offset:8192\n"
      "v_mfma_f32_16x16x32_bf16 v[180:183], a[108:111], v[52:55], v[180:183]\n"
      "v_mfma_f32_16x16x32_bf16 v[180:183], a[116:119], v[56:59], v[180:183]\n"
      "ds_read_b128 v[36:39], v161 offset:8192\n"
      "v_mfma_f32_16x16x32_bf16 v[180:183], a[124:127], v[60:63], v[180:183]\n"
      "v_mfma_f32_16x16x32_bf16 v[184:187], a[224:227], v[0:3], 0\n"
      "ds_read_b128 v[40:43], v162 offset:8192\n"
      "v_mfma_f32_16x16x32_bf16 v[184:187], a[232:235], v[4:7], v[184:187]\n"
      "v_mfma_f32_16x16x32_bf16 v[184:187], a[240:243], v[8:11], v[184:187]\n"
      "ds_read_b128 v[44:47], v163 offset:8192\n"
      "v_mfma_f32_16x16x32_bf16 v[184:187], a[248:251], v[12:15], v[184:187]\n"
      "v_mfma_f32_16x16x32_bf16 v[188:191], a[228:231], v[0:3], 0\n"
      "ds_read_b128 v[48:51], v160 offset:12288\n"
      "v_mfma_f32_16x16x32_bf16 v[188:191], a[236:239], v[4:7], v[188:191]\n"
      "v_cvt_pk_bf16_f32 v176, v176, v177\n"
      "v_cvt_pk_bf16_f32 v177, v178, v179\n"
      "v_mfma_f32_16x16x32_bf16 v[188:191], a[244:247], v[8:11], v[188:191]\n"
      "ds_read_b128 v[52:55], v161 offset:12288\n"
      "v_mfma_f32_16x16x32_bf16 v[188:191], a[252:255], v[12:15], v[188:191]\n"
      "v_cvt_pk_bf16_f32 v178, v180, v181\n"
      "v_cvt_pk_bf16_f32 v179, v182, v183\n"
      "v_mfma_f32_16x16x32_bf16 v[184:187], a[128:131], v[16:19], v[184:187]\n"
      "s_waitcnt lgkmcnt(2)\n"
      "ds_read_b128 v[56:59], v162 offset:12288\n"
      "v_mfma_f32_16x16x32_bf16 v[184:187], a[136:139], v[20:23], v[184:187]\n"
      "buffer_store_dwordx4 v[176:179], v248, s[36:39], s43 offen offset:0 sc1\n"
      "v_mfma_f32_16x16x32_bf16 v[184:187], a[144:147], v[24:27], v[184:187]\n"
      "ds_read_b128 v[60:63], v163 offset:12288\n"
      "v_mfma_f32_16x16x32_bf16 v[184:187], a[152:155], v[28:31], v[184:187]\n"
      "v_mfma_f32_16x16x32_bf16 v[188:191], a[132:135], v[16:19], v[188:191]\n"
      "ds_read_b128 v[0:3], v160 offset:49152\n"
      "v_mfma_f32_16x16x32_bf16 v[188:191], a[140:143], v[20:23], v[188:191]\n"
      "v_mfma_f32_16x16x32_bf16 v[188:191], a[148:151], v[24:27], v[188:191]\n"
      "ds_read_b128 v[4:7], v161 offset:49152\n"
      "v_mfma_f32_16x16x32_bf16 v[188:191], a[156:159], v[28:31], v[188:191]\n"
      "v_mfma_f32_16x16x32_bf16 v[184:187], a[160:163], v[32:35], v[184:187]\n"
      "ds_read_b128 v[8:11], v162 offset:49152\n"
      "v_mfma_f32_16x16x32_bf16 v[184:187], a[168:171], v[36:39], v[184:187]\n"
      "v_mfma_f32_16x16x32_bf16 v[184:187], a[192:195], v[40:43], v[184:187]\n"
      "ds_read_b128 v[12:15], v163 offset:49152\n"
      "v_mfma_f32_16x16x32_bf16 v[184:187], a[200:203], v[44:47], v[184:187]\n"
      "v_mfma_f32_16x16x32_bf16 v[188:191], a[164:167], v[32:35], v[188:191]\n"
      "ds_read_b128 v[16:19], v160 offset:53248\n"
      "v_mfma_f32_16x16x32_bf16 v[188:191], a[172:175], v[36:39], v[188:191]\n"
      "buffer_load_dword v235, v253, s[36:39], 0 offen sc1\n"
      "v_mfma_f32_16x16x32_bf16 v[188:191], a[196:199], v[40:43], v[188:191]\n"
      "ds_read_b128 v[20:23], v161 offset:53248\n"
      "v_mfma_f32_16x16x32_bf16 v[188:191], a[204:207], v[44:47], v[188:191]\n"
      "s_waitcnt lgkmcnt(3)\n"
      "v_mfma_f32_16x16x32_bf16 v[184:187], a[0:3], v[48:51], v[184:187]\n"
      "ds_read_b128 v[24:27], v162 offset:53248\n"
      "v_mfma_f32_16x16x32_bf16 v[184:187], a[8:11], v[52:55], v[184:187]\n"
      "v_mfma_f32_16x16x32_bf16 v[184:187], a[16:19], v[56:59], v[184:187]\n"
      "ds_read_b128 v[28:31], v163 offset:53248\n"
      "v_mfma_f32_16x16x32_bf16 v[184:187], a[24:27], v[60:63], v[184:187]\n"
      "v_mfma_f32_16x16x32_bf16 v[188:191], a[4:7], v[48:51], v[188:191]\n"
      "ds_read_b128 v[32:35], v160 offset:57344\n"
      "v_mfma_f32_16x16x32_bf16 v[188:191], a[12:15], v[52:55], v[188:191]\n"
      "v_mfma_f32_16x16x32_bf16 v[188:191], a[20:23], v[56:59], v[188:191]\n"
      "ds_read_b128 v[36:39], v161 offset:57344\n"
      "v_mfma_f32_16x16x32_bf16 v[188:191], a[28:31], v[60:63], v[188:191]\n"
      "v_mfma_f32_16x16x32_bf16 v[184:187], a[32:35], v[0:3], v[184:187]\n"
      "ds_read_b128 v[40:43], v162 offset:57344\n"
      "v_mfma_f32_16x16x32_bf16 v[184:187], a[40:43], v[4:7], v[184:187]\n"
      "v_mfma_f32_16x16x32_bf16 v[184:187], a[48:51], v[8:11], v[184:187]\n"
      "s_waitcnt lgkmcnt(2)\n"
      "ds_read_b128 v[44:47], v163 offset:57344\n"
      "v_mfma_f32_16x16x32_bf16 v[184:187], a[56:59], v[12:15], v[184:187]\n"
      "v_mfma_f32_16x16x32_bf16 v[188:191], a[36:39], v[0:3], v[188:191]\n"
      "ds_read_b128 v[48:51], v160 offset:61440\n"
      "v_mfma_f32_16x16x32_bf16 v[188:191], a[44:47], v[4:7], v[188:191]\n"
      "v_mfma_f32_16x16x32_bf16 v[188:191], a[52:55], v[8:11], v[188:191]\n"
      "ds_read_b128 v[52:55], v161 offset:61440\n"
      "v_mfma_f32_16x16x32_bf16 v[188:191], a[60:63], v[12:15], v[188:191]\n"
      "v_mfma_f32_16x16x32_bf16 v[184:187], a[176:179], v[16:19], v[184:187]\n"
      "ds_read_b128 v[56:59], v162 offset:61440\n"
      "s_waitcnt vmcnt(1)\n"
      "s_add_u32 s73, s92, 1\n"
      "v_mov_b32 v234, s73\n"
      "buffer_store_dword v234, v249, s[36:39], s90 offen sc1\n"
      "v_mfma_f32_16x16x32_bf16 v[184:187], a[184:187], v[20:23], v[184:187]\n"
      "v_mfma_f32_16x16x32_bf16 v[184:187], a[208:211], v[24:27], v[184:187]\n"
      "s_waitcnt vmcnt(3)\n"
      "ds_read_b128 v[60:63], v163 offset:61440\n"
      "v_mfma_f32_16x16x32_bf16 v[184:187], a[216:219], v[28:31], v[184:187]\n"
      "s_barrier\n"
      "v_mfma_f32_16x16x32_bf16 v[188:191], a[180:183], v[16:19], v[188:191]\n"
      "ds_read_b128 v[0:3], v164 offset:16384\n"
      "v_mfma_f32_16x16x32_bf16 v[188:191], a[188:191], v[20:23], v[188:191]\n"
      "s_waitcnt vmcnt(1)\n"
      "s_lshl_b32 s73, 0x10000, s94\n"
      "v_cmp_eq_u32 vcc, v235, v254\n"
      "s_xor_b32 s73, s73, 0xf0000\n"
      "s_and_b32 s20, vcc_lo, s73\n"
      "s_cbranch_scc1 NOTYET_%=\n"
      "s_add_u32 s73, s94, 1\n"
      "s_and_b32 s73, s73, 3\n"
      "s_mul_i32 s73, s73, 12288\n"
      "s_add_u32 s73, s73, s47\n"
      "buffer_load_dwordx4 v[64:67], v248, s[36:39], s73 offen offset:0 sc1\n"
      "buffer_load_dwordx4 v[68:71], v248, s[36:39], s73 offen offset:1024 sc1\n"
      "s_add_u32 s73, s94, 2\n"
      "s_and_b32 s73, s73, 3\n"
      "s_mul_i32 s73, s73, 12288\n"
      "s_add_u32 s73, s73, s47\n"
      "buffer_load_dwordx4 v[72:75], v248, s[36:39], s73 offen offset:0 sc1\n"
      "buffer_load_dwordx4 v[76:79], v248, s[36:39], s73 offen offset:1024 sc1\n"
      "s_add_u32 s73, s94, 3\n"
      "s_and_b32 s73, s73, 3\n"
      "s_mul_i32 s73, s73, 12288\n"
      "s_add_u32 s73, s73, s47\n"
      "buffer_load_dwordx4 v[80:83], v248, s[36:39], s73 offen offset:0 sc1\n"
      "buffer_load_dwordx4 v[84:87], v248, s[36:39], s73 offen offset:1024 sc1\n"
      "s_cmp_eq_u32 s22, s94\n"
      "s_cbranch_scc0 NOSHL0_%=\n"
      "s_add_u32 s73, s47, 0\n"
      "buffer_load_dwordx4 v[88:91], v248, s[36:39], s73 offen offset:2048 sc1\n"
      "s_add_u32 s73, s47, 12288\n"
      "buffer_load_dwordx4 v[92:95], v248, s[36:39], s73 offen offset:2048 sc1\n"
      "s_add_u32 s73, s47, 24576\n"
      "buffer_load_dwordx4 v[96:99], v248, s[36:39], s73 offen offset:2048 sc1\n"
      "s_add_u32 s73, s47, 36864\n"
      "buffer_load_dwordx4 v[100:103], v248, s[36:39], s73 offen offset:2048 sc1\n"
      "NOSHL0_%=:\n"
      "NOTYET_%=:\n"
      "v_mfma_f32_16x16x32_bf16 v[188:191], a[212:215], v[24:27], v[188:191]\n"
      "ds_read_b128 v[4:7], v165 offset:16384\n"
      "v_mfma_f32_16x16x32_bf16 v[188:191], a[220:223], v[28:31], v[188:191]\n"
      "v_mfma_f32_16x16x32_bf16 v[184:187], a[64:67], v[32:35], v[184:187]\n"
      "s_waitcnt lgkmcnt(2)\n"
      "ds_read_b128 v[8:11], v166 offset:16384\n"
      "v_mfma_f32_16x16x32_bf16 v[184:187], a[72:75], v[36:39], v[184:187]\n"
      "v_mfma_f32_16x16x32_bf16 v[184:187], a[80:83], v[40:43], v[184:187]\n"
      "ds_read_b128 v[12:15], v167 offset:16384\n"
      "v_mfma_f32_16x16x32_bf16 v[184:187], a[88:91], v[44:47], v[184:187]\n"
      "v_mfma_f32_16x16x32_bf16 v[188:191], a[68:71], v[32:35], v[188:191]\n"
      "ds_read_b128 v[16:19], v164 offset:20480\n"
      "v_mfma_f32_16x16x32_bf16 v[188:191], a[76:79], v[36:39], v[188:191]\n"
      "v_mfma_f32_16x16x32_bf16 v[188:191], a[84:87], v[40:43], v[188:191]\n"
      "ds_read_b128 v[20:23], v165 offset:20480\n"
      "v_mfma_f32_16x16x32_bf16 v[188:191], a[92:95], v[44:47], v[188:191]\n"
      "v_mfma_f32_16x16x32_bf16 v[184:187], a[96:99], v[48:51], v[184:187]\n"
      "ds_read_b128 v[24:27], v166 offset:20480\n"
      "v_mfma_f32_16x16x32_bf16 v[184:187], a[104:107], v[52:55], v[184:187]\n"
      "v_mfma_f32_16x16x32_bf16 v[184:187], a[112:115], v[56:59], v[184:187]\n"
      "ds_read_b128 v[28:31], v167 offset:20480\n"
      "v_mfma_f32_16x16x32_bf16 v[184:187], a[120:123], v[60:63], v[184:187]\n"
      "v_mfma_f32_16x16x32_bf16 v[188:191], a[100:103], v[48:51], v[188:191]\n"
      "ds_read_b128 v[32:35], v164 offset:24576\n"
      "v_mfma_f32_16x16x32_bf16 v[188:191], a[108:111], v[52:55], v[188:191]\n"
      "v_mfma_f32_16x16x32_bf16 v[188:191], a[116:119], v[56:59], v[188:191]\n"
      "ds_read_b128 v[36:39], v165 offset:24576\n"
      "v_mfma_f32_16x16x32_bf16 v[188:191], a[124:127], v[60:63], v[188:191]\n"
      "s_waitcnt lgkmcnt(3)\n"
      "v_mfma_f32_16x16x32_bf16 v[176:179], a[224:227], v[0:3], 0\n"
      "ds_read_b128 v[40:43], v166 offset:24576\n"
      "v_mfma_f32_16x16x32_bf16 v[176:179], a[232:235], v[4:7], v[176:179]\n"
      "v_mfma_f32_16x16x32_bf16 v[176:179], a[240:243], v[8:11], v[176:179]\n"
      "ds_read_b128 v[44:47], v167 offset:24576\n"
      "v_mfma_f32_16x16x32_bf16 v[176:179], a[248:251], v[12:15], v[176:179]\n"
      "v_mfma_f32_16x16x32_bf16 v[180:183], a[228:231], v[0:3], 0\n"
      "ds_read_b128 v[48:51], v164 offset:28672\n"
      "v_mfma_f32_16x16x32_bf16 v[180:183], a[236:239], v[4:7], v[180:183]\n"
      "v_mfma_f32_16x16x32_bf16 v[180:183], a[244:247], v[8:11], v[180:183]\n"
      "ds_read_b128 v[52:55], v165 offset:28672\n"
      "v_mfma_f32_16x16x32_bf16 v[180:183], a[252:255], v[12:15], v[180:183]\n"
      "v_mfma_f32_16x16x32_bf16 v[176:179], a[128:131], v[16:19], v[176:179]\n"
      "ds_read_b128 v[56:59], v166 offset:28672\n"
      "v_mfma_f32_16x16x32_bf16 v[176:179], a[136:139], v[20:23], v[176:179]\n"
      "v_mfma_f32_16x16x32_bf16 v[176:179], a[144:147], v[24:27], v[176:179]\n"
      "s_waitcnt lgkmcnt(2)\n"
      "ds_read_b128 v[60:63], v167 offset:28672\n"
      "v_mfma_f32_16x16x32_bf16 v[176:179], a[152:155], v[28:31], v[176:179]\n"
      "v_mfma_f32_16x16x32_bf16 v[180:183], a[132:135], v[16:19], v[180:183]\n"
      "ds_read_b128 v[0:3], v160 offset:32768\n"
      "v_mfma_f32_16x16x32_bf16 v[180:183], a[140:143], v[20:23], v[180:183]\n"
      "v_mfma_f32_16x16x32_bf16 v[180:183], a[148:151], v[24:27], v[180:183]\n"
      "ds_read_b128 v[4:7], v161 offset:32768\n"
      "v_mfma_f32_16x16x32_bf16 v[180:183], a[156:159], v[28:31], v[180:183]\n"
      "v_mfma_f32_16x16x32_bf16 v[176:179], a[160:163], v[32:35], v[176:179]\n"
      "ds_read_b128 v[8:11], v162 offset:32768\n"
      "v_mfma_f32_16x16x32_bf16 v[176:179], a[168:171], v[36:39], v[176:179]\n"
      "v_mfma_f32_16x16x32_bf16 v[176:179], a[192:195], v[40:43], v[176:179]\n"
      "ds_read_b128 v[12:15], v163 offset:32768\n"
      "v_mfma_f32_16x16x32_bf16 v[176:179], a[200:203], v[44:47], v[176:179]\n"
      "v_mfma_f32_16x16x32_bf16 v[180:183], a[164:167], v[32:35], v[180:183]\n"
      "ds_read_b128 v[16:19], v160 offset:36864\n"
      "v_mfma_f32_16x16x32_bf16 v[180:183], a[172:175], v[36:39], v[180:183]\n"
      "v_mfma_f32_16x16x32_bf16 v[180:183], a[196:199], v[40:43], v[180:183]\n"
      "ds_read_b128 v[20:23], v161 offset:36864\n"
      "v_mfma_f32_16x16x32_bf16 v[180:183], a[204:207], v[44:47], v[180:183]\n"
      "v_mfma_f32_16x16x32_bf16 v[176:179], a[0:3], v[48:51], v[176:179]\n"
      "s_waitcnt lgkmcnt(2)\n"
      "ds_read_b128 v[24:27], v162 offset:36864\n"
      "v_mfma_f32_16x16x32_bf16 v[176:179], a[8:11], v[52:55], v[176:179]\n"
      "v_mfma_f32_16x16x32_bf16 v[176:179], a[16:19], v[56:59], v[176:179]\n"
      "ds_read_b128 v[28:31], v163 offset:36864\n"
      "v_mfma_f32_16x16x32_bf16 v[176:179], a[24:27], v[60:63], v[176:179]\n"
      "v_mfma_f32_16x16x32_bf16 v[180:183], a[4:7], v[48:51], v[180:183]\n"
      "ds_read_b128 v[32:35], v160 offset:40960\n"
      "v_mfma_f32_16x16x32_bf16 v[180:183], a[12:15], v[52:55], v[180:183]\n"
      "v_mfma_f32_16x16x32_bf16 v[180:183], a[20:23], v[56:59], v[180:183]\n"
      "ds_read_b128 v[36:39], v161 offset:40960\n"
      "s_cmp_eq_u32 s20, 0\n"
      "s_cbranch_scc1 HAVE_%=\n"
      "buffer_load_dword v235, v253, s[36:39], 0 offen sc1\n"
      "s_waitcnt vmcnt(0)\n"
      "v_sub_u32 v235, v235, v254\n"
      "v_cmp_gt_u32 vcc, 1, v235\n"
      "s_and_b32 s73, vcc_lo, 0xf0000\n"
      "s_cbranch_scc0 POLLED5_%=\n"
      "s_mov_b32 s95, 0x80000\n"
      "POLL5_%=:\n"
      "buffer_load_dword v235, v253, s[36:39], 0 offen sc1\n"
      "s_waitcnt vmcnt(0)\n"
      "v_sub_u32 v235, v235, v254\n"
      "v_cmp_gt_u32 vcc, 1, v235\n"
      "s_and_b32 s73, vcc_lo, 0xf0000\n"
      "s_cbranch_scc0 POLLED5_%=\n"
      "s_sleep 1\n"
      "s_sub_u32 s95, s95, 1\n"
      "s_cbranch_scc0 POLL5_%=\n"
      "POLLED5_%=:\n"
      "s_add_u32 s73, s94, 1\n"
      "s_and_b32 s73, s73, 3\n"
      "s_mul_i32 s73, s73, 12288\n"
      "s_add_u32 s73, s73, s47\n"
      "buffer_load_dwordx4 v[64:67], v248, s[36:39], s73 offen offset:0 sc1\n"
      "buffer_load_dwordx4 v[68:71], v248, s[36:39], s73 offen offset:1024 sc1\n"
      "s_add_u32 s73, s94, 2\n"
      "s_and_b32 s73, s73, 3\n"
      "s_mul_i32 s73, s73, 12288\n"
      "s_add_u32 s73, s73, s47\n"
      "buffer_load_dwordx4 v[72:75], v248, s[36:39], s73 offen offset:0 sc1\n"
      "buffer_load_dwordx4 v[76:79], v248, s[36:39], s73 offen offset:1024 sc1\n"
      "s_add_u32 s73, s94, 3\n"
      "s_and_b32 s73, s73, 3\n"
      "s_mul_i32 s73, s73, 12288\n"
      "s_add_u32 s73, s73, s47\n"
      "buffer_load_dwordx4 v[80:83], v248, s[36:39], s73 offen offset:0 sc1\n"
      "buffer_load_dwordx4 v[84:87], v248, s[36:39], s73 offen offset:1024 sc1\n"
      "s_cmp_eq_u32 s22, s94\n"
      "s_cbranch_scc0 NOSHL1_%=\n"
      "s_add_u32 s73, s47, 0\n"
      "buffer_load_dwordx4 v[88:91], v248, s[36:39], s73 offen offset:2048 sc1\n"
      "s_add_u32 s73, s47, 12288\n"
      "buffer_load_dwordx4 v[92:95], v248, s[36:39], s73 offen offset:2048 sc1\n"
      "s_add_u32 s73, s47, 24576\n"
      "buffer_load_dwordx4 v[96:99], v248, s[36:39], s73 offen offset:2048 sc1\n"
      "s_add_u32 s73, s47, 36864\n"
      "buffer_load_dwordx4 v[100:103], v248, s[36:39], s73 offen offset:2048 sc1\n"
      "NOSHL1_%=:\n"
      "HAVE_%=:\n"
      "v_mfma_f32_16x16x32_bf16 v[180:183], a[28:31], v[60:63], v[180:183]\n"
      "v_mfma_f32_16x16x32_bf16 v[176:179], a[32:35], v[0:3], v[176:179]\n"
      "ds_read_b128 v[40:43], v162 offset:40960\n"
      "v_mfma_f32_16x16x32_bf16 v[176:179], a[40:43], v[4:7], v[176:179]\n"
      "v_mfma_f32_16x16x32_bf16 v[176:179], a[48:51], v[8:11], v[176:179]\n"
      "ds_read_b128 v[44:47], v163 offset:40960\n"
      "v_mfma_f32_16x16x32_bf16 v[176:179], a[56:59], v[12:15], v[176:179]\n"
      "v_mfma_f32_16x16x32_bf16 v[180:183], a[36:39], v[0:3], v[180:183]\n"
      "ds_read_b128 v[48:51], v160 offset:45056\n"
      "v_mfma_f32_16x16x32_bf16 v[180:183], a[44:47], v[4:7], v[180:183]\n"
      "v_mfma_f32_16x16x32_bf16 v[180:183], a[52:55], v[8:11], v[180:183]\n"
      "ds_read_b128 v[52:55], v161 offset:45056\n"
      "v_mfma_f32_16x16x32_bf16 v[180:183], a[60:63], v[12:15], v[180:183]\n"
      "s_waitcnt lgkmcnt(3)\n"
      "v_mfma_f32_16x16x32_bf16 v[176:179], a[176:179], v[16:19], v[176:179]\n"
      "ds_read_b128 v[56:59], v162 offset:45056\n"
      "v_mfma_f32_16x16x32_bf16 v[176:179], a[184:187], v[20:23], v[176:179]\n"
      "v_mfma_f32_16x16x32_bf16 v[176:179], a[208:211], v[24:27], v[176:179]\n"
      "ds_read_b128 v[60:63], v163 offset:45056\n"
      "v_mfma_f32_16x16x32_bf16 v[176:179], a[216:219], v[28:31], v[176:179]\n"
      "v_mfma_f32_16x16x32_bf16 v[180:183], a[180:183], v[16:19], v[180:183]\n"
      "v_mfma_f32_16x16x32_bf16 v[180:183], a[188:191], v[20:23], v[180:183]\n"
      "v_mfma_f32_16x16x32_bf16 v[180:183], a[212:215], v[24:27], v[180:183]\n"
      "v_mfma_f32_16x16x32_bf16 v[180:183], a[220:223], v[28:31], v[180:183]\n"
      "v_mfma_f32_16x16x32_bf16 v[176:179], a[64:67], v[32:35], v[176:179]\n"
      "v_mfma_f32_16x16x32_bf16 v[176:179], a[72:75], v[36:39], v[176:179]\n"
      "v_mfma_f32_16x16x32_bf16 v[176:179], a[80:83], v[40:43], v[176:179]\n"
      "s_waitcnt lgkmcnt(0)\n"
      "v_mfma_f32_16x16x32_bf16 v[176:179], a[88:91], v[44:47], v[176:179]\n"
      "v_mfma_f32_16x16x32_bf16 v[180:183], a[68:71], v[32:35], v[180:183]\n"
      "v_mfma_f32_16x16x32_bf16 v[180:183], a[76:79], v[36:39], v[180:183]\n"
      "v_mfma_f32_16x16x32_bf16 v[180:183], a[84:87], v[40:43], v[180:183]\n"
      "v_mfma_f32_16x16x32_bf16 v[180:183], a[92:95], v[44:47], v[180:183]\n"
      "v_mfma_f32_16x16x32_bf16 v[176:179], a[96:99], v[48:51], v[176:179]\n"
      "v_mfma_f32_16x16x32_bf16 v[176:179], a[104:107], v[52:55], v[176:179]\n"
      "v_mfma_f32_16x16x32_bf16 v[176:179], a[112:115], v[56:59], v[176:179]\n"
      "v_mfma_f32_16x16x32_bf16 v[176:179], a[120:123], v[60:63], v[176:179]\n"
      "v_mfma_f32_16x16x32_bf16 v[180:183], a[100:103], v[48:51], v[180:183]\n"
      "v_mfma_f32_16x16x32_bf16 v[180:183], a[108:111], v[52:55], v[180:183]\n"
      "v_mfma_f32_16x16x32_bf16 v[180:183], a[116:119], v[56:59], v[180:183]\n"
      "v_mfma_f32_16x16x32_bf16 v[180:183], a[124:127], v[60:63], v[180:183]\n"
      "s_cmp_eq_u32 s22, s94\n"
      "s_waitcnt vmcnt(0)\n"
      "s_cbranch_scc0 NOSH_%=\n"
      "v_mfma_f32_16x16x32_bf16 v[192:195], v[104:107], v[88:91], 0\n"
      "v_mfma_f32_16x16x32_bf16 v[192:195], v[104:107], v[92:95], v[192:195]\n"
      "v_mfma_f32_16x16x32_bf16 v[192:195], v[104:107], v[96:99], v[192:195]\n"
      "v_mfma_f32_16x16x32_bf16 v[192:195], v[104:107], v[100:103], v[192:195]\n"
      "v_mfma_f32_16x16x32_bf16 v[196:199], v[106:109], v[88:91], 0\n"
      "v_mfma_f32_16x16x32_bf16 v[196:199], v[106:109], v[92:95], v[196:199]\n"
      "v_mfma_f32_16x16x32_bf16 v[196:199], v[106:109], v[96:99], v[196:199]\n"
      "v_mfma_f32_16x16x32_bf16 v[196:199], v[106:109], v[100:103], v[196:199]\n"
      "NOSH_%=:\n"
      "v_mfma_f32_16x16x32_bf16 v[184:187], v[104:107], v[64:67], v[184:187]\n"
      "v_mfma_f32_16x16x32_bf16 v[184:187], v[104:107], v[72:75], v[184:187]\n"
      "v_mfma_f32_16x16x32_bf16 v[184:187], v[104:107], v[80:83], v[184:187]\n"
      "v_mfma_f32_16x16x32_bf16 v[188:191], v[106:109], v[64:67], v[188:191]\n"
      "v_mfma_f32_16x16x32_bf16 v[188:191], v[106:109], v[72:75], v[188:191]\n"
      "v_mfma_f32_16x16x32_bf16 v[188:191], v[106:109], v[80:83], v[188:191]\n"
      "v_mfma_f32_16x16x32_bf16 v[176:179], v[104:107], v[68:71], v[176:179]\n"
      "v_cvt_pk_bf16_f32 v192, v192, v193\n"
      "v_cvt_pk_bf16_f32 v193, v194, v195\n"
      "v_mfma_f32_16x16x32_bf16 v[176:179], v[104:107], v[76:79], v[176:179]\n"
      "v_cvt_pk_bf16_f32 v194, v196, v197\n"
      "v_cvt_pk_bf16_f32 v195, v198, v199\n"
      "v_mfma_f32_16x16x32_bf16 v[176:179], v[104:107], v[84:87], v[176:179]\n"
      "v_permlane16_swap_b32 v192, v194\n"
      "v_permlane16_swap_b32 v193, v195\n"
      "v_mfma_f32_16x16x32_bf16 v[180:183], v[106:109], v[68:71], v[180:183]\n"
      "v_mfma_f32_16x16x32_bf16 v[180:183], v[106:109], v[76:79], v[180:183]\n"
      "v_mfma_f32_16x16x32_bf16 v[180:183], v[106:109], v[84:87], v[180:183]\n"
      "s_cbranch_scc0 NOSH2_%=\n"
      "buffer_store_dwordx4 v[192:195], v172, s[52:55], s46 offen sc1\n"
      "NOSH2_%=:\n"
      "v_cvt_pk_bf16_f32 v184, v184, v185\n"
      "v_cvt_pk_bf16_f32 v185, v186, v187\n"
      "v_cvt_pk_bf16_f32 v186, v188, v189\n"
      "v_cvt_pk_bf16_f32 v187, v190, v191\n"
      "v_cvt_pk_bf16_f32 v176, v176, v177\n"
      "v_cvt_pk_bf16_f32 v177, v178, v179\n"
      "v_permlane16_swap_b32 v184, v186\n"
      "v_permlane16_swap_b32 v185, v187\n"
      "v_cvt_pk_bf16_f32 v178, v180, v181\n"
      "v_cvt_pk_bf16_f32 v179, v182, v183\n"
      "buffer_store_dwordx4 v[184:187], v172, s[52:55], s44 offen sc1\n"
      "v_permlane16_swap_b32 v176, v178\n"
      "v_permlane16_swap_b32 v177, v179\n"
      "buffer_store_dwordx4 v[176:179], v172, s[52:55], s45 offen sc1\n"
      "s_add_u32 s73, s92, 2\n"
      "v_mov_b32 v234, s73\n"
      "buffer_store_dword v234, v249, s[36:39], s90 offen sc1\n"
      "END_%=:\n"
      "s_mov_b32 %[flag], s72\n"
      "s_branch FIN_%=\n"
      "SPINp0_%=:\n"
      "s_sub_u32 s79, s78, s76\n"
      "s_min_u32 s79, s79, 64\n"
      "s_lshr_b32 s79, s79, 1\n"
      "WAITp0_%=:\n"
      "s_sub_u32 s79, s79, 1\n"
      "s_cbranch_scc0 WAITp0_%=\n"
      "s_branch BACKp0_%=\n"
      "SPINp1_%=:\n"
      "s_sub_u32 s79, s78, s76\n"
      "s_min_u32 s79, s79, 64\n"
      "s_lshr_b32 s79, s79, 1\n"
      "WAITp1_%=:\n"
      "s_sub_u32 s79, s79, 1\n"
      "s_cbranch_scc0 WAITp1_%=\n"
      "s_branch BACKp1_%=\n"
      "SPINp2_%=:\n"
      "s_sub_u32 s79, s78, s76\n"
      "s_min_u32 s79, s79, 64\n"
      "s_lshr_b32 s79, s79, 1\n"
      "WAITp2_%=:\n"
      "s_sub_u32 s79, s79, 1\n"
      "s_cbranch_scc0 WAITp2_%=\n"
      "s_branch BACKp2_%=\n"
      "SPINp3_%=:\n"
      "s_sub_u32 s79, s78, s76\n"
      "s_min_u32 s79, s79, 64\n"
      "s_lshr_b32 s79, s79, 1\n"
      "WAITp3_%=:\n"
      "s_sub_u32 s79, s79, 1\n"
      "s_cbranch_scc0 WAITp3_%=\n"
      "s_branch BACKp3_%=\n"
      "FIN_%=:\n"
      : [flag] "=s"(flag), "={v254}"(snap)
      : [wave] "s"(wave), [blk] "s"(blk), [ka] "s"(__builtin_amdgcn_kernarg_segment_ptr()), [lds] "s"((unsigned)(size_t)lds)
      :
      "v0", "v1", "v2", "v3", "v4", "v5", "v6", "v7", "v8", "v9", "v10", "v11", "v12", "v13", "v14", "v15", "v16", "v17", "v18", "v19", "v20", "v21", "v22", "v23",
      "v24", "v25", "v26", "v27", "v28", "v29", "v30", "v31", "v32", "v33", "v34", "v35", "v36", "v37", "v38", "v39", "v40", "v41", "v42", "v43", "v44", "v45", "v46", "v47",
      "v48", "v49", "v50", "v51", "v52", "v53", "v54", "v55", "v56", "v57", "v58", "v59", "v60", "v61", "v62", "v63", "v64", "v65", "v66", "v67", "v68", "v69", "v70", "v71",
      "v72", "v73", "v74", "v75", "v76", "v77", "v78", "v79", "v80", "v81", "v82", "v83", "v84", "v85", "v86", "v87", "v88", "v89", "v90", "v91", "v92", "v93", "v94", "v95",
      "v96", "v97", "v98", "v99", "v100", "v101", "v102", "v103", "v104", "v105", "v106", "v107", "v108", "v109", "v110", "v111", "v112", "v113", "v114", "v115", "v116", "v117", "v118", "v119",
      "v120", "v121", "v122", "v123", "v124", "v125", "v126", "v127", "v128", "v129", "v130", "v131", "v132", "v133", "v134", "v135", "v136", "v137", "v138", "v139", "v140", "v141", "v142", "v143",
      "v144", "v145", "v146", "v147", "v148", "v149", "v150", "v151", "v152", "v153", "v154", "v155", "v156", "v157", "v158", "v159", "v160", "v161", "v162", "v163", "v164", "v165", "v166", "v167",
      "v168", "v169", "v170", "v171", "v172", "v173", "v174", "v175", "v176", "v177", "v178", "v179", "v180", "v181", "v182", "v183", "v184", "v185", "v186", "v187", "v188", "v189", "v190", "v191",
      "v192", "v193", "v194", "v195", "v196", "v197", "v198", "v199", "v200", "v201", "v202", "v203", "v204", "v205", "v206", "v207", "v208", "v209", "v210", "v211", "v212", "v213", "v214", "v215",
      "v216", "v217", "v218", "v219", "v220", "v221", "v222", "v223", "v224", "v225", "v226", "v227", "v228", "v229", "v230", "v231", "v232", "v233", "v234", "v235", "v236", "v237", "v238", "v239",
      "v240", "v241", "v242", "v243", "v244", "v245", "v246", "v247", "v248", "v249", "v250", "v251", "v252", "v253", "v255", "a0", "a1", "a2", "a3", "a4", "a5", "a6", "a7", "a8",
      "a9", "a10", "a11", "a12", "a13", "a14", "a15", "a16", "a17", "a18", "a19", "a20", "a21", "a22", "a23", "a24", "a25", "a26", "a27", "a28", "a29", "a30", "a31", "a32",
      "a33", "a34", "a35", "a36", "a37", "a38", "a39", "a40", "a41", "a42", "a43", "a44", "a45", "a46", "a47", "a48", "a49", "a50", "a51", "a52", "a53", "a54", "a55", "a56",
      "a57", "a58", "a59", "a60", "a61", "a62", "a63", "a64", "a65", "a66", "a67", "a68", "a69", "a70", "a71", "a72", "a73", "a74", "a75", "a76", "a77", "a78", "a79", "a80",
      "a81", "a82", "a83", "a84", "a85", "a86", "a87", "a88", "a89", "a90", "a91", "a92", "a93", "a94", "a95", "a96", "a97", "a98", "a99", "a100", "a101", "a102", "a103", "a104",
      "a105", "a106", "a107", "a108", "a109", "a110", "a111", "a112", "a113", "a114", "a115", "a116", "a117", "a118", "a119", "a120", "a121", "a122", "a123", "a124", "a125", "a126", "a127", "a128",
      "a129", "a130", "a131", "a132", "a133", "a134", "a135", "a136", "a137", "a138", "a139", "a140", "a141", "a142", "a143", "a144", "a145", "a146", "a147", "a148", "a149", "a150", "a151", "a152",
      "a153", "a154", "a155", "a156", "a157", "a158", "a159", "a160", "a161", "a162", "a163", "a164", "a165", "a166", "a167", "a168", "a169", "a170", "a171", "a172", "a173", "a174", "a175", "a176",
      "a177", "a178", "a179", "a180", "a181", "a182", "a183", "a184", "a185", "a186", "a187", "a188", "a189", "a190", "a191", "a192", "a193", "a194", "a195", "a196", "a197", "a198", "a199", "a200",
      "a201", "a202", "a203", "a204", "a205", "a206", "a207", "a208", "a209", "a210", "a211", "a212", "a213", "a214", "a215", "a216", "a217", "a218", "a219", "a220", "a221", "a222", "a223", "a224",
      "a225", "a226", "a227", "a228", "a229", "a230", "a231", "a232", "a233", "a234", "a235", "a236", "a237", "a238", "a239", "a240", "a241", "a242", "a243", "a244", "a245", "a246", "a247", "a248",
      "a249", "a250", "a251", "a252", "a253", "a254", "a255", "s8", "s9", "s10", "s11", "s12", "s13", "s14", "s15", "s16", "s17", "s18", "s19", "s20", "s21", "s22", "s23", "s24",
      "s25", "s26", "s27", "s28", "s29", "s30", "s31", "s34", "s35", "s36", "s37", "s38", "s39", "s40", "s41", "s42", "s43", "s44", "s45", "s46", "s47", "s48", "s49", "s50",
      "s51", "s52", "s53", "s54", "s55", "s56", "s57", "s58", "s59", "s60", "s61", "s62", "s63", "s64", "s65", "s66", "s67", "s68", "s69", "s70", "s71", "s72", "s73", "s74",
      "s75", "s76", "s77", "s78", "s79", "s80", "s81", "s82", "s83", "s84", "s85", "s86", "s87", "s88", "s89", "s90", "s91", "s92", "s93", "s94", "s95", "vcc", "scc", "memory");
  if (flag) {
    asm volatile("s_waitcnt vmcnt(0)" ::: "memory");
    int lane;
    asm volatile("v_mbcnt_lo_u32_b32 %0, -1, 0\n v_mbcnt_hi_u32_b32 %0, -1, %0" : "=v"(lane));
    const int b = 4 * (blk & 1) + ((blk >> 3) & 3), h = 8 * ((blk >> 1) & 3) + (blk >> 5);
    const u16* kh = kc + (size_t)(b * H + h) * SK * DH;
    const u16* vh = vc + (size_t)(b * H + h) * SK * DH;
#pragma unroll 1
    for (int j = flag & 2; j < 5; ++j)
      attend_exact(x, wq, kh, vh, ows, b, h, j < 4 ? 4 * wave + (((h >> 3) + 1 + j) & 3) : 16 + (wave >> 1), lane, j < 4 ? 0 : 2 * (wave & 1), j < 4 ? 4 : 2 * (wave & 1) + 2);
    project_out(wo, out, ows, b, h, wave, lane, snap);
  }
}

extern "C" void kernel_launch(void* const* d_in, const int* in_sizes, int n_in, void* d_out, int out_size, void* d_ws, size_t ws_size, hipStream_t stream) {
  mha_block<<<B * H, 256, 0, stream>>>((const u16*)d_in[0], (const u16*)d_in[1], (const u16*)d_in[2], (const u16*)d_in[3], (const u16*)d_in[4], (u16*)d_out, (char*)d_ws);
}
```
